```python
import torch

def _softmax(x, axis=-1):
    return torch.softmax(x, dim=axis)
def _gelu(x):
    return torch.nn.functional.gelu(x, approximate="tanh")
def _silu(x):
    return torch.nn.functional.silu(x)
def _sigmoid(x):
    return torch.sigmoid(x)
def _layernorm(x, axis=-1, eps=1e-5):
    m = x.mean(dim=axis, keepdim=True)
    v = x.var(dim=axis, keepdim=True, unbiased=False)
    return (x - m) / torch.sqrt(v + eps)
def _scatter_add(tgt, idx, src, axis=0):
    return tgt.clone().index_add_(axis, idx, src)
def _segment_sum(src, seg, n):
    out = torch.zeros((n,) + src.shape[1:], dtype=src.dtype)
    return out.index_add_(0, seg, src)
def _pad(x, pw):
    flat = tuple(v for pair in reversed(pw) for v in pair)
    return torch.nn.functional.pad(x, flat)

kernel_name = 'recurrent_scan__linear_kernel_hedgehog__dense_activation_squared_relu__prompt'

def setup_inputs():

    _w = (lambda a: (lambda t: t.to(torch.bfloat16) if t.is_floating_point() else t)(torch.as_tensor(a)))
    def _A_setup():
        B, S, D, H = 1, 2048, 448, 16
        Dh = D // H
        return {
            "x": _w((lambda *s: torch.randn(*s, dtype=torch.bfloat16) * 0.05)(B, S, D)),
            "wqkv": _w((lambda *s: torch.randn(*s, dtype=torch.bfloat16) * 0.2)(D, 3 * D)),
            "w_phi": _w((lambda *s: torch.randn(*s, dtype=torch.bfloat16) * 0.2)(H, Dh, Dh) * (Dh ** -0.5)),
            "wo": _w((lambda *s: torch.randn(*s, dtype=torch.bfloat16) * 0.05)(D, D)),
        }
    _A_in = _A_setup()

    def _B_setup():
        B, S, D, F = 1, 2048, 448, 1792
        return {
            "x": _w((lambda *s: torch.randn(*s, dtype=torch.bfloat16) * 0.05)(B, S, D)),
            "w1": _w((lambda *s: torch.randn(*s, dtype=torch.bfloat16) * 0.05)(D, F)),
            "b1": _w((lambda *s: torch.randn(*s, dtype=torch.bfloat16) * 0.2)(F)),
            "w2": _w((lambda *s: torch.randn(*s, dtype=torch.bfloat16) * 0.05)(F, D)),
            "b2": _w((lambda *s: torch.randn(*s, dtype=torch.bfloat16) * 0.8)(D)),
        }
    _B_in = _B_setup()

    return {
        "A_x": _A_in["x"],
        "A_wqkv": _A_in["wqkv"],
        "A_w_phi": _A_in["w_phi"],
        "A_wo": _A_in["wo"],
        "B_x": _B_in["x"],
        "B_w1": _B_in["w1"],
        "B_b1": _B_in["b1"],
        "B_w2": _B_in["w2"],
        "B_b2": _B_in["b2"],
    }

def reference(A_x, A_wqkv, A_w_phi, A_wo, B_x, B_w1, B_b1, B_w2, B_b2):
    def _A_ref(x, wqkv, w_phi, wo):
        B, S, D = x.shape
        H = 16; Dh = D // H
        qkv = (lambda x, *d: x.permute(*d))((x @ wqkv).reshape(B, S, 3, H, Dh), 2, 0, 3, 1, 4)
        q, k, v = qkv[0], qkv[1], qkv[2]
        pq = torch.einsum("bhsd,hdf->bhsf", q, w_phi)
        pk = torch.einsum("bhsd,hdf->bhsf", k, w_phi)
        phi_q = torch.concatenate([_softmax(pq, axis=-1), _softmax(-pq, axis=-1)], axis=-1)
        phi_k = torch.concatenate([_softmax(pk, axis=-1), _softmax(-pk, axis=-1)], axis=-1)
        kv = torch.einsum("bhsf,bhsd->bhfd", phi_k, v)
        z  = torch.einsum("bhsf->bhf", phi_k)
        num = torch.einsum("bhqf,bhfd->bhqd", phi_q, kv)
        den = torch.einsum("bhqf,bhf->bhq", phi_q, z)[..., None] + 1e-6
        o = num / den
        return (lambda x, *d: x.permute(*d))(o, 0, 2, 1, 3).reshape(B, S, D) @ wo

    def _B_ref(x, w1, b1, w2, b2):
        h = torch.einsum("bsd,df->bsf", x, w1) + b1
        zero = torch.zeros_like(h)
        r = torch.where(h > zero, h, zero)
        h = r * r
        out = torch.einsum("bsf,fd->bsd", h, w2) + b2
        return out

    x = A_x
    xn = _layernorm(x, axis=-1)
    s = _A_ref(xn, A_wqkv, A_w_phi, A_wo)
    g = _silu(_B_ref(xn, B_w1, B_b1, B_w2, B_b2))
    return x + s * g
```

```cpp
#include <hip/hip_runtime.h>
#include <hip/hip_bf16.h>
typedef unsigned short u16;
__device__ inline float b2f(u16 v){ return __uint_as_float(((unsigned)v)<<16); }
__device__ inline u16 f2b(float f){ unsigned u=__float_as_uint(f); u += 0x7fffu + ((u>>16)&1u); return (u16)(u>>16); }
__device__ inline float rb(float f){ return b2f(f2b(f)); }
constexpr int S=2048,D=448,H=16,DH=28,F=1792;
__global__ void k_ln(const u16* x, u16* xn){
  int r=blockIdx.x; __shared__ float red[64];
  int t=threadIdx.x; float s=0; for(int i=t;i<D;i+=64) s+=b2f(x[r*D+i]);
  for(int m=32;m>=1;m>>=1) s+=__shfl_xor(s,m);
  float mean=s/D; float v=0; for(int i=t;i<D;i+=64){float d=b2f(x[r*D+i])-mean; v+=d*d;}
  for(int m=32;m>=1;m>>=1) v+=__shfl_xor(v,m);
  v/=D; float rs=1.0f/sqrtf(v+1e-5f);
  for(int i=t;i<D;i+=64) xn[r*D+i]=f2b((b2f(x[r*D+i])-mean)*rs);
}
__global__ void k_gemm(const u16* a,const u16* b,const u16* bias,u16* o,int M,int N,int K,int mode){
  int n=blockIdx.x*blockDim.x+threadIdx.x; int m=blockIdx.y; if(n>=N) return;
  float acc=0; for(int k=0;k<K;k++) acc+=b2f(a[m*K+k])*b2f(b[k*N+n]);
  if(mode==1){ acc+=b2f(bias[n]); acc=acc>0?acc:0; acc=acc*acc; }
  if(mode==2){ acc+=b2f(bias[n]); acc=acc/(1.0f+expf(-acc)); }
  o[m*N+n]=f2b(acc);
}
__global__ void k_phi(const u16* qkv,const u16* wphi,float* phi){
  int s=blockIdx.x, h=blockIdx.y, w=blockIdx.z; int t=threadIdx.x; __shared__ float p[32];
  float a=0; if(t<DH){ for(int d=0;d<DH;d++) a+=b2f(qkv[s*1344+w*448+h*DH+d])*b2f(wphi[(h*DH+d)*DH+t]); p[t]=a; }
  __syncthreads();
  if(t<DH){ float mx=-1e30f,mn=1e30f; for(int i=0;i<DH;i++){mx=fmaxf(mx,p[i]);mn=fminf(mn,p[i]);}
    float s1=0,s2=0; for(int i=0;i<DH;i++){s1+=expf(p[i]-mx); s2+=expf(mn-p[i]);}
    float* o=phi+(((size_t)w*S+s)*H+h)*56; o[t]=rb(expf(a-mx)/s1); o[28+t]=rb(expf(mn-a)/s2); }
}
__global__ void k_kv(const u16* qkv,const float* phi,float* kv){
  int h=blockIdx.x; int f=blockIdx.y; int d=threadIdx.x; if(d>DH) return;
  const float* pk=phi+((size_t)S*H)*56; float a=0;
  for(int s=0;s<S;s++){ float p=pk[((size_t)s*H+h)*56+f]; float v=d<DH?b2f(qkv[s*1344+896+h*DH+d]):1.0f; a+=p*v; }
  kv[(h*56+f)*32+d]=a;
}
__global__ void k_o(const float* phi,const float* kv,u16* o){
  int s=blockIdx.x,h=blockIdx.y,d=threadIdx.x; if(d>=DH) return;
  const float* pq=phi+((size_t)s*H+h)*56; float num=0,den=0;
  for(int f=0;f<56;f++){ num+=pq[f]*kv[(h*56+f)*32+d]; den+=pq[f]*kv[(h*56+f)*32+28]; }
  o[s*D+h*DH+d]=f2b(num/(den+1e-6f));
}
__global__ void k_fin(const u16* x,const u16* sv,const u16* g,u16* out){
  int i=blockIdx.x*blockDim.x+threadIdx.x; if(i<S*D) out[i]=f2b(b2f(x[i])+b2f(sv[i])*b2f(g[i]));
}
extern "C" void kernel_launch(void* const* d_in, const int* in_sizes, int n_in, void* d_out, int out_size, void* d_ws, size_t ws_size, hipStream_t stream){
  const u16* x=(const u16*)d_in[0]; const u16* wqkv=(const u16*)d_in[1]; const u16* wphi=(const u16*)d_in[2]; const u16* wo=(const u16*)d_in[3];
  const u16* w1=(const u16*)d_in[5]; const u16* b1=(const u16*)d_in[6]; const u16* w2=(const u16*)d_in[7]; const u16* b2=(const u16*)d_in[8];
  char* ws=(char*)d_ws; u16* xn=(u16*)ws; u16* qkv=(u16*)(ws+(4<<20)); u16* hh=(u16*)(ws+(12<<20)); u16* g=(u16*)(ws+(24<<20));
  float* phi=(float*)(ws+(32<<20)); float* kv=(float*)(ws+(64<<20)); u16* o=(u16*)(ws+(68<<20)); u16* sv=(u16*)(ws+(72<<20));
  k_ln<<<S,64,0,stream>>>(x,xn);
  k_gemm<<<dim3((1344+255)/256,S),256,0,stream>>>(xn,wqkv,nullptr,qkv,S,1344,D,0);
  k_gemm<<<dim3(F/256,S),256,0,stream>>>(xn,w1,b1,hh,S,F,D,1);
  k_gemm<<<dim3(2,S),256,0,stream>>>(hh,w2,b2,g,S,D,F,2);
  k_phi<<<dim3(S,H,2),64,0,stream>>>(qkv,wphi,phi);
  k_kv<<<dim3(H,56),64,0,stream>>>(qkv,phi,kv);
  k_o<<<dim3(S,H),64,0,stream>>>(phi,kv,o);
  k_gemm<<<dim3(2,S),256,0,stream>>>(o,wo,nullptr,sv,S,D,D,0);
  k_fin<<<(S*D+255)/256,256,0,stream>>>(x,sv,g,(u16*)d_out);
}
```

```cpp
#include <hip/hip_runtime.h>
#include <hip/hip_bf16.h>
#include <stdint.h>
#include <type_traits>

typedef unsigned u32;
typedef u32 u32x4 __attribute__((ext_vector_type(4)));
typedef u32 u32x2 __attribute__((ext_vector_type(2)));
typedef float f32x16 __attribute__((ext_vector_type(16)));
typedef float f32x4 __attribute__((ext_vector_type(4)));
typedef float f32x2 __attribute__((ext_vector_type(2)));
typedef short s16x8 __attribute__((ext_vector_type(8)));
typedef short s16x4 __attribute__((ext_vector_type(4)));
typedef __bf16 bf16x2 __attribute__((ext_vector_type(2)));
typedef __bf16 bf16x8 __attribute__((ext_vector_type(8)));
#define LAS __attribute__((address_space(3)))
#define GAS __attribute__((address_space(1)))
#define DI __device__ __forceinline__
typedef __amdgpu_buffer_rsrc_t rsrc_t;
typedef LAS char lchar;

namespace {
constexpr int DM = 448, DF = 1792;
constexpr u32 XROW = DM * 2;
constexpr u32 QKVROW = 3 * DM * 2;
constexpr u32 W1ROW = DF * 2;
constexpr u32 WS_EPOCH = 0;
constexpr u32 WS_AF = 4096;
constexpr u32 WS_OF = WS_AF + 32768;
constexpr u32 WS_KVP = 131072;
constexpr u32 WS_H = WS_KVP + (1u << 20);
constexpr u32 WS_O = WS_H + 64 * 112 * 1024;
constexpr u32 WS_END = WS_O + 64 * 32 * 1024;
constexpr u32 L_SLOT = 16384, NSLOT = 9;
constexpr u32 L_MISC = NSLOT * L_SLOT;
constexpr u32 L_WPHI = L_MISC;
constexpr u32 L_ZERO = L_MISC + 2048;
constexpr u32 L_CSP = L_MISC + 2304;
constexpr u32 L_TOK = L_CSP + 4096;
constexpr u32 L_KVF = L_TOK + 1024;
constexpr u32 L_END = L_KVF + 4096;
constexpr u32 LDS_BYTES = 163840;
static_assert(L_END <= LDS_BYTES, "lds");
constexpr u32 ATT_TILE = DM * 64;
constexpr int SPIN_CAP = 1 << 16;
constexpr float LOG2E = 1.4426950408889634f;

template <int B, int E, class F> DI void sfor(F &&f) {
  if constexpr (B < E) { f(std::integral_constant<int, B>{}); sfor<B + 1, E>(f); }
}
DI u32 rfl(u32 v) { return __builtin_amdgcn_readfirstlane(v); }
DI f32x16 mfma(u32x4 a, u32x4 b, f32x16 c) {
  return __builtin_amdgcn_mfma_f32_32x32x16_bf16(__builtin_bit_cast(bf16x8, a), __builtin_bit_cast(bf16x8, b), c, 0, 0, 0);
}
DI u32 pk2(float lo, float hi) { f32x2 p = {lo, hi}; return __builtin_bit_cast(u32, __builtin_convertvector(p, bf16x2)); }
DI float dot2(u32 a, u32 b, float c) { return __builtin_amdgcn_fdot2_f32_bf16(__builtin_bit_cast(bf16x2, a), __builtin_bit_cast(bf16x2, b), c, false); }
DI float bf2f(u32 lo16) { return __builtin_bit_cast(float, lo16 << 16); }
DI float bfhi(u32 v) { return __builtin_bit_cast(float, v & 0xffff0000u); }
DI float asf(u32 v) { return __builtin_bit_cast(float, v); }
DI f32x2 both_halves(float v) {
  const u32 a = __builtin_bit_cast(u32, v);
  auto r = __builtin_amdgcn_permlane32_swap(a, a, false, false);
  return f32x2{asf(r[0]), asf(r[1])};
}
DI float half_lo(float v) { return both_halves(v).x; }
DI u32x4 pack8(const f32x16 &x, int s) { return u32x4{pk2(x[8 * s], x[8 * s + 1]), pk2(x[8 * s + 2], x[8 * s + 3]), pk2(x[8 * s + 4], x[8 * s + 5]), pk2(x[8 * s + 6], x[8 * s + 7])}; }
DI u32x2 trd(lchar *p) {
  typedef short v4i16 __attribute__((ext_vector_type(4)));
  return __builtin_bit_cast(u32x2, __builtin_amdgcn_ds_read_tr16_b64_v4i16((LAS v4i16 *)p));
}
DI u32x4 trfrag(lchar *p, int tstride) { u32x2 a = trd(p), b = trd(p + tstride); return u32x4{a.x, a.y, b.x, b.y}; }
DI f32x16 zero16() { f32x16 z; for (int i = 0; i < 16; ++i) z[i] = 0.f; return z; }
#define BAR() asm volatile("s_waitcnt lgkmcnt(0)\n\ts_barrier" ::: "memory")
template <int N> DI void vmwait() { asm volatile("s_waitcnt vmcnt(%0)" ::"i"(N) : "memory"); }
DI void dma16(rsrc_t r, u32 voff, u32 soff, u32 ldsaddr) {
  asm volatile("s_mov_b32 m0, %0\n\ts_nop 0\n\tbuffer_load_dwordx4 %1, %2, %3 offen lds" ::"s"(ldsaddr), "v"(voff), "s"(r), "s"(soff) : "memory", "m0");
}
template <int OFF> DI void ald128(u32x4 &d, rsrc_t r, u32 voff, u32 soff) {
  asm volatile("buffer_load_dwordx4 %0, %1, %2, %3 offen offset:%4" : "=v"(d) : "v"(voff), "s"(r), "s"(soff), "i"(OFF) : "memory");
}
DI u32x4 ld128_sc1(rsrc_t r, u32 voff, u32 soff) { return __builtin_amdgcn_raw_buffer_load_b128(r, voff, soff, 16); }
DI void st128_sc1(u32x4 v, rsrc_t r, u32 voff, u32 soff) { __builtin_amdgcn_raw_buffer_store_b128(v, r, voff, soff, 16); }
typedef GAS u32 gu32;
DI u32 ld_flag(const char *ws, u32 off) { return __hip_atomic_load((gu32 *)(ws + off), __ATOMIC_RELAXED, __HIP_MEMORY_SCOPE_AGENT); }
DI void st_flag(char *ws, u32 off, u32 v) { __hip_atomic_store((gu32 *)(ws + off), v, __ATOMIC_RELAXED, __HIP_MEMORY_SCOPE_AGENT); }
DI void poll_all(const char *ws, u32 off, u32 E) {
#pragma unroll 1
  for (int spin = 0; spin < SPIN_CAP; ++spin) {
    if (__all(ld_flag(ws, off) == E)) break;
    __builtin_amdgcn_s_sleep(1);
  }
  __builtin_amdgcn_fence(__ATOMIC_ACQUIRE, "wavefront");
}
DI u32 att_block(int head, int rg) { return (u32)((2 * (rg >> 1) + (head >> 3)) + 8 * ((head & 7) + 8 * (rg & 1))); }
DI u32 ffn_block(int ct, int rg) { const int hi = ct >= 7; return (u32)((2 * (rg >> 1) + hi) + 8 * (16 + (ct - 7 * hi) + 7 * (rg & 1))); }

struct Ptrs {
  const char *x, *wqkv, *wphi, *wo, *w1, *b1, *w2, *b2;
  char *out, *ws;
};

template <bool ATT>
DI void gemmA(lchar *L, rsrc_t rx, rsrc_t rw, u32 soffx, u32 wsoff, int w, int lane, f32x16 (&acc)[ATT ? 3 : 4], float &ssum, float &ssq, float (&cs)[ATT ? 3 : 4]) {
  constexpr int T = ATT ? 3 : 4, DEP = 3;
  const u32 voxA = (lane & 15) * XROW + (lane >> 4) * 16, voxB = voxA + 16 * XROW;
  u32 vow;
  if constexpr (ATT) { const u32 c = lane & 3; vow = (lane >> 2) * QKVROW + (c == 3 ? 40 : 16 * c); }
  else vow = (lane >> 4) * W1ROW + 16 * ((lane & 15) ^ ((lane >> 4) << 2));
  u32x4 xr[28];
  auto issue = [&](auto C) {
    constexpr int c = C;
    if constexpr (ATT) {
      { const u32 id = w, t = id >> 2, rb = id & 3;
        dma16(rw, vow, wsoff + (64 * c + 16 * rb) * QKVROW + (t == 0 ? 896u : 1792u), t * ATT_TILE + (64 * c + 16 * rb) * 64); }
      if (w < 4) dma16(rw, vow, wsoff + (64 * c + 16 * w) * QKVROW, 2 * ATT_TILE + (64 * c + 16 * w) * 64);
    } else {
      dma16(rw, vow, wsoff + (64 * c + 8 * w) * W1ROW, (64 * c + 8 * w) * 256);
      dma16(rw, vow, wsoff + (64 * c + 8 * w + 4) * W1ROW, (64 * c + 8 * w + 4) * 256);
    }
    ald128<128 * c>(xr[4 * c], rx, voxA, soffx);
    ald128<128 * c>(xr[4 * c + 1], rx, voxB, soffx);
    ald128<128 * c + 64>(xr[4 * c + 2], rx, voxA, soffx);
    ald128<128 * c + 64>(xr[4 * c + 3], rx, voxB, soffx);
  };
  sfor<0, DEP>([&](auto C) { issue(C); asm volatile("s_barrier" ::: "memory"); });
  const int h = lane >> 5, blk = (lane >> 4) & 1, q = (lane & 15) >> 2, p = lane & 3;
  lchar *wb[T];
  if constexpr (ATT) { for (int t = 0; t < T; ++t) wb[t] = L + t * ATT_TILE + (16 * h + q) * 64 + 32 * blk + 8 * p; }
  else { for (int t = 0; t < T; ++t) wb[t] = L + (16 * h + q) * 256 + 64 * (t ^ q) + 32 * blk + 8 * p; }
  constexpr int ROWB = ATT ? 64 : 256;
  float s0 = 0.f, s1 = 0.f, q0 = 0.f, q1 = 0.f;
  const u32 ONES = 0x3F803F80u;
  for (int t = 0; t < T; ++t) { acc[t] = zero16(); cs[t] = 0.f; }
  sfor<0, 7>([&](auto C) {
    constexpr int c = C;
    constexpr int ahead = (c + DEP - 1 > 6 ? 6 : c + DEP - 1) - c;
    if constexpr (ATT) { if (w < 4) vmwait<ahead * 6>(); else vmwait<ahead * 5>(); }
    else vmwait<ahead * 6>();
    asm volatile("" : "+v"(xr[4 * c]), "+v"(xr[4 * c + 1]), "+v"(xr[4 * c + 2]), "+v"(xr[4 * c + 3]));
    asm volatile("s_barrier" ::: "memory");
    if constexpr (c + DEP < 7) issue(std::integral_constant<int, c + DEP>{});
    sfor<0, 2>([&](auto I) {
      constexpr int G = 2 * c + I;
      u32x4 a = xr[2 * G], b = xr[2 * G + 1];
      for (int d = 0; d < 4; ++d) { auto r = __builtin_amdgcn_permlane16_swap(a[d], b[d], false, false); a[d] = r[0]; b[d] = r[1]; }
      sfor<0, 2>([&](auto U) {
        constexpr int u = U, ks = 2 * G + u;
        const u32x4 xf = u ? b : a;
        u32x4 wf[T];
        for (int t = 0; t < T; ++t) wf[t] = trfrag(wb[t] + (32 * G + 8 * u) * ROWB, 4 * ROWB);
        for (int t = 0; t < T; ++t) acc[t] = (ATT && t == 1) ? mfma(xf, wf[t], acc[t]) : mfma(wf[t], xf, acc[t]);
        s0 = dot2(xf[0], ONES, s0); q0 = dot2(xf[0], xf[0], q0);
        s1 = dot2(xf[1], ONES, s1); q1 = dot2(xf[1], xf[1], q1);
        s0 = dot2(xf[2], ONES, s0); q0 = dot2(xf[2], xf[2], q0);
        s1 = dot2(xf[3], ONES, s1); q1 = dot2(xf[3], xf[3], q1);
        if ((ks & 7) == w) {
          for (int t = 0; t < T; ++t) { float a0 = dot2(wf[t][0], ONES, cs[t]); float a1 = dot2(wf[t][1], ONES, 0.f); a0 = dot2(wf[t][2], ONES, a0); a1 = dot2(wf[t][3], ONES, a1); cs[t] = a0 + a1; }
        }
      });
    });
  });
  ssum = s0 + s1; ssq = q0 + q1;
}

struct RowStat { float rstd; u32x4 xaug; };
DI RowStat row_stats(float ssum, float ssq, int h) {
  f32x2 a = both_halves(ssum), b = both_halves(ssq);
  const float mean = (a.x + a.y) * (1.0f / DM);
  float var = (b.x + b.y) * (1.0f / DM) - mean * mean;
  var = fmaxf(var, 0.f) + 1e-5f;
  const float sig = __builtin_sqrtf(var);
  RowStat r; r.rstd = 1.0f / sig;
  const float m = -mean, ml = m - bfhi(pk2(m, m)), sl = sig - bfhi(pk2(sig, sig));
  const u32 mm = pk2(m, ml), ss = pk2(sig, sl);
  r.xaug = h ? u32x4{0, 0, 0, 0} : u32x4{mm, mm, ss, 0};
  return r;
}
DI u32x4 waug(float cs, u32 bb) {
  const u32 hh = pk2(cs, cs); const float cl = cs - bfhi(hh);
  return u32x4{hh, pk2(cl, cl), bb, 0};
}
template <int T> DI void cs_write(lchar *L, const float (&cs)[T], int w, int lane) {
  for (int t = 0; t < T; ++t) { f32x2 a = both_halves(cs[t]); if (lane < 32) *(LAS float *)(L + L_CSP + (t * 32 + lane) * 32 + 4 * w) = a.x + a.y; }
}
DI float cs_read(lchar *L, int t, int lane) {
  const f32x4 a = *(LAS f32x4 *)(L + L_CSP + (t * 32 + (lane & 31)) * 32), b = *(LAS f32x4 *)(L + L_CSP + (t * 32 + (lane & 31)) * 32 + 16);
  return ((a.x + a.y) + (a.z + a.w)) + ((b.x + b.y) + (b.z + b.w));
}

DI void feature_map(const f32x16 &P, float rstd, int h, u32x4 (&pf)[2][2]) {
  const float c = rstd * LOG2E;
  float mx = fmaxf(fmaxf(P[0], P[1]), P[2]), mn = fminf(fminf(P[0], P[1]), P[2]);
  for (int i = 3; i < 15; i += 2) { mx = fmaxf(fmaxf(mx, P[i]), P[i + 1]); mn = fminf(fminf(mn, P[i]), P[i + 1]); }
  mx = fmaxf(mx, P[15]); mn = fminf(mn, P[15]);
  { f32x2 a = both_halves(mx), b = both_halves(mn); mx = fmaxf(a.x, a.y); mn = fminf(b.x, b.y); }
  const float mxc = -mx * c, mnc = mn * c;
  f32x16 ep, en;
  for (int i = 0; i < 16; ++i) { ep[i] = __builtin_amdgcn_exp2f(__builtin_fmaf(P[i], c, mxc)); en[i] = __builtin_amdgcn_exp2f(__builtin_fmaf(P[i], -c, mnc)); }
  float sp = 0.f, sn = 0.f, sp2 = 0.f, sn2 = 0.f;
  for (int i = 0; i < 12; i += 2) { sp += ep[i]; sp2 += ep[i + 1]; sn += en[i]; sn2 += en[i + 1]; }
  const float keep = h ? 0.f : 1.f;
  sp = (sp + sp2) + keep * ((ep[12] + ep[13]) + (ep[14] + ep[15]));
  sn = (sn + sn2) + keep * ((en[12] + en[13]) + (en[14] + en[15]));
  { f32x2 a = both_halves(sp), b = both_halves(sn); sp = a.x + a.y; sn = b.x + b.y; }
  const float rp = __builtin_amdgcn_rcpf(sp), rn = __builtin_amdgcn_rcpf(sn);
  for (int i = 0; i < 16; ++i) { ep[i] *= rp; en[i] *= rn; }
  pf[0][0] = pack8(ep, 0); pf[0][1] = pack8(ep, 1); pf[1][0] = pack8(en, 0); pf[1][1] = pack8(en, 1);
}

DI void stage_dma(rsrc_t rw2, rsrc_t rwo, u32 vob, int st, int lw, u32 cc) {
  const u32 kg = lw >> 1;
  for (int i = 0; i < 4; ++i) {
    const u32 j7 = 4 * (lw & 1) + i;
    const u32 dst = (st % NSLOT) * L_SLOT + kg * 8192 + j7 * 1024;
    if (st < 14) dma16(rw2, vob, (896 * kg + 64 * st + 8 * j7) * XROW + 128 * cc, dst);
    else { const u32 head = 8 * kg + 2 * (st - 14) + (j7 >> 2), r = (j7 & 3) == 3 ? 20u : 8 * (j7 & 3); dma16(rwo, vob, (28 * head + r) * XROW + 128 * cc, dst); }
  }
}
}

__global__ void __launch_bounds__(512, 2) fused_hedgehog_ffn(const char *__restrict__ gx, const char *__restrict__ gwqkv, const char *__restrict__ gwphi, const char *__restrict__ gwo,
                                                              const char *__restrict__ gw1, const char *__restrict__ gb1, const char *__restrict__ gw2, const char *__restrict__ gb2,
                                                              char *__restrict__ gout, char *__restrict__ gws) {
  __shared__ __attribute__((aligned(1024))) char smem[LDS_BYTES];
  lchar *L = (lchar *)smem;
  const int tid = threadIdx.x, lane = tid & 63, w = rfl(tid >> 6);
  const int b = blockIdx.x, xcd = b & 7, slot = b >> 3;
  const int h = lane >> 5;
  const rsrc_t rx = __builtin_amdgcn_make_buffer_rsrc((void *)gx, 0, 2048 * XROW, 0x00020000);
  const rsrc_t rws = __builtin_amdgcn_make_buffer_rsrc((void *)gws, 0, WS_END, 0x00020000);
  const rsrc_t rw2 = __builtin_amdgcn_make_buffer_rsrc((void *)gw2, 0, DF * XROW, 0x00020000);
  const rsrc_t rwo = __builtin_amdgcn_make_buffer_rsrc((void *)gwo, 0, DM * XROW, 0x00020000);
  u32 ev;
  asm volatile("global_load_dword %0, %1, off sc1" : "=v"(ev) : "v"(gws + WS_EPOCH) : "memory");
  const u32 vob = (lane >> 3) * XROW + 16 * ((lane & 7) ^ (((lane >> 4) & 1) << 2));
  const int tc = slot >= 16 ? slot - 16 : slot + 16;
  const bool has_tile = tc < 28;
  const u32 rt = tc / 7, cc = tc % 7;
  u32 E;

  if (slot < 16) {
    const int head = 8 * (xcd & 1) + (slot & 7), rg = 2 * (xcd >> 1) + (slot >> 3);
    const rsrc_t rw = __builtin_amdgcn_make_buffer_rsrc((void *)gwqkv, 0, DM * QKVROW, 0x00020000);
    const rsrc_t rp = __builtin_amdgcn_make_buffer_rsrc((void *)gwphi, 0, 16 * 28 * 28 * 2, 0x00020000);
    if (w < 2) dma16(rp, lane * 16 + w * 1024, head * 1568, L_WPHI + w * 1024);
    if (tid < 16) *(LAS u32 *)(L + L_ZERO + 4 * tid) = 0;
    f32x16 acc[3]; float ssum, ssq, cs[3];
    gemmA<true>(L, rx, rw, rfl((256 * rg + 32 * w) * XROW), rfl(56 * head), w, lane, acc, ssum, ssq, cs);
    asm volatile("" : "+v"(ev));
    E = rfl(ev) + 1;
    cs_write<3>(L, cs, w, lane);
    BAR();
    if (has_tile && w >= 4) sfor<4, 9>([&](auto S) { stage_dma(rw2, rwo, vob, S, w - 4, cc); });
    const RowStat st = row_stats(ssum, ssq, h);
    *(LAS float *)(L + L_TOK + w * 128 + 4 * (lane & 31)) = st.rstd;
    acc[0] = mfma(waug(cs_read(L, 0, lane), 0), st.xaug, acc[0]);
    acc[1] = mfma(st.xaug, waug(cs_read(L, 1, lane), 0), acc[1]);
    acc[2] = mfma(waug(cs_read(L, 2, lane), 0), st.xaug, acc[2]);
    u32x4 wp[2];
    {
      const int blk = (lane >> 4) & 1, q = (lane & 15) >> 2, p = lane & 3;
      const u32 col = (blk && p == 3) ? 24 : 16 * blk + 4 * p;
      for (int s = 0; s < 2; ++s) {
        u32x2 r[2];
        for (int t = 0; t < 2; ++t) {
          const int dp = 16 * s + 8 * t + 4 * h + q;
          const u32 a = (dp >= 24 && dp < 28) ? L_ZERO + 8 * p : L_WPHI + (dp >= 28 ? dp - 4 : dp) * 56 + col * 2;
          r[t] = trd(L + a);
        }
        wp[s] = u32x4{r[0].x, r[0].y, r[1].x, r[1].y};
      }
    }
    u32x4 idf[2];
    for (int s = 0; s < 2; ++s)
      for (int d = 0; d < 4; ++d) {
        const int k0 = 16 * s + 8 * (d >> 1) + 4 * h + 2 * (d & 1), n = lane & 31;
        idf[s][d] = (n == k0 ? 0x3F80u : 0u) | (n == k0 + 1 ? 0x3F800000u : 0u);
      }
    f32x16 kvp[2];
    {
      f32x16 P = mfma(wp[0], pack8(acc[0], 0), zero16());
      P = mfma(wp[1], pack8(acc[0], 1), P);
      u32x4 pf[2][2];
      feature_map(P, st.rstd, h, pf);
      f32x16 v = acc[1];
      for (int g = 0; g < 4; ++g) {
        const f32x4 r = *(LAS f32x4 *)(L + L_TOK + w * 128 + (8 * g + 4 * h) * 4);
        for (int e = 0; e < 4; ++e) v[4 * g + e] *= r[e];
      }
      u32x4 vf[2] = {pack8(v, 0), pack8(v, 1)};
      if ((lane & 31) == 24) { vf[0] = u32x4{0x3F803F80u, 0x3F803F80u, 0x3F803F80u, 0x3F803F80u}; vf[1] = vf[0]; }
      for (int t = 0; t < 2; ++t) {
        f32x16 Z = mfma(pf[t][0], idf[0], zero16());
        Z = mfma(pf[t][1], idf[1], Z);
        kvp[t] = mfma(pack8(Z, 0), vf[0], zero16());
        kvp[t] = mfma(pack8(Z, 1), vf[1], kvp[t]);
      }
    }
    for (int t = 0; t < 2; ++t)
      for (int g = 0; g < 4; ++g)
        *(LAS f32x4 *)(L + w * 8192 + (t * 4 + g) * 1024 + lane * 16) = f32x4{kvp[t][4 * g], kvp[t][4 * g + 1], kvp[t][4 * g + 2], kvp[t][4 * g + 3]};
    BAR();
    {
      f32x4 s = *(LAS f32x4 *)(L + tid * 16);
      for (int ww = 1; ww < 8; ++ww) s += *(LAS f32x4 *)(L + ww * 8192 + tid * 16);
      st128_sc1(__builtin_bit_cast(u32x4, s), rws, tid * 16, WS_KVP + (head * 8 + rg) * 8192);
    }
    vmwait<0>();
    BAR();
    if (tid == 0) st_flag(gws, WS_AF + b * 128, E);
    if (has_tile && w >= 4) sfor<0, 4>([&](auto S) { stage_dma(rw2, rwo, vob, S, w - 4, cc); });
    u32x4 pq[2][2];
    {
      f32x16 P = mfma(wp[0], pack8(acc[2], 0), zero16());
      P = mfma(wp[1], pack8(acc[2], 1), P);
      feature_map(P, st.rstd, h, pq);
    }
    poll_all(gws, WS_AF + att_block(head, lane & 7) * 128, E);
    {
      f32x4 s = {0.f, 0.f, 0.f, 0.f};
      u32x4 part[8];
      for (int r = 0; r < 8; ++r) part[r] = ld128_sc1(rws, tid * 16, WS_KVP + (head * 8 + r) * 8192);
      for (int r = 0; r < 8; ++r) s += __builtin_bit_cast(f32x4, part[r]);
      const int quad = w & 3, tile = w >> 2;
      u32x2 pk = {pk2(s.x, s.y), pk2(s.z, s.w)};
      if (quad == 3 && h) pk = u32x2{0, 0};
      *(LAS u32x2 *)(L + L_KVF + ((tile * 2 + (quad >> 1)) * 64 + lane) * 16 + (quad & 1) * 8) = pk;
    }
    BAR();
    {
      f32x16 num = zero16();
      for (int t = 0; t < 2; ++t)
        for (int s = 0; s < 2; ++s) num = mfma(*(LAS u32x4 *)(L + L_KVF + ((t * 2 + s) * 64 + lane) * 16), pq[t][s], num);
      const float r = __builtin_amdgcn_rcpf(half_lo(num[12]) + 1e-6f);
      for (int i = 0; i < 16; ++i) num[i] *= r;
      if (!h) { num[12] = 0.f; num[13] = 0.f; num[14] = 0.f; num[15] = 0.f; }
      const u32 so = WS_O + ((8 * rg + w) * 32 + 2 * head) * 1024;
      st128_sc1(pack8(num, 0), rws, lane * 16, so);
      st128_sc1(pack8(num, 1), rws, lane * 16, so + 1024);
    }
    vmwait<0>();
    BAR();
    if (tid == 0) st_flag(gws, WS_OF + b * 128, E);
    if (!has_tile) {
      if (b == 8 * 12 && w == 0) {
        sfor<0, 4>([&](auto I) { poll_all(gws, WS_AF + (64 * I + lane) * 128, E); });
        if (lane == 0) st_flag(gws, WS_EPOCH, E);
      }
      return;
    }
  } else if (slot < 30) {
    const int j = slot - 16, hi = j >= 7, ct = 7 * (xcd & 1) + (j - 7 * hi), rg = 2 * (xcd >> 1) + hi;
    const rsrc_t rw = __builtin_amdgcn_make_buffer_rsrc((void *)gw1, 0, DM * W1ROW, 0x00020000);
    u32 bias[4];
    for (int t = 0; t < 4; ++t)
      asm volatile("global_load_ushort %0, %1, off" : "=v"(bias[t]) : "v"(gb1 + (128 * ct + 32 * t + (lane & 31)) * 2) : "memory");
    f32x16 acc[4]; float ssum, ssq, cs[4];
    gemmA<false>(L, rx, rw, rfl((256 * rg + 32 * w) * XROW), rfl(256 * ct), w, lane, acc, ssum, ssq, cs);
    asm volatile("" : "+v"(ev), "+v"(bias[0]), "+v"(bias[1]), "+v"(bias[2]), "+v"(bias[3]));
    E = rfl(ev) + 1;
    cs_write<4>(L, cs, w, lane);
    BAR();
    const RowStat st = row_stats(ssum, ssq, h);
    for (int t = 0; t < 4; ++t) {
      f32x16 a = mfma(waug(cs_read(L, t, lane), bias[t] | (bias[t] << 16)), st.xaug, acc[t]);
      for (int i = 0; i < 16; ++i) { const float r = fmaxf(a[i], 0.f) * st.rstd; a[i] = r * r; }
      const u32 so = WS_H + ((8 * rg + w) * 112 + 8 * ct + 2 * t) * 1024;
      st128_sc1(pack8(a, 0), rws, lane * 16, so);
      st128_sc1(pack8(a, 1), rws, lane * 16, so + 1024);
    }
    if (w >= 4) { sfor<0, 9>([&](auto S) { stage_dma(rw2, rwo, vob, S, w - 4, cc); }); vmwait<36>(); }
    else vmwait<0>();
    BAR();
    if (tid == 0) st_flag(gws, WS_AF + b * 128, E);
  } else {
    if (w >= 4) { sfor<0, 9>([&](auto S) { stage_dma(rw2, rwo, vob, S, w - 4, cc); }); vmwait<36>(); }
    else vmwait<0>();
    asm volatile("" : "+v"(ev));
    E = rfl(ev) + 1;
    BAR();
    if (tid == 0) st_flag(gws, WS_AF + b * 128, E);
  }

  const int rg = xcd;
  if (w >= 4) {
    const int lw = w - 4;
    u32 ofl = 0;
    sfor<0, 18>([&](auto S) {
      constexpr int s = S;
      if constexpr (s == 0) vmwait<28>();
      else if constexpr (s <= 10) vmwait<24>();
      else if constexpr (s == 11) vmwait<20 + 0>();
      else if constexpr (s == 12) {
        vmwait<0>();
        asm volatile("" : "+v"(ofl));
        if (!__all(ofl == E)) poll_all(gws, WS_OF + att_block(lane & 15, rg) * 128, E);
      }
      asm volatile("s_barrier" ::: "memory");
      if constexpr (s >= 1 && s <= 9) stage_dma(rw2, rwo, vob, s + 8, lw, cc);
      if constexpr (s == 9) asm volatile("global_load_dword %0, %1, off sc1" : "=v"(ofl) : "v"(gws + WS_OF + att_block(lane & 15, rg) * 128) : "memory");
    });
    asm volatile("s_barrier" ::: "memory");
    return;
  }
  {
    const int rh = w & 1, kg = w >> 1;
    const u32 rb = 8 * rg + 2 * rt + rh;
    const u32 row = 32 * rb + (lane & 31), col0 = 64 * cc + 32 * kg;
    u32x2 xres[4], b2v[4];
    for (int g = 0; g < 4; ++g) {
      xres[g] = *(const GAS u32x2 *)(gx + row * XROW + (col0 + 8 * g + 4 * h) * 2);
      b2v[g] = *(const GAS u32x2 *)(gb2 + (col0 + 8 * g + 4 * h) * 2);
    }
    poll_all(gws, WS_AF + ffn_block(lane < 14 ? lane : 0, rg) * 128, E);
    constexpr int RD = 8;
    const u32 soh = WS_H + (rb * 112 + 56 * kg) * 1024, soo = WS_O + (rb * 32 + 16 * kg) * 1024;
    u32x4 ring[RD];
    for (int i = 0; i < RD; ++i) ring[i] = ld128_sc1(rws, lane * 16, soh + i * 1024);
    __builtin_amdgcn_sched_barrier(0);
    const int blk = (lane >> 4) & 1, q = (lane & 15) >> 2, p = lane & 3;
    lchar *bb[2];
    for (int c2 = 0; c2 < 2; ++c2) bb[c2] = L + kg * 8192 + (4 * h + q) * 128 + ((c2 ^ (q >> 1)) * 64) + 32 * blk + 8 * p;
    f32x16 af[2] = {zero16(), zero16()}, aa[2] = {zero16(), zero16()};
    u32x4 bf[2][2];
    sfor<0, 72>([&](auto I) {
      constexpr int i = I, s = i >> 2;
      if constexpr ((i & 3) == 0) asm volatile("s_barrier" ::: "memory");
      if constexpr (i == 0) for (int c2 = 0; c2 < 2; ++c2) bf[0][c2] = trfrag(bb[c2], 1024);
      const u32x4 a = ring[i % RD];
      if constexpr (i + 1 < 72) {
        constexpr int n = i + 1, so = ((n >> 2) % NSLOT) * L_SLOT + (n & 3) * 2048;
        for (int c2 = 0; c2 < 2; ++c2) bf[n & 1][c2] = trfrag(bb[c2] + so, 1024);
      }
      if constexpr (i < 56) { af[0] = mfma(bf[i & 1][0], a, af[0]); af[1] = mfma(bf[i & 1][1], a, af[1]); }
      else { aa[0] = mfma(bf[i & 1][0], a, aa[0]); aa[1] = mfma(bf[i & 1][1], a, aa[1]); }
      if constexpr (i + RD < 56) ring[i % RD] = ld128_sc1(rws, lane * 16, soh + (i + RD) * 1024);
      else if constexpr (i + RD < 72) ring[i % RD] = ld128_sc1(rws, lane * 16, soo + (i + RD - 56) * 1024);
      __builtin_amdgcn_sched_barrier(0);
      (void)s;
    });
    {
      const f32x16 &sf = kg ? af[0] : af[1], &sa = kg ? aa[0] : aa[1];
      for (int g = 0; g < 4; ++g) {
        *(LAS f32x4 *)(L + w * 8192 + g * 1024 + lane * 16) = f32x4{sf[4 * g], sf[4 * g + 1], sf[4 * g + 2], sf[4 * g + 3]};
        *(LAS f32x4 *)(L + w * 8192 + 4096 + g * 1024 + lane * 16) = f32x4{sa[4 * g], sa[4 * g + 1], sa[4 * g + 2], sa[4 * g + 3]};
      }
    }
    BAR();
    f32x16 yf = kg ? af[1] : af[0], ya = kg ? aa[1] : aa[0];
    u32x2 o2[4];
    for (int g = 0; g < 4; ++g) {
      const f32x4 pf = *(LAS f32x4 *)(L + (w ^ 2) * 8192 + g * 1024 + lane * 16), pa = *(LAS f32x4 *)(L + (w ^ 2) * 8192 + 4096 + g * 1024 + lane * 16);
      float r[4];
      for (int e = 0; e < 4; ++e) {
        const u32 bw = b2v[g][e >> 1], xw = xres[g][e >> 1];
        const float y = yf[4 * g + e] + pf[e] + ((e & 1) ? bfhi(bw) : bf2f(bw));
        const float sg = y * __builtin_amdgcn_rcpf(1.0f + __builtin_amdgcn_exp2f(-LOG2E * y));
        r[e] = __builtin_fmaf(ya[4 * g + e] + pa[e], sg, (e & 1) ? bfhi(xw) : bf2f(xw));
      }
      o2[g] = u32x2{pk2(r[0], r[1]), pk2(r[2], r[3])};
    }
    for (int g = 0; g < 4; g += 2) {
      auto rx0 = __builtin_amdgcn_permlane32_swap(o2[g].x, o2[g + 1].x, false, false);
      auto ry0 = __builtin_amdgcn_permlane32_swap(o2[g].y, o2[g + 1].y, false, false);
      *(GAS u32x4 *)(gout + row * XROW + (col0 + 8 * (g + h)) * 2) = u32x4{rx0[0], ry0[0], rx0[1], ry0[1]};
    }
  }
}

extern "C" void kernel_launch(void *const *d_in, const int *in_sizes, int n_in, void *d_out, int out_size, void *d_ws, size_t ws_size, hipStream_t stream) {
  fused_hedgehog_ffn<<<256, 512, 0, stream>>>((const char *)d_in[0], (const char *)d_in[1], (const char *)d_in[2], (const char *)d_in[3], (const char *)d_in[5],
                                              (const char *)d_in[6], (const char *)d_in[7], (const char *)d_in[8], (char *)d_out, (char *)d_ws);
}
```

```cpp
#include <hip/hip_runtime.h>
#include <hip/hip_bf16.h>
#include <stdint.h>
#include <type_traits>

typedef unsigned u32;
typedef u32 u32x4 __attribute__((ext_vector_type(4)));
typedef u32 u32x2 __attribute__((ext_vector_type(2)));
typedef float f32x16 __attribute__((ext_vector_type(16)));
typedef float f32x4 __attribute__((ext_vector_type(4)));
typedef float f32x2 __attribute__((ext_vector_type(2)));
typedef short s16x8 __attribute__((ext_vector_type(8)));
typedef short s16x4 __attribute__((ext_vector_type(4)));
typedef __bf16 bf16x2 __attribute__((ext_vector_type(2)));
typedef __bf16 bf16x8 __attribute__((ext_vector_type(8)));
#define LAS __attribute__((address_space(3)))
#define GAS __attribute__((address_space(1)))
#define DI __device__ __forceinline__
typedef __amdgpu_buffer_rsrc_t rsrc_t;
typedef LAS char lchar;

namespace {
constexpr int DM = 448, DF = 1792;
constexpr u32 XROW = DM * 2;
constexpr u32 QKVROW = 3 * DM * 2;
constexpr u32 W1ROW = DF * 2;
constexpr u32 WS_EPOCH = 0;
constexpr u32 WS_STAT = 4096;
constexpr u32 WS_KF = 65536;
constexpr u32 WS_HF = 131072;
constexpr u32 WS_OF = 196608;
constexpr u32 WS_KVP = 262144;
constexpr u32 WS_H = WS_KVP + (1u << 20);
constexpr u32 WS_O = WS_H + 64 * 112 * 1024;
constexpr u32 WS_END = WS_O + 64 * 32 * 1024;
constexpr u32 L_SLOT = 16384, NSLOT = 9;
constexpr u32 A_SLOT = 49152, A_WOFF = 32768;
constexpr u32 L_MISC = NSLOT * L_SLOT;
constexpr u32 L_WPHI = L_MISC;
constexpr u32 L_ZERO = L_MISC + 2048;
constexpr u32 L_E = L_MISC + 2112;
constexpr u32 L_DUMMY = L_MISC + 2304;
constexpr u32 L_CSP = L_MISC + 2560;
constexpr u32 L_TOK = L_CSP + 2048;
constexpr u32 L_KVF = L_TOK + 1024;
constexpr u32 L_END = L_KVF + 4096;
constexpr u32 LDS_BYTES = 163840;
static_assert(L_END <= LDS_BYTES, "lds");
constexpr int SPIN_CAP = 1 << 16;
constexpr float LOG2E = 1.4426950408889634f;
#define PF_N 0
#define RING 16

#define STAMP(i) do {} while (0)
#define STAMP_DECL do {} while (0)
#define STAMP_OUT() do {} while (0)
#define STAMP_ARG
#define STAMP_PASS
template <int B, int E, class F> DI void sfor(F &&f) {
  if constexpr (B < E) { f(std::integral_constant<int, B>{}); sfor<B + 1, E>(f); }
}
DI u32 rfl(u32 v) { return __builtin_amdgcn_readfirstlane(v); }
DI f32x16 mfma(u32x4 a, u32x4 b, f32x16 c) {
  return __builtin_amdgcn_mfma_f32_32x32x16_bf16(__builtin_bit_cast(bf16x8, a), __builtin_bit_cast(bf16x8, b), c, 0, 0, 0);
}
DI u32 pk2(float lo, float hi) { f32x2 p = {lo, hi}; return __builtin_bit_cast(u32, __builtin_convertvector(p, bf16x2)); }
DI float dot2(u32 a, u32 b, float c) { return __builtin_amdgcn_fdot2_f32_bf16(__builtin_bit_cast(bf16x2, a), __builtin_bit_cast(bf16x2, b), c, false); }
DI float bf2f(u32 lo16) { return __builtin_bit_cast(float, lo16 << 16); }
DI float bfhi(u32 v) { return __builtin_bit_cast(float, v & 0xffff0000u); }
DI float asf(u32 v) { return __builtin_bit_cast(float, v); }
DI f32x2 both_halves(float v) {
  const u32 a = __builtin_bit_cast(u32, v);
  auto r = __builtin_amdgcn_permlane32_swap(a, a, false, false);
  return f32x2{asf(r[0]), asf(r[1])};
}
DI float half_lo(float v) { return both_halves(v).x; }
DI u32x4 pack8(const f32x16 &x, int s) { return u32x4{pk2(x[8 * s], x[8 * s + 1]), pk2(x[8 * s + 2], x[8 * s + 3]), pk2(x[8 * s + 4], x[8 * s + 5]), pk2(x[8 * s + 6], x[8 * s + 7])}; }
DI u32x2 trd(lchar *p) {
  typedef short v4i16 __attribute__((ext_vector_type(4)));
  return __builtin_bit_cast(u32x2, __builtin_amdgcn_ds_read_tr16_b64_v4i16((LAS v4i16 *)p));
}
DI u32x4 trfrag(lchar *p, int tstride) { u32x2 a = trd(p), b = trd(p + tstride); return u32x4{a.x, a.y, b.x, b.y}; }
DI f32x16 zero16() { f32x16 z; for (int i = 0; i < 16; ++i) z[i] = 0.f; return z; }
#define BAR() asm volatile("s_waitcnt lgkmcnt(0)\n\ts_barrier" ::: "memory")
template <int N> DI void vmwait() { asm volatile("s_waitcnt vmcnt(%0)" ::"i"(N) : "memory"); }
DI void dma16(rsrc_t r, u32 voff, u32 soff, u32 ldsaddr) {
  asm volatile("s_mov_b32 m0, %0\n\ts_nop 0\n\tbuffer_load_dwordx4 %1, %2, %3 offen lds" ::"s"(ldsaddr), "v"(voff), "s"(r), "s"(soff) : "memory", "m0");
}
template <int OFF> DI void ald128(u32x4 &d, rsrc_t r, u32 voff, u32 soff) {
  asm volatile("buffer_load_dwordx4 %0, %1, %2, %3 offen offset:%4" : "=v"(d) : "v"(voff), "s"(r), "s"(soff), "i"(OFF) : "memory");
}
DI u32x4 ld128_sc1(rsrc_t r, u32 voff, u32 soff) { return __builtin_amdgcn_raw_buffer_load_b128(r, voff, soff, 16); }
DI void st128_sc1(u32x4 v, rsrc_t r, u32 voff, u32 soff) {
  __builtin_amdgcn_raw_buffer_store_b128(v, r, voff, soff, 16);
  asm volatile("s_nop 1" ::"v"(v) : "memory");
}
typedef GAS u32 gu32;
DI u32 ld_flag(const char *ws, u32 off) { return __hip_atomic_load((gu32 *)(ws + off), __ATOMIC_RELAXED, __HIP_MEMORY_SCOPE_AGENT); }
DI void st_flag(char *ws, u32 off, u32 v) { __hip_atomic_store((gu32 *)(ws + off), v, __ATOMIC_RELAXED, __HIP_MEMORY_SCOPE_AGENT); }
DI void poll_all(const char *ws, u32 off, u32 E) {
#pragma unroll 1
  for (int spin = 0; spin < SPIN_CAP; ++spin) {
    if (__all(ld_flag(ws, off) == E)) break;
    __builtin_amdgcn_s_sleep(1);
  }
  __builtin_amdgcn_fence(__ATOMIC_ACQUIRE, "wavefront");
}

template <u32 LI, u32 SI> DI void dmai(rsrc_t r, u32 voff, u32 sob, u32 ldsb) {
  u32 t;
  asm volatile("s_add_u32 m0, %2, %5\n\ts_add_u32 %0, %1, %6\n\tbuffer_load_dwordx4 %3, %4, %0 offen lds"
               : "=&s"(t) : "s"(sob), "s"(ldsb), "v"(voff), "s"(r), "i"(LI), "i"(SI) : "memory", "m0", "scc");
}
template <int CTRL, int RMASK> DI float dpp_add(float v) {
  return v + asf(__builtin_amdgcn_update_dpp(0u, __builtin_bit_cast(u32, v), CTRL, RMASK, 0xf, true));
}
DI float wave_sum63(float v) {
  v = dpp_add<0x111, 0xf>(v); v = dpp_add<0x112, 0xf>(v); v = dpp_add<0x114, 0xf>(v); v = dpp_add<0x118, 0xf>(v);
  v = dpp_add<0x142, 0xa>(v); v = dpp_add<0x143, 0xc>(v);
  return v;
}
DI void st64_sc1(char *ws, u32 off, u32 lo, u32 hi) {
  __hip_atomic_store((GAS unsigned long long *)(ws + off), (unsigned long long)lo | ((unsigned long long)hi << 32), __ATOMIC_RELAXED, __HIP_MEMORY_SCOPE_AGENT);
}
DI u32 early_stats(char *ws, rsrc_t rx, int row0, int lane) {
  const u32 ev = ld_flag(ws, WS_EPOCH);
  const u32 vo = lane < 56 ? (u32)lane * 16 : 0x7ffffff0u;
  const u32x4 xa = __builtin_amdgcn_raw_buffer_load_b128(rx, vo, row0 * XROW, 0), xb = __builtin_amdgcn_raw_buffer_load_b128(rx, vo, (row0 + 1) * XROW, 0);
  const u32 E = rfl(ev) + 1, ONES = 0x3F803F80u;
  float s[2], q[2];
  for (int r = 0; r < 2; ++r) {
    const u32x4 &x = r ? xb : xa;
    s[r] = dot2(x[0], ONES, 0.f); q[r] = dot2(x[0], x[0], 0.f);
    for (int d = 1; d < 4; ++d) { s[r] = dot2(x[d], ONES, s[r]); q[r] = dot2(x[d], x[d], q[r]); }
    s[r] = wave_sum63(s[r]); q[r] = wave_sum63(q[r]);
    const float mean = s[r] * (1.0f / DM);
    const float sig = __builtin_sqrtf(fmaxf(q[r] * (1.0f / DM) - mean * mean, 0.f) + 1e-5f);
    if (lane == 63) { st64_sc1(ws, WS_STAT + (row0 + r) * 16, __builtin_bit_cast(u32, mean), E); st64_sc1(ws, WS_STAT + (row0 + r) * 16 + 8, __builtin_bit_cast(u32, sig), E); }
  }
  return E;
}
DI u32x4 stat_settle(u32x4 g, rsrc_t rws, u32 vostat, u32 E) {
#pragma unroll 1
  for (int spin = 0; spin < SPIN_CAP && !__all(g[1] == E && g[3] == E); ++spin) { __builtin_amdgcn_s_sleep(1); g = ld128_sc1(rws, vostat, 0); }
  return g;
}
struct RowStat { float rstd; u32x4 xaug; };
DI RowStat row_stats(u32x4 g, int h) {
  const float m = -asf(g[0]), sig = asf(g[2]);
  RowStat r; r.rstd = __builtin_amdgcn_rcpf(sig);
  const float ml = m - bfhi(pk2(m, m)), sl = sig - bfhi(pk2(sig, sig));
  const u32 mm = pk2(m, ml), ss = pk2(sig, sl);
  r.xaug = h ? u32x4{0, 0, 0, 0} : u32x4{mm, mm, ss, 0};
  return r;
}
DI u32x4 waug(float cs, u32 bb) {
  const u32 hh = pk2(cs, cs); const float cl = cs - bfhi(hh);
  return u32x4{hh, pk2(cl, cl), bb, 0};
}
DI float cs_read(lchar *L, int t, int lane) {
  const f32x4 a = *(LAS f32x4 *)(L + L_CSP + (t * 32 + (lane & 31)) * 16);
  return (a.x + a.y) + (a.z + a.w);
}

template <bool ATT, class Mid>
DI void gemmA_compute(lchar *L, int w, int lane, f32x16 (&acc)[2][ATT ? 3 : 4], Mid &&mid STAMP_ARG) {
  constexpr int T = ATT ? 3 : 4, ROWB = ATT ? 64 : 256;
  const int h = lane >> 5, blk = (lane >> 4) & 1, q = (lane & 15) >> 2, p = lane & 3;
  const u32 lb = (u32)(size_t)L;
  u32 wb0[T], xb0[4], wb[T], xb[4];
  for (int t = 0; t < T; ++t) wb0[t] = lb + A_WOFF + (ATT ? t * 4096 + (8 * h + q) * 64 : (8 * h + q) * 256 + 64 * (t ^ q)) + 32 * blk + 8 * p;
  for (int u = 0; u < 4; ++u) xb0[u] = lb + w * 8192 + (lane & 31) * 128 + 16 * ((2 * u + h) ^ ((lane >> 1) & 7));
  for (int r = 0; r < 2; ++r) for (int t = 0; t < T; ++t) acc[r][t] = zero16();
  u32x4 xf[2][2], wf[2][T];
  sfor<0, 29>([&](auto I) {
    constexpr int i = I, c = i >> 2, u = i & 3;
    if constexpr (u == 0 && i < 28) {
      BAR();
      if constexpr (c == 0) STAMP(1);
      constexpr u32 so = (c % 3) * A_SLOT;
      for (int t = 0; t < T; ++t) { wb[t] = wb0[t] + so; asm volatile("" : "+v"(wb[t])); }
      for (int j = 0; j < 4; ++j) { xb[j] = xb0[j] + so; asm volatile("" : "+v"(xb[j])); }
    }
    if constexpr (i < 28) {
      for (int r = 0; r < 2; ++r) xf[i & 1][r] = *(LAS u32x4 *)((lchar *)(size_t)xb[u] + r * 4096);
      for (int t = 0; t < T; ++t) wf[i & 1][t] = trfrag((lchar *)(size_t)wb[t] + 16 * u * ROWB, 4 * ROWB);
    }
    if constexpr (i == 20) mid();
    if constexpr (i > 0) {
      constexpr int j = (i - 1) & 1;
      for (int t = 0; t < T; ++t)
        for (int r = 0; r < 2; ++r) acc[r][t] = (ATT && t == 1) ? mfma(xf[j][r], wf[j][t], acc[r][t]) : mfma(wf[j][t], xf[j][r], acc[r][t]);
    }
    __builtin_amdgcn_sched_barrier(0);
  });
  STAMP(2);
}
template <bool ATT>
DI void gemmA_loader(lchar *L, rsrc_t rx, rsrc_t rw, u32 sox, u32 sow, const char *gw2, const char *gwo, u32 pfu, int lw, int lane STAMP_ARG) {
  constexpr int T = ATT ? 3 : 4, ROWB = ATT ? 64 : 256, NW = ATT ? 3 : 4, N = 8 + NW, PF = PF_N;
  const u32 lb = (u32)(size_t)L;
  const u32 vox0 = (lane >> 3) * XROW + 16 * ((lane & 7) ^ ((lane >> 4) & 3)), vox1 = (lane >> 3) * XROW + 16 * ((lane & 7) ^ (4 + ((lane >> 4) & 3)));
  const u32 ldx = rfl(lb + lw * 8192);
  u32 vow, sw[NW], ldw[NW];
  if constexpr (ATT) {
    const u32 c = lane & 3;
    vow = (lane >> 2) * QKVROW + (c == 3 ? 40 : 16 * c);
    for (int i = 0; i < NW; ++i) { const u32 id = 3 * lw + i, t = id >> 2, rb = id & 3; sw[i] = rfl(sow + 16 * rb * QKVROW + (t == 0 ? 896u : t == 1 ? 1792u : 0u)); ldw[i] = rfl(lb + A_WOFF + t * 4096 + rb * 1024); }
  } else {
    vow = (lane >> 4) * W1ROW + 16 * ((lane & 15) ^ ((lane >> 4) << 2));
    for (int i = 0; i < NW; ++i) { sw[i] = rfl(sow + (16 * lw + 4 * i) * W1ROW); ldw[i] = rfl(lb + A_WOFF + (16 * lw + 4 * i) * 256); }
  }
  auto issue = [&](auto C) {
    constexpr u32 c = C, so = (c % 3) * A_SLOT;
    sfor<0, NW>([&](auto I) { dmai<so, 64 * c * (ATT ? QKVROW : W1ROW)>(rw, vow, sw[I], ldw[I]); });
    sfor<0, 8>([&](auto I) { constexpr u32 i = I; dmai<so + i * 1024, 8 * i * XROW + 128 * c>(rx, (i & 1) ? vox1 : vox0, sox, ldx); });
  };
  issue(std::integral_constant<int, 0>{});
  issue(std::integral_constant<int, 1>{});
  const int h = lane >> 5, blk = (lane >> 4) & 1, q = (lane & 15) >> 2, p = lane & 3;
  u32 cb0[T];
  for (int t = 0; t < T; ++t) cb0[t] = lb + A_WOFF + (ATT ? t * 4096 + (8 * h + q) * 64 : (8 * h + q) * 256 + 64 * (t ^ q)) + 32 * blk + 8 * p + lw * 16 * ROWB;
  f32x2 ca[T];
  for (int t = 0; t < T; ++t) ca[t] = f32x2{0.f, 0.f};
  sfor<0, 7>([&](auto C) {
    constexpr int c = C;
    vmwait<(c == 6 ? 0 : N) + ((c >= 1 && c - 1 < PF) ? 1 : 0)>();
    BAR();
    if constexpr (c == 0) STAMP(1);
    if constexpr (c < PF) {
      const u32 u = pfu + c;
      const bool a = u < 392;
      const rsrc_t rp = __builtin_amdgcn_make_buffer_rsrc((void *)(a ? gw2 : gwo), 0, a ? DF * XROW : DM * XROW, 0x00020000);
      asm volatile("s_mov_b32 m0, %0\n\ts_nop 0\n\tbuffer_load_dword %1, %2, %3 offen lds" ::"s"(lb + L_DUMMY), "v"(lane * 64), "s"(rp), "s"(rfl((a ? u : u - 392) * 4096)) : "memory", "m0");
    }
    if constexpr (c + 2 < 7) issue(std::integral_constant<int, c + 2>{});
    u32x4 cf[T];
    for (int t = 0; t < T; ++t) cf[t] = trfrag((lchar *)(size_t)(cb0[t] + (c % 3) * A_SLOT), 4 * ROWB);
    for (int t = 0; t < T; ++t) {
      for (int d = 0; d < 4; ++d) ca[t] += f32x2{bf2f(cf[t][d]), bfhi(cf[t][d])};
      asm volatile("" : "+v"(ca[t]));
    }
  });
  for (int t = 0; t < T; ++t) { f32x2 a = both_halves(ca[t].x + ca[t].y); if (lane < 32) *(LAS float *)(L + L_CSP + (t * 32 + lane) * 16 + 4 * lw) = a.x + a.y; }
  STAMP(2);
}

DI void feature_map(const f32x16 &P, float rstd, int h, u32x4 (&pf)[2][2]) {
  const float c = rstd * LOG2E;
  float mx = fmaxf(fmaxf(P[0], P[1]), P[2]), mn = fminf(fminf(P[0], P[1]), P[2]);
  for (int i = 3; i < 15; i += 2) { mx = fmaxf(fmaxf(mx, P[i]), P[i + 1]); mn = fminf(fminf(mn, P[i]), P[i + 1]); }
  mx = fmaxf(mx, P[15]); mn = fminf(mn, P[15]);
  { f32x2 a = both_halves(mx), b = both_halves(mn); mx = fmaxf(a.x, a.y); mn = fminf(b.x, b.y); }
  const float mxc = -mx * c, mnc = mn * c;
  f32x16 ep, en;
  for (int i = 0; i < 16; ++i) { ep[i] = __builtin_amdgcn_exp2f(__builtin_fmaf(P[i], c, mxc)); en[i] = __builtin_amdgcn_exp2f(__builtin_fmaf(P[i], -c, mnc)); }
  float sp = 0.f, sn = 0.f, sp2 = 0.f, sn2 = 0.f;
  for (int i = 0; i < 12; i += 2) { sp += ep[i]; sp2 += ep[i + 1]; sn += en[i]; sn2 += en[i + 1]; }
  const float keep = h ? 0.f : 1.f;
  sp = (sp + sp2) + keep * ((ep[12] + ep[13]) + (ep[14] + ep[15]));
  sn = (sn + sn2) + keep * ((en[12] + en[13]) + (en[14] + en[15]));
  { f32x2 a = both_halves(sp), b = both_halves(sn); sp = a.x + a.y; sn = b.x + b.y; }
  const float rp = __builtin_amdgcn_rcpf(sp), rn = __builtin_amdgcn_rcpf(sn);
  for (int i = 0; i < 16; ++i) { ep[i] *= rp; en[i] *= rn; }
  pf[0][0] = pack8(ep, 0); pf[0][1] = pack8(ep, 1); pf[1][0] = pack8(en, 0); pf[1][1] = pack8(en, 1);
}

struct StageCtx { rsrc_t rw2, rwo; u32 vob, so2, soo, ld; };
template <int ST> DI void stage_dma(const StageCtx &c) {
  sfor<0, 4>([&](auto I) {
    constexpr u32 i = I, dst = ((ST + 4) % NSLOT) * L_SLOT + i * 1024;
    if constexpr (ST < 14) dmai<dst, (64 * ST + 8 * i) * XROW>(c.rw2, c.vob, c.so2, c.ld);
    else dmai<dst, (56 * (ST - 14) + (i == 3 ? 20 : 8 * i)) * XROW>(c.rwo, c.vob, c.soo, c.ld);
  });
}
}

__global__ void __launch_bounds__(512, 2) fused_hedgehog_ffn(const char *__restrict__ gx, const char *__restrict__ gwqkv, const char *__restrict__ gwphi, const char *__restrict__ gwo,
                                                              const char *__restrict__ gw1, const char *__restrict__ gb1, const char *__restrict__ gw2, const char *__restrict__ gb2,
                                                              char *__restrict__ gout, char *__restrict__ gws) {
  __shared__ __attribute__((aligned(1024))) char smem[LDS_BYTES];
  lchar *L = (lchar *)smem;
  const int tid = threadIdx.x, lane = tid & 63, w = rfl(tid >> 6);
  const int b = blockIdx.x, xcd = b & 7, slot = b >> 3;
  const int h = lane >> 5;
  STAMP_DECL; STAMP(0);
  const rsrc_t rx = __builtin_amdgcn_make_buffer_rsrc((void *)gx, 0, 2048 * XROW, 0x00020000);
  const rsrc_t rws = __builtin_amdgcn_make_buffer_rsrc((void *)gws, 0, WS_END, 0x00020000);
  const int tc = slot >= 16 ? slot - 16 : slot + 16;
  const bool has_tile = tc < 28;
  const u32 rt = tc / 7, cc = tc % 7;
  const bool att = slot < 16, ffn = !att && slot < 30;
  const int head = 8 * (xcd & 1) + (slot & 7);
  const int jf = slot - 16, ct = 7 * (xcd & 1) + (jf >= 7 ? jf - 7 : jf);
  const int rga = 2 * (xcd >> 1) + (att ? (slot >> 3) : (jf >= 7));

  if (w >= 4) {
    const int lw = w - 4;
    StageCtx sc;
    sc.rw2 = __builtin_amdgcn_make_buffer_rsrc((void *)gw2, 0, DF * XROW, 0x00020000);
    sc.rwo = __builtin_amdgcn_make_buffer_rsrc((void *)gwo, 0, DM * XROW, 0x00020000);
    sc.vob = (lane >> 3) * XROW + 16 * ((lane & 7) ^ (((lane >> 4) & 1) << 2));
    sc.so2 = rfl((896 * (lw >> 1) + 32 * (lw & 1)) * XROW + 128 * cc);
    sc.soo = rfl(28 * (8 * (lw >> 1) + (lw & 1)) * XROW + 128 * cc);
    sc.ld = rfl((u32)(size_t)L + (lw >> 1) * 8192 + (lw & 1) * 4096);
    u32 E;
    const u32 pfu = (slot * 4 + lw) * PF_N;
    if (att) {
      const rsrc_t rw = __builtin_amdgcn_make_buffer_rsrc((void *)gwqkv, 0, DM * QKVROW, 0x00020000);
      if (lw < 2) {
        const rsrc_t rp = __builtin_amdgcn_make_buffer_rsrc((void *)gwphi, 0, 16 * 28 * 28 * 2, 0x00020000);
        dma16(rp, lane * 16 + lw * 1024, head * 1568, (u32)(size_t)L + L_WPHI + lw * 1024);
      }
      gemmA_loader<true>(L, rx, rw, rfl((256 * rga + 64 * lw) * XROW), rfl(56 * head), gw2, gwo, pfu, lw, lane STAMP_PASS);
      BAR();
      E = rfl(*(LAS u32 *)(L + L_E));
      if (has_tile) sfor<0, 5>([&](auto S) { stage_dma<S>(sc); });
      BAR();
      STAMP(4);
      for (int t = 0; t < 2; ++t) {
        f32x4 s = *(LAS f32x4 *)(L + (t * 4 + lw) * 1024 + lane * 16);
        for (int ww = 1; ww < 4; ++ww) s += *(LAS f32x4 *)(L + ww * 8192 + (t * 4 + lw) * 1024 + lane * 16);
        st128_sc1(__builtin_bit_cast(u32x4, s), rws, lane * 16, WS_KVP + (head * 8 + rga) * 8192 + (t * 4 + lw) * 1024);
      }
      vmwait<0>();
      if (lane == 0) st_flag(gws, WS_KF + ((head * 8 + rga) * 4 + lw) * 64, E);
      STAMP(5);
      poll_all(gws, WS_KF + ((head * 8 + (lane & 7)) * 4 + lw) * 64, E);
      STAMP(7);
      u32x4 part[2][8];
      for (int t = 0; t < 2; ++t) for (int r = 0; r < 8; ++r) part[t][r] = ld128_sc1(rws, lane * 16, WS_KVP + (head * 8 + r) * 8192 + (t * 4 + lw) * 1024);
      for (int t = 0; t < 2; ++t) {
        f32x4 s = __builtin_bit_cast(f32x4, part[t][0]);
        for (int r = 1; r < 8; ++r) s += __builtin_bit_cast(f32x4, part[t][r]);
        u32x2 pk = {pk2(s.x, s.y), pk2(s.z, s.w)};
        if (lw == 3 && h) pk = u32x2{0, 0};
        *(LAS u32x2 *)(L + L_KVF + ((t * 2 + (lw >> 1)) * 64 + lane) * 16 + (lw & 1) * 8) = pk;
      }
      BAR();
      STAMP(8);
      if (!has_tile) {
        if (b == 8 * 12 && lw == 0) {
#pragma unroll 1
          for (int spin = 0; spin < SPIN_CAP; ++spin) {
            bool ok = true;
            for (int i = 0; i < 32; ++i) ok &= ld_flag(gws, WS_STAT + (64 * i + lane) * 16 + 4) == E;
            if (__all(ok)) break;
            __builtin_amdgcn_s_sleep(8);
          }
          if (lane == 0) st_flag(gws, WS_EPOCH, E);
        }
        STAMP_OUT();
        return;
      }
      sfor<5, 9>([&](auto S) { stage_dma<S>(sc); });
    } else if (ffn) {
      const rsrc_t rw = __builtin_amdgcn_make_buffer_rsrc((void *)gw1, 0, DM * W1ROW, 0x00020000);
      gemmA_loader<false>(L, rx, rw, rfl((256 * rga + 64 * lw) * XROW), rfl(256 * ct), gw2, gwo, pfu, lw, lane STAMP_PASS);
      BAR();
      E = rfl(*(LAS u32 *)(L + L_E));
      sfor<0, 9>([&](auto S) { stage_dma<S>(sc); });
    } else {
      sfor<0, 9>([&](auto S) { stage_dma<S>(sc); });
      BAR();
      E = rfl(*(LAS u32 *)(L + L_E));
    }
    constexpr int SO = (56 - RING) / 4;
    u32 ofl = 0;
    const u32 ofoff = WS_OF + ((8 * xcd + 2 * rt + ((lane >> 4) & 1)) * 16 + (lane & 15)) * 64;
    sfor<0, 18>([&](auto S) {
      constexpr int s = S;
      if constexpr (s == SO) {
        vmwait<12>();
        asm volatile("" : "+v"(ofl));
        if (!__all(ofl == E)) poll_all(gws, ofoff, E);
      }
      else if constexpr (s == 0) vmwait<28>();
      else if constexpr (s <= 10) vmwait<24>();
      else if constexpr (s <= 16) vmwait<4 * (16 - s)>();
      asm volatile("s_barrier" ::: "memory");
      if constexpr (s >= 1 && s <= 9) stage_dma<s + 8>(sc);
      if constexpr (s == SO - 4) asm volatile("global_load_dword %0, %1, off sc1" : "=v"(ofl) : "v"(gws + ofoff) : "memory");
    });
    STAMP(12);
    asm volatile("s_barrier" ::: "memory");
    STAMP_OUT();
    return;
  }

  const u32 E = early_stats(gws, rx, 8 * b + 2 * w, lane);
  if (tid == 0) *(LAS u32 *)(L + L_E) = E;
  if (att) {
    if (tid < 16) *(LAS u32 *)(L + L_ZERO + 4 * tid) = 0;
    f32x16 acc[2][3]; u32x4 stat[2];
    const u32 vostat = WS_STAT + (256 * rga + 64 * w + (lane & 31)) * 16;
    gemmA_compute<true>(L, w, lane, acc, [&]() { for (int r = 0; r < 2; ++r) stat[r] = ld128_sc1(rws, vostat + 512 * r, 0); } STAMP_PASS);
    RowStat st[2];
    for (int r = 0; r < 2; ++r) {
      st[r] = row_stats(stat_settle(stat[r], rws, vostat + 512 * r, E), h);
      *(LAS float *)(L + L_TOK + w * 256 + r * 128 + 4 * (lane & 31)) = st[r].rstd;
    }
    BAR();
    STAMP(3);
    {
      const u32x4 wk = waug(cs_read(L, 0, lane), 0), wv = waug(cs_read(L, 1, lane), 0), wq = waug(cs_read(L, 2, lane), 0);
      for (int r = 0; r < 2; ++r) { acc[r][0] = mfma(wk, st[r].xaug, acc[r][0]); acc[r][1] = mfma(st[r].xaug, wv, acc[r][1]); acc[r][2] = mfma(wq, st[r].xaug, acc[r][2]); }
    }
    u32x4 wp[2];
    {
      const int blk = (lane >> 4) & 1, q = (lane & 15) >> 2, p = lane & 3;
      const u32 col = (blk && p == 3) ? 24 : 16 * blk + 4 * p;
      for (int s = 0; s < 2; ++s) {
        u32x2 r[2];
        for (int t = 0; t < 2; ++t) {
          const int dp = 16 * s + 8 * t + 4 * h + q;
          const u32 a = (dp >= 24 && dp < 28) ? L_ZERO + 8 * p : L_WPHI + (dp >= 28 ? dp - 4 : dp) * 56 + col * 2;
          r[t] = trd(L + a);
        }
        wp[s] = u32x4{r[0].x, r[0].y, r[1].x, r[1].y};
      }
    }
    u32x4 idf[2];
    for (int s = 0; s < 2; ++s)
      for (int d = 0; d < 4; ++d) {
        const int k0 = 16 * s + 8 * (d >> 1) + 4 * h + 2 * (d & 1), n = lane & 31;
        idf[s][d] = (n == k0 ? 0x3F80u : 0u) | (n == k0 + 1 ? 0x3F800000u : 0u);
      }
    f32x16 kvp[2] = {zero16(), zero16()};
    for (int r = 0; r < 2; ++r) {
      f32x16 P = mfma(wp[0], pack8(acc[r][0], 0), zero16());
      P = mfma(wp[1], pack8(acc[r][0], 1), P);
      u32x4 pf[2][2];
      feature_map(P, st[r].rstd, h, pf);
      f32x16 v = acc[r][1];
      for (int g = 0; g < 4; ++g) {
        const f32x4 rs = *(LAS f32x4 *)(L + L_TOK + w * 256 + r * 128 + (8 * g + 4 * h) * 4);
        for (int e = 0; e < 4; ++e) v[4 * g + e] *= rs[e];
      }
      u32x4 vf[2] = {pack8(v, 0), pack8(v, 1)};
      if ((lane & 31) == 24) { vf[0] = u32x4{0x3F803F80u, 0x3F803F80u, 0x3F803F80u, 0x3F803F80u}; vf[1] = vf[0]; }
      for (int t = 0; t < 2; ++t) {
        f32x16 Z = mfma(pf[t][0], idf[0], zero16());
        Z = mfma(pf[t][1], idf[1], Z);
        kvp[t] = mfma(pack8(Z, 0), vf[0], kvp[t]);
        kvp[t] = mfma(pack8(Z, 1), vf[1], kvp[t]);
      }
    }
    for (int t = 0; t < 2; ++t)
      for (int g = 0; g < 4; ++g)
        *(LAS f32x4 *)(L + w * 8192 + (t * 4 + g) * 1024 + lane * 16) = f32x4{kvp[t][4 * g], kvp[t][4 * g + 1], kvp[t][4 * g + 2], kvp[t][4 * g + 3]};
    STAMP(4);
    BAR();
    u32x4 pq[2][2][2];
    for (int r = 0; r < 2; ++r) {
      f32x16 P = mfma(wp[0], pack8(acc[r][2], 0), zero16());
      P = mfma(wp[1], pack8(acc[r][2], 1), P);
      feature_map(P, st[r].rstd, h, pq[r]);
      for (int t = 0; t < 2; ++t) for (int s = 0; s < 2; ++s) asm volatile("" : "+v"(pq[r][t][s]));
    }
    STAMP(6);
    BAR();
    STAMP(8);
    {
      u32x4 kf[2][2];
      for (int t = 0; t < 2; ++t) for (int s = 0; s < 2; ++s) kf[t][s] = *(LAS u32x4 *)(L + L_KVF + ((t * 2 + s) * 64 + lane) * 16);
      for (int r = 0; r < 2; ++r) {
        f32x16 num = zero16();
        for (int t = 0; t < 2; ++t) for (int s = 0; s < 2; ++s) num = mfma(kf[t][s], pq[r][t][s], num);
        const float rc = __builtin_amdgcn_rcpf(both_halves(num[12]).x + 1e-6f);
        for (int i = 0; i < 16; ++i) num[i] *= rc;
        if (!h) { num[12] = 0.f; num[13] = 0.f; num[14] = 0.f; num[15] = 0.f; }
        const u32 so = WS_O + ((8 * rga + 2 * w + r) * 32 + 2 * head) * 1024;
        st128_sc1(pack8(num, 0), rws, lane * 16, so);
        st128_sc1(pack8(num, 1), rws, lane * 16, so + 1024);
      }
    }
    vmwait<0>();
    if (lane < 2) st_flag(gws, WS_OF + ((8 * rga + 2 * w + lane) * 16 + head) * 64, E);
    STAMP(9);
    if (!has_tile) { STAMP_OUT(); return; }
  } else if (ffn) {
    u32 bias[4];
    for (int t = 0; t < 4; ++t) bias[t] = *(const GAS unsigned short *)(gb1 + (128 * ct + 32 * t + (lane & 31)) * 2);
    f32x16 acc[2][4]; u32x4 stat[2];
    const u32 vostat = WS_STAT + (256 * rga + 64 * w + (lane & 31)) * 16;
    gemmA_compute<false>(L, w, lane, acc, [&]() { for (int r = 0; r < 2; ++r) stat[r] = ld128_sc1(rws, vostat + 512 * r, 0); } STAMP_PASS);
    RowStat st[2];
    for (int r = 0; r < 2; ++r) st[r] = row_stats(stat_settle(stat[r], rws, vostat + 512 * r, E), h);
    BAR();
    STAMP(3);
    for (int t = 0; t < 4; ++t) {
      const u32x4 wa = waug(cs_read(L, t, lane), bias[t] | (bias[t] << 16));
      for (int r = 0; r < 2; ++r) {
        f32x16 a = mfma(wa, st[r].xaug, acc[r][t]);
        for (int i = 0; i < 16; i += 2) { f32x2 v = {fmaxf(a[i], 0.f), fmaxf(a[i + 1], 0.f)}; v *= v; a[i] = v.x; a[i + 1] = v.y; }
        const u32 so = WS_H + ((8 * rga + 2 * w + r) * 112 + 8 * ct + 2 * t) * 1024;
        st128_sc1(pack8(a, 0), rws, lane * 16, so);
        st128_sc1(pack8(a, 1), rws, lane * 16, so + 1024);
      }
    }
    STAMP(4);
    vmwait<0>();
    if (lane < 2) st_flag(gws, WS_HF + ((8 * rga + 2 * w + lane) * 14 + ct) * 64, E);
    STAMP(5);
  } else {
    BAR();
  }

  {
    const int rh = w & 1, kg = w >> 1;
    const u32 rb = 8 * xcd + 2 * rt + rh;
    const u32 row = 32 * rb + (lane & 31), col0 = 64 * cc + 32 * kg;
    u32x2 xres[4], b2v[4];
    for (int g = 0; g < 4; ++g) {
      xres[g] = *(const GAS u32x2 *)(gx + row * XROW + (col0 + 8 * g + 4 * h) * 2);
      b2v[g] = *(const GAS u32x2 *)(gb2 + (col0 + 8 * g + 4 * h) * 2);
    }
    const u32x4 sg0 = ld128_sc1(rws, WS_STAT + row * 16, 0);
    poll_all(gws, WS_HF + (rb * 14 + 7 * kg + ((lane & 7) < 7 ? (lane & 7) : 0)) * 64, E);
    STAMP(10);
    constexpr int RD = RING;
    const u32 soh = WS_H + (rb * 112 + 56 * kg) * 1024, soo = WS_O + (rb * 32 + 16 * kg) * 1024;
    u32x4 ring[RD];
    for (int i = 0; i < RD; ++i) ring[i] = ld128_sc1(rws, lane * 16, soh + i * 1024);
    __builtin_amdgcn_sched_barrier(0);
    const float sig = asf(stat_settle(sg0, rws, WS_STAT + row * 16, E)[2]);
    const float rs1 = __builtin_amdgcn_rcpf(sig), rs2 = rs1 * rs1;
    f32x2 bx[8], xx[8];
    for (int g = 0; g < 4; ++g) for (int e = 0; e < 2; ++e) {
      bx[2 * g + e] = f32x2{bf2f(b2v[g][e]), bfhi(b2v[g][e])}; xx[2 * g + e] = f32x2{bf2f(xres[g][e]), bfhi(xres[g][e])};
      asm volatile("" : "+v"(bx[2 * g + e]), "+v"(xx[2 * g + e]));
    }
    const int blk = (lane >> 4) & 1, q = (lane & 15) >> 2, p = lane & 3;
    u32 bb0[2], bb[2];
    for (int c2 = 0; c2 < 2; ++c2) bb0[c2] = (u32)(size_t)L + kg * 8192 + (4 * h + q) * 128 + ((c2 ^ (q >> 1)) * 64) + 32 * blk + 8 * p;
    f32x16 af[2] = {zero16(), zero16()}, aa[2] = {zero16(), zero16()};
    u32x4 bf[2][2];
    sfor<0, 72>([&](auto I) {
      constexpr int i = I;
      if constexpr ((i & 3) == 0) asm volatile("s_barrier" ::: "memory");
      if constexpr (i == 0) STAMP(11);
      if constexpr (i == 40) STAMP(14);
      if constexpr (i == 0) {
        for (int c2 = 0; c2 < 2; ++c2) { bb[c2] = bb0[c2] + 4 * L_SLOT; asm volatile("" : "+v"(bb[c2])); bf[0][c2] = trfrag((lchar *)(size_t)bb[c2], 1024); }
      }
      const u32x4 a = ring[i % RD];
      if constexpr (i + 1 < 72) {
        constexpr int n = i + 1;
        if constexpr ((n & 3) == 0) for (int c2 = 0; c2 < 2; ++c2) { bb[c2] = bb0[c2] + (((n >> 2) + 4) % NSLOT) * L_SLOT; asm volatile("" : "+v"(bb[c2])); }
        for (int c2 = 0; c2 < 2; ++c2) bf[n & 1][c2] = trfrag((lchar *)(size_t)bb[c2] + (n & 3) * 2048, 1024);
      }
      if constexpr (i < 56) { af[0] = mfma(bf[i & 1][0], a, af[0]); af[1] = mfma(bf[i & 1][1], a, af[1]); }
      else { aa[0] = mfma(bf[i & 1][0], a, aa[0]); aa[1] = mfma(bf[i & 1][1], a, aa[1]); }
      if constexpr (i + RD < 56) ring[i % RD] = ld128_sc1(rws, lane * 16, soh + (i + RD) * 1024);
      else if constexpr (i + RD < 72) ring[i % RD] = ld128_sc1(rws, lane * 16, soo + (i + RD - 56) * 1024);
      __builtin_amdgcn_sched_barrier(0);
    });
    STAMP(12);
    lchar *X = L + 4 * L_SLOT;
    {
      const f32x16 &sf = kg ? af[0] : af[1], &sa = kg ? aa[0] : aa[1];
      for (int g = 0; g < 4; ++g) {
        *(LAS f32x4 *)(X + w * 8192 + g * 1024 + lane * 16) = f32x4{sf[4 * g], sf[4 * g + 1], sf[4 * g + 2], sf[4 * g + 3]};
        *(LAS f32x4 *)(X + w * 8192 + 4096 + g * 1024 + lane * 16) = f32x4{sa[4 * g], sa[4 * g + 1], sa[4 * g + 2], sa[4 * g + 3]};
      }
    }
    BAR();
    const f32x16 &yf = kg ? af[1] : af[0], &ya = kg ? aa[1] : aa[0];
    u32x2 o2[4];
    for (int g = 0; g < 4; ++g) {
      const f32x4 pf = *(LAS f32x4 *)(X + (w ^ 2) * 8192 + g * 1024 + lane * 16), pa = *(LAS f32x4 *)(X + (w ^ 2) * 8192 + 4096 + g * 1024 + lane * 16);
      for (int e = 0; e < 2; ++e) {
        const int i = 4 * g + 2 * e;
        const f32x2 y = (f32x2{yf[i], yf[i + 1]} + f32x2{pf[2 * e], pf[2 * e + 1]}) * rs2 + bx[2 * g + e];
        const f32x2 t = y * -LOG2E;
        const f32x2 d = f32x2{__builtin_amdgcn_exp2f(t.x), __builtin_amdgcn_exp2f(t.y)} + 1.0f;
        const f32x2 sg = y * f32x2{__builtin_amdgcn_rcpf(d.x), __builtin_amdgcn_rcpf(d.y)};
        const f32x2 r = (f32x2{ya[i], ya[i + 1]} + f32x2{pa[2 * e], pa[2 * e + 1]}) * sg + xx[2 * g + e];
        o2[g][e] = pk2(r.x, r.y);
      }
    }
    for (int g = 0; g < 4; g += 2) {
      auto rx0 = __builtin_amdgcn_permlane32_swap(o2[g].x, o2[g + 1].x, false, false);
      auto ry0 = __builtin_amdgcn_permlane32_swap(o2[g].y, o2[g + 1].y, false, false);
      *(GAS u32x4 *)(gout + row * XROW + (col0 + 8 * (g + h)) * 2) = u32x4{rx0[0], ry0[0], rx0[1], ry0[1]};
    }
    STAMP(13);
    STAMP(15);
    STAMP_OUT();
  }
}

extern "C" void kernel_launch(void *const *d_in, const int *in_sizes, int n_in, void *d_out, int out_size, void *d_ws, size_t ws_size, hipStream_t stream) {
  fused_hedgehog_ffn<<<256, 512, 0, stream>>>((const char *)d_in[0], (const char *)d_in[1], (const char *)d_in[2], (const char *)d_in[3], (const char *)d_in[5],
                                              (const char *)d_in[6], (const char *)d_in[7], (const char *)d_in[8], (char *)d_out, (char *)d_ws);
}
```

```cpp
#include <hip/hip_runtime.h>
#include <hip/hip_bf16.h>
#include <stdint.h>
#include <type_traits>

typedef unsigned u32;
typedef u32 u32x4 __attribute__((ext_vector_type(4)));
typedef u32 u32x2 __attribute__((ext_vector_type(2)));
typedef float f32x16 __attribute__((ext_vector_type(16)));
typedef float f32x4 __attribute__((ext_vector_type(4)));
typedef float f32x2 __attribute__((ext_vector_type(2)));
typedef short s16x8 __attribute__((ext_vector_type(8)));
typedef short s16x4 __attribute__((ext_vector_type(4)));
typedef __bf16 bf16x2 __attribute__((ext_vector_type(2)));
typedef __bf16 bf16x8 __attribute__((ext_vector_type(8)));
#define LAS __attribute__((address_space(3)))
#define GAS __attribute__((address_space(1)))
#define DI __device__ __forceinline__
typedef __amdgpu_buffer_rsrc_t rsrc_t;
typedef LAS char lchar;

namespace {
constexpr int DM = 448, DF = 1792;
constexpr u32 XROW = DM * 2;
constexpr u32 QKVROW = 3 * DM * 2;
constexpr u32 W1ROW = DF * 2;
constexpr u32 WS_EPOCH = 0;
constexpr u32 WS_STAT = 4096;
constexpr u32 WS_KF = 65536;
constexpr u32 WS_HF = 131072;
constexpr u32 WS_OF = 196608;
constexpr u32 WS_KVP = 262144;
constexpr u32 WS_H = WS_KVP + (1u << 20);
constexpr u32 WS_O = WS_H + 64 * 112 * 1024;
constexpr u32 WS_END = WS_O + 64 * 32 * 1024;
constexpr u32 L_SLOT = 16384, NSLOT = 9;
constexpr u32 A_SLOT = 49152, A_WOFF = 32768;
constexpr u32 L_MISC = NSLOT * L_SLOT;
constexpr u32 L_WPHI = L_MISC;
constexpr u32 L_ZERO = L_MISC + 2048;
constexpr u32 L_E = L_MISC + 2112;
constexpr u32 L_CSP = L_MISC + 2304;
constexpr u32 L_TOK = L_CSP + 512;
constexpr u32 L_KVF = L_TOK + 1024;
constexpr u32 L_END = L_KVF + 4096;
constexpr u32 LDS_BYTES = 163840;
static_assert(L_END <= LDS_BYTES, "lds");
constexpr int SPIN_CAP = 1 << 16;
constexpr float LOG2E = 1.4426950408889634f;
#define CS_DOT2 0
#define SGB 1
#define SPREAD 0
#define PIN_C 0
#define RING 8

#define STAMP(i) do {} while (0)
#define STAMP_DECL do {} while (0)
#define STAMP_OUT() do {} while (0)
#define STAMP_ARG
#define STAMP_PASS
template <int B, int E, class F> DI void sfor(F &&f) {
  if constexpr (B < E) { f(std::integral_constant<int, B>{}); sfor<B + 1, E>(f); }
}
DI u32 rfl(u32 v) { return __builtin_amdgcn_readfirstlane(v); }
DI f32x16 mfma(u32x4 a, u32x4 b, f32x16 c) {
  return __builtin_amdgcn_mfma_f32_32x32x16_bf16(__builtin_bit_cast(bf16x8, a), __builtin_bit_cast(bf16x8, b), c, 0, 0, 0);
}
DI u32 pk2(float lo, float hi) { f32x2 p = {lo, hi}; return __builtin_bit_cast(u32, __builtin_convertvector(p, bf16x2)); }
DI float dot2(u32 a, u32 b, float c) { return __builtin_amdgcn_fdot2_f32_bf16(__builtin_bit_cast(bf16x2, a), __builtin_bit_cast(bf16x2, b), c, false); }
DI float bf2f(u32 lo16) { return __builtin_bit_cast(float, lo16 << 16); }
DI float bfhi(u32 v) { return __builtin_bit_cast(float, v & 0xffff0000u); }
DI float asf(u32 v) { return __builtin_bit_cast(float, v); }
DI f32x2 both_halves(float v) {
  const u32 a = __builtin_bit_cast(u32, v);
  auto r = __builtin_amdgcn_permlane32_swap(a, a, false, false);
  return f32x2{asf(r[0]), asf(r[1])};
}
DI float half_lo(float v) { return both_halves(v).x; }
DI u32x4 pack8(const f32x16 &x, int s) { return u32x4{pk2(x[8 * s], x[8 * s + 1]), pk2(x[8 * s + 2], x[8 * s + 3]), pk2(x[8 * s + 4], x[8 * s + 5]), pk2(x[8 * s + 6], x[8 * s + 7])}; }
DI u32x2 trd(lchar *p) {
  typedef short v4i16 __attribute__((ext_vector_type(4)));
  return __builtin_bit_cast(u32x2, __builtin_amdgcn_ds_read_tr16_b64_v4i16((LAS v4i16 *)p));
}
DI u32x4 trfrag(lchar *p, int tstride) { u32x2 a = trd(p), b = trd(p + tstride); return u32x4{a.x, a.y, b.x, b.y}; }
DI float relu(float v) { return __builtin_amdgcn_fmed3f(v, 0.f, __builtin_inff()); }
DI f32x16 zero16() { f32x16 z; for (int i = 0; i < 16; ++i) z[i] = 0.f; return z; }
#define BAR() asm volatile("s_waitcnt lgkmcnt(0)\n\ts_barrier" ::: "memory")
template <int N> DI void vmwait() { asm volatile("s_waitcnt vmcnt(%0)" ::"i"(N) : "memory"); }
DI void dma16(rsrc_t r, u32 voff, u32 soff, u32 ldsaddr) {
  asm volatile("s_mov_b32 m0, %0\n\ts_nop 0\n\tbuffer_load_dwordx4 %1, %2, %3 offen lds" ::"s"(ldsaddr), "v"(voff), "s"(r), "s"(soff) : "memory", "m0");
}
template <int OFF> DI void ald128(u32x4 &d, rsrc_t r, u32 voff, u32 soff) {
  asm volatile("buffer_load_dwordx4 %0, %1, %2, %3 offen offset:%4" : "=v"(d) : "v"(voff), "s"(r), "s"(soff), "i"(OFF) : "memory");
}
DI u32x4 ld128_sc1(rsrc_t r, u32 voff, u32 soff) { return __builtin_amdgcn_raw_buffer_load_b128(r, voff, soff, 16); }
DI void st128_sc1(u32x4 v, rsrc_t r, u32 voff, u32 soff) {
  __builtin_amdgcn_raw_buffer_store_b128(v, r, voff, soff, 16);
  asm volatile("s_nop 1" ::"v"(v) : "memory");
}
typedef GAS u32 gu32;
DI u32 ld_flag(const char *ws, u32 off) { return __hip_atomic_load((gu32 *)(ws + off), __ATOMIC_RELAXED, __HIP_MEMORY_SCOPE_AGENT); }
DI void st_flag(char *ws, u32 off, u32 v) { __hip_atomic_store((gu32 *)(ws + off), v, __ATOMIC_RELAXED, __HIP_MEMORY_SCOPE_AGENT); }
DI void poll_all(const char *ws, u32 off, u32 E) {
#pragma unroll 1
  for (int spin = 0; spin < SPIN_CAP; ++spin) {
    if (__all(ld_flag(ws, off) == E)) break;
    __builtin_amdgcn_s_sleep(1);
  }
  __builtin_amdgcn_fence(__ATOMIC_ACQUIRE, "wavefront");
}

template <u32 LI, u32 SI> DI void dmai(rsrc_t r, u32 voff, u32 sob, u32 ldsb) {
  u32 t;
  asm volatile("s_add_u32 m0, %2, %5\n\ts_add_u32 %0, %1, %6\n\tbuffer_load_dwordx4 %3, %4, %0 offen lds"
               : "=&s"(t) : "s"(sob), "s"(ldsb), "v"(voff), "s"(r), "i"(LI), "i"(SI) : "memory", "m0", "scc");
}
template <int CTRL, int RMASK> DI float dpp_add(float v) {
  return v + asf(__builtin_amdgcn_update_dpp(0u, __builtin_bit_cast(u32, v), CTRL, RMASK, 0xf, true));
}
DI float wave_sum63(float v) {
  v = dpp_add<0x111, 0xf>(v); v = dpp_add<0x112, 0xf>(v); v = dpp_add<0x114, 0xf>(v); v = dpp_add<0x118, 0xf>(v);
  v = dpp_add<0x142, 0xa>(v); v = dpp_add<0x143, 0xc>(v);
  return v;
}
DI void st64_sc1(char *ws, u32 off, u32 lo, u32 hi) {
  __hip_atomic_store((GAS unsigned long long *)(ws + off), (unsigned long long)lo | ((unsigned long long)hi << 32), __ATOMIC_RELAXED, __HIP_MEMORY_SCOPE_AGENT);
}
DI u32 early_stats(char *ws, rsrc_t rx, int row0, int lane) {
  const u32 ev = ld_flag(ws, WS_EPOCH);
  const u32 vo = lane < 56 ? (u32)lane * 16 : 0x7ffffff0u;
  const u32x4 xa = __builtin_amdgcn_raw_buffer_load_b128(rx, vo, row0 * XROW, 0), xb = __builtin_amdgcn_raw_buffer_load_b128(rx, vo, (row0 + 1) * XROW, 0);
  const u32 E = rfl(ev) + 1, ONES = 0x3F803F80u;
  float s[2], q[2];
  for (int r = 0; r < 2; ++r) {
    const u32x4 &x = r ? xb : xa;
    s[r] = dot2(x[0], ONES, 0.f); q[r] = dot2(x[0], x[0], 0.f);
    for (int d = 1; d < 4; ++d) { s[r] = dot2(x[d], ONES, s[r]); q[r] = dot2(x[d], x[d], q[r]); }
    s[r] = wave_sum63(s[r]); q[r] = wave_sum63(q[r]);
    const float mean = s[r] * (1.0f / DM);
    const float sig = __builtin_sqrtf(fmaxf(q[r] * (1.0f / DM) - mean * mean, 0.f) + 1e-5f);
    if (lane == 63) { st64_sc1(ws, WS_STAT + (row0 + r) * 16, __builtin_bit_cast(u32, mean), E); st64_sc1(ws, WS_STAT + (row0 + r) * 16 + 8, __builtin_bit_cast(u32, sig), E); }
  }
  return E;
}
DI u32x4 stat_settle(u32x4 g, rsrc_t rws, u32 vostat, u32 E) {
#pragma unroll 1
  for (int spin = 0; spin < SPIN_CAP && !__all(g[1] == E && g[3] == E); ++spin) { __builtin_amdgcn_s_sleep(1); g = ld128_sc1(rws, vostat, 0); }
  return g;
}
struct RowStat { float rstd; u32x4 xaug; };
DI RowStat row_stats(u32x4 g, int h) {
  const float m = -asf(g[0]), sig = asf(g[2]);
  RowStat r; r.rstd = __builtin_amdgcn_rcpf(sig);
  const float ml = m - bfhi(pk2(m, m)), sl = sig - bfhi(pk2(sig, sig));
  const u32 mm = pk2(m, ml), ss = pk2(sig, sl);
  r.xaug = h ? u32x4{0, 0, 0, 0} : u32x4{mm, mm, ss, 0};
  return r;
}
DI u32x4 waug(float cs, u32 bb) {
  const u32 hh = pk2(cs, cs); const float cl = cs - bfhi(hh);
  return u32x4{hh, pk2(cl, cl), bb, 0};
}
DI float cs_read(lchar *L, int t, int lane) { return *(LAS float *)(L + L_CSP + (t * 32 + (lane & 31)) * 4); }

template <bool ATT, class Mid>
DI void gemmA_compute(lchar *L, int w, int lane, f32x16 (&acc)[2][ATT ? 3 : 4], Mid &&mid STAMP_ARG) {
  constexpr int T = ATT ? 3 : 4, ROWB = ATT ? 64 : 256;
  const int h = lane >> 5, blk = (lane >> 4) & 1, q = (lane & 15) >> 2, p = lane & 3;
  const u32 lb = (u32)(size_t)L;
  u32 wb0[T], xb0[4], wb[T], xb[4], cb0, cb;
  for (int t = 0; t < T; ++t) wb0[t] = lb + A_WOFF + (ATT ? t * 4096 + (8 * h + q) * 64 : (8 * h + q) * 256 + 64 * (t ^ q)) + 32 * blk + 8 * p;
  for (int u = 0; u < 4; ++u) xb0[u] = lb + w * 8192 + (lane & 31) * 128 + 16 * ((2 * u + h) ^ ((lane >> 1) & 7));
  const int tw = w < T ? w : T - 1;
  cb0 = lb + A_WOFF + (ATT ? tw * 4096 + (8 * h + q) * 64 : (8 * h + q) * 256 + 64 * (tw ^ q)) + 32 * blk + 8 * p;
  f32x2 ca = {0.f, 0.f};
  for (int r = 0; r < 2; ++r) for (int t = 0; t < T; ++t) acc[r][t] = zero16();
  u32x4 xf[2][2], wf[2][T], cf[2];
  sfor<0, 29>([&](auto I) {
    constexpr int i = I, c = i >> 2, u = i & 3;
    if constexpr (u == 0 && i < 28) {
      BAR();
      if constexpr (c == 0) STAMP(1);
      constexpr u32 so = (c % 3) * A_SLOT;
      for (int t = 0; t < T; ++t) { wb[t] = wb0[t] + so; asm volatile("" : "+v"(wb[t])); }
      for (int j = 0; j < 4; ++j) { xb[j] = xb0[j] + so; asm volatile("" : "+v"(xb[j])); }
      cb = cb0 + so; asm volatile("" : "+v"(cb));
    }
    if constexpr (i < 28) {
      cf[i & 1] = trfrag((lchar *)(size_t)cb + 16 * u * ROWB, 4 * ROWB);
      for (int r = 0; r < 2; ++r) xf[i & 1][r] = *(LAS u32x4 *)((lchar *)(size_t)xb[u] + r * 4096);
      for (int t = 0; t < T; ++t) wf[i & 1][t] = trfrag((lchar *)(size_t)wb[t] + 16 * u * ROWB, 4 * ROWB);
    }
    if constexpr (i == 20) mid();
    if constexpr (i > 0) {
      constexpr int j = (i - 1) & 1;
      for (int t = 0; t < T; ++t)
        for (int r = 0; r < 2; ++r) acc[r][t] = (ATT && t == 1) ? mfma(xf[j][r], wf[j][t], acc[r][t]) : mfma(wf[j][t], xf[j][r], acc[r][t]);
      for (int d = 0; d < 4; ++d) ca += f32x2{bf2f(cf[j][d]), bfhi(cf[j][d])};
      asm volatile("" : "+v"(ca));
      for (int m = 0; m < 2 * T; ++m) { __builtin_amdgcn_sched_group_barrier(0x8, 1, 0); __builtin_amdgcn_sched_group_barrier(0x100, 2, 0); __builtin_amdgcn_sched_group_barrier(0x2, 2, 0); }
    }
    __builtin_amdgcn_sched_barrier(0);
  });
  { const f32x2 a = both_halves(ca.x + ca.y); if (lane < 32 && w < T) *(LAS float *)(L + L_CSP + (w * 32 + lane) * 4) = a.x + a.y; }
  STAMP(2);
}
struct StageCtx { rsrc_t rw2, rwo; u32 vob, so2, soo, ld; };
template <int ST> DI void stage_dma(const StageCtx &c) {
  sfor<0, 4>([&](auto I) {
    constexpr u32 i = I, dst = ((ST + 4) % NSLOT) * L_SLOT + i * 1024;
    dmai<dst, (64 * ST + 8 * i) * XROW>(c.rw2, c.vob, c.so2, c.ld);
  });
}
template <int ST> DI void stage_load(const StageCtx &c, u32x4 (&br)[36]) {
  sfor<0, 4>([&](auto I) {
    constexpr u32 i = I;
    if constexpr (ST < 14) br[4 * (ST - 9) + i] = __builtin_amdgcn_raw_buffer_load_b128(c.rw2, c.vob, c.so2 + (64 * ST + 8 * i) * XROW, 0);
    else br[4 * (ST - 9) + i] = __builtin_amdgcn_raw_buffer_load_b128(c.rwo, c.vob, c.soo + (56 * (ST - 14) + (i == 3 ? 20 : 8 * i)) * XROW, 0);
  });
}
template <bool ATT>
DI void gemmA_loader(lchar *L, rsrc_t rx, rsrc_t rw, u32 sox, u32 sow, int lw, int lane, const StageCtx &sc, u32x4 (&br)[36] STAMP_ARG) {
  constexpr int NW = ATT ? 3 : 4, N = 8 + NW;
  const u32 lb = (u32)(size_t)L;
  const u32 vox0 = (lane >> 3) * XROW + 16 * ((lane & 7) ^ ((lane >> 4) & 3)), vox1 = vox0 ^ 64;
  const u32 ldx = rfl(lb + lw * 8192);
  const u32 vow = ATT ? (lane >> 2) * QKVROW + ((lane & 3) == 3 ? 40 : 16 * (lane & 3)) : (lane >> 4) * W1ROW + 16 * ((lane & 15) ^ ((lane >> 4) << 2));
  const u32 swb = rfl(sow + 16 * lw * (ATT ? QKVROW : W1ROW)), ldw = rfl(lb + A_WOFF + lw * (ATT ? 1024 : 4096));
  auto issue = [&](auto C) {
    constexpr u32 c = C, so = (c % 3) * A_SLOT;
    sfor<0, NW>([&](auto I) {
      constexpr u32 i = I;
      if constexpr (ATT) dmai<so + i * 4096, 64 * c * QKVROW + (i == 0 ? 896 : i == 1 ? 1792 : 0)>(rw, vow, swb, ldw);
      else dmai<so + i * 1024, (64 * c + 4 * i) * W1ROW>(rw, vow, swb, ldw);
    });
    sfor<0, 8>([&](auto I) { constexpr u32 i = I; dmai<so + i * 1024, 8 * i * XROW + 128 * c>(rx, (i & 1) ? vox1 : vox0, sox, ldx); });
  };
  issue(std::integral_constant<int, 0>{});
  issue(std::integral_constant<int, 1>{});
  sfor<0, 7>([&](auto C) {
    constexpr int c = C;
    vmwait<(c == 6 ? 12 : N)>();
    asm volatile("s_barrier" ::: "memory");
    if constexpr (c == 0) STAMP(1);
    if constexpr (c + 2 < 7) issue(std::integral_constant<int, c + 2>{});
    else { stage_load<9 + 3 * (c - 5)>(sc, br); stage_load<10 + 3 * (c - 5)>(sc, br); stage_load<11 + 3 * (c - 5)>(sc, br); }
  });
  STAMP(2);
}

DI void feature_map(const f32x16 &P, float rstd, int h, u32x4 (&pf)[2][2]) {
  const float c = rstd * LOG2E;
  float mx = fmaxf(fmaxf(P[0], P[1]), P[2]), mn = fminf(fminf(P[0], P[1]), P[2]);
  for (int i = 3; i < 15; i += 2) { mx = fmaxf(fmaxf(mx, P[i]), P[i + 1]); mn = fminf(fminf(mn, P[i]), P[i + 1]); }
  mx = fmaxf(mx, P[15]); mn = fminf(mn, P[15]);
  { f32x2 a = both_halves(mx), b = both_halves(mn); mx = fmaxf(a.x, a.y); mn = fminf(b.x, b.y); }
  const float mxc = -mx * c, mnc = mn * c;
  f32x16 ep, en;
  for (int i = 0; i < 16; i += 2) {
    const f32x2 p2 = {P[i], P[i + 1]}, ap = p2 * c + mxc, an = mnc - p2 * c;
    ep[i] = __builtin_amdgcn_exp2f(ap.x); ep[i + 1] = __builtin_amdgcn_exp2f(ap.y);
    en[i] = __builtin_amdgcn_exp2f(an.x); en[i + 1] = __builtin_amdgcn_exp2f(an.y);
  }
  f32x2 sp2 = {ep[0], ep[1]}, sn2 = {en[0], en[1]};
  for (int i = 2; i < 12; i += 2) { sp2 += f32x2{ep[i], ep[i + 1]}; sn2 += f32x2{en[i], en[i + 1]}; }
  const float keep = h ? 0.f : 1.f;
  sp2 += (f32x2{ep[12], ep[13]} + f32x2{ep[14], ep[15]}) * keep;
  sn2 += (f32x2{en[12], en[13]} + f32x2{en[14], en[15]}) * keep;
  const f32x2 a = both_halves(sp2.x + sp2.y), b = both_halves(sn2.x + sn2.y);
  const float rp = __builtin_amdgcn_rcpf(a.x + a.y), rn = __builtin_amdgcn_rcpf(b.x + b.y);
  for (int i = 0; i < 16; i += 2) {
    const f32x2 u = f32x2{ep[i], ep[i + 1]} * rp, v = f32x2{en[i], en[i + 1]} * rn;
    ep[i] = u.x; ep[i + 1] = u.y; en[i] = v.x; en[i + 1] = v.y;
  }
  pf[0][0] = pack8(ep, 0); pf[0][1] = pack8(ep, 1); pf[1][0] = pack8(en, 0); pf[1][1] = pack8(en, 1);
}

constexpr int refill_end(int s) { return s <= 0 ? 0 : (12 * s + 4) / 5 > 36 ? 36 : (12 * s + 4) / 5; }
constexpr bool refill_ok() { for (int s = 1; s <= 17; ++s) for (int p = refill_end(s - 1); p < refill_end(s); ++p) if (s < 1 + p / 4 || s > 7 + p / 4) return false; return refill_end(15) == 36; }
static_assert(refill_ok(), "refill schedule");
}

__global__ void __launch_bounds__(512, 2) fused_hedgehog_ffn(const char *__restrict__ gx, const char *__restrict__ gwqkv, const char *__restrict__ gwphi, const char *__restrict__ gwo,
                                                              const char *__restrict__ gw1, const char *__restrict__ gb1, const char *__restrict__ gw2, const char *__restrict__ gb2,
                                                              char *__restrict__ gout, char *__restrict__ gws) {
  __shared__ __attribute__((aligned(1024))) char smem[LDS_BYTES];
  lchar *L = (lchar *)smem;
  asm volatile("" ::"s"(gx), "s"(gwqkv), "s"(gwphi), "s"(gwo), "s"(gw1), "s"(gb1), "s"(gw2), "s"(gb2), "s"(gout), "s"(gws));
  const int tid = threadIdx.x, lane = tid & 63, w = rfl(tid >> 6);
  const int b = blockIdx.x, xcd = b & 7, slot = b >> 3;
  const int h = lane >> 5;
  STAMP_DECL; STAMP(0);
  const rsrc_t rx = __builtin_amdgcn_make_buffer_rsrc((void *)gx, 0, 2048 * XROW, 0x00020000);
  const rsrc_t rws = __builtin_amdgcn_make_buffer_rsrc((void *)gws, 0, WS_END, 0x00020000);
  const int tc = slot >= 16 ? slot - 16 : slot + 16;
  const bool has_tile = tc < 28;
  const u32 rt = tc / 7, cc = tc % 7;
  const bool att = slot < 16, ffn = !att && slot < 30;
  const int head = 8 * (xcd & 1) + (slot & 7);
  const int jf = slot - 16, ct = 7 * (xcd & 1) + (jf >= 7 ? jf - 7 : jf);
  const int rga = 2 * (xcd >> 1) + (att ? (slot >> 3) : (jf >= 7));

  if (w >= 4) {
    const int lw = w - 4;
    StageCtx sc;
    sc.rw2 = __builtin_amdgcn_make_buffer_rsrc((void *)gw2, 0, DF * XROW, 0x00020000);
    sc.rwo = __builtin_amdgcn_make_buffer_rsrc((void *)gwo, 0, DM * XROW, 0x00020000);
    sc.vob = (lane >> 3) * XROW + 16 * ((lane & 7) ^ (((lane >> 4) & 1) << 2));
    sc.so2 = rfl((896 * (lw >> 1) + 32 * (lw & 1)) * XROW + 128 * cc);
    sc.soo = rfl(28 * (8 * (lw >> 1) + (lw & 1)) * XROW + 128 * cc);
    sc.ld = rfl((u32)(size_t)L + (lw >> 1) * 8192 + (lw & 1) * 4096);
    u32 E;
    u32x4 br[36];
    if (att) {
      const rsrc_t rw = __builtin_amdgcn_make_buffer_rsrc((void *)gwqkv, 0, DM * QKVROW, 0x00020000);
      if (lw < 2) {
        const rsrc_t rp = __builtin_amdgcn_make_buffer_rsrc((void *)gwphi, 0, 16 * 28 * 28 * 2, 0x00020000);
        dma16(rp, lane * 16 + lw * 1024, head * 1568, (u32)(size_t)L + L_WPHI + lw * 1024);
      }
      gemmA_loader<true>(L, rx, rw, rfl((256 * rga + 64 * lw) * XROW), rfl(56 * head), lw, lane, sc, br STAMP_PASS);
      BAR();
      E = rfl(*(LAS u32 *)(L + L_E));
      if (has_tile) sfor<0, 5>([&](auto S) { stage_dma<S>(sc); });
      sfor<15, 18>([&](auto S) { stage_load<S>(sc, br); });
      BAR();
      STAMP(4);
      for (int t = 0; t < 2; ++t) {
        f32x4 s = *(LAS f32x4 *)(L + (t * 4 + lw) * 1024 + lane * 16);
        for (int ww = 1; ww < 4; ++ww) s += *(LAS f32x4 *)(L + ww * 8192 + (t * 4 + lw) * 1024 + lane * 16);
        st128_sc1(__builtin_bit_cast(u32x4, s), rws, lane * 16, WS_KVP + (head * 8 + rga) * 8192 + (t * 4 + lw) * 1024);
      }
      vmwait<0>();
      if (lane == 0) st_flag(gws, WS_KF + ((head * 8 + rga) * 4 + lw) * 64, E);
      STAMP(5);
      poll_all(gws, WS_KF + ((head * 8 + (lane & 7)) * 4 + lw) * 64, E);
      STAMP(7);
      u32x4 part[2][8];
      for (int t = 0; t < 2; ++t) for (int r = 0; r < 8; ++r) part[t][r] = ld128_sc1(rws, lane * 16, WS_KVP + (head * 8 + r) * 8192 + (t * 4 + lw) * 1024);
      for (int t = 0; t < 2; ++t) {
        f32x4 s = __builtin_bit_cast(f32x4, part[t][0]);
        for (int r = 1; r < 8; ++r) s += __builtin_bit_cast(f32x4, part[t][r]);
        u32x2 pk = {pk2(s.x, s.y), pk2(s.z, s.w)};
        if (lw == 3 && h) pk = u32x2{0, 0};
        *(LAS u32x2 *)(L + L_KVF + ((t * 2 + (lw >> 1)) * 64 + lane) * 16 + (lw & 1) * 8) = pk;
      }
      BAR();
      STAMP(8);
      if (!has_tile) {
        if (b == 8 * 12 && lw == 0) {
#pragma unroll 1
          for (int spin = 0; spin < SPIN_CAP; ++spin) {
            bool ok = true;
            for (int i = 0; i < 32; ++i) ok &= ld_flag(gws, WS_STAT + (64 * i + lane) * 16 + 4) == E;
            if (__all(ok)) break;
            __builtin_amdgcn_s_sleep(8);
          }
          if (lane == 0) st_flag(gws, WS_EPOCH, E);
        }
        STAMP_OUT();
        return;
      }
      sfor<5, 9>([&](auto S) { stage_dma<S>(sc); });
    } else if (ffn) {
      const rsrc_t rw = __builtin_amdgcn_make_buffer_rsrc((void *)gw1, 0, DM * W1ROW, 0x00020000);
      gemmA_loader<false>(L, rx, rw, rfl((256 * rga + 64 * lw) * XROW), rfl(256 * ct), lw, lane, sc, br STAMP_PASS);
      BAR();
      E = rfl(*(LAS u32 *)(L + L_E));
      sfor<0, 9>([&](auto S) { stage_dma<S>(sc); });
      sfor<15, 18>([&](auto S) { stage_load<S>(sc, br); });
    } else {
      sfor<0, 9>([&](auto S) { stage_dma<S>(sc); });
      sfor<9, 18>([&](auto S) { stage_load<S>(sc, br); });
      BAR();
      E = rfl(*(LAS u32 *)(L + L_E));
    }
    poll_all(gws, WS_HF + ((8 * xcd + 2 * rt) * 14 + (lane < 28 ? lane : 0)) * 64, E);
    vmwait<0>();
    STAMP(10);
    constexpr int SO = (56 - RING) / 4;
    u32 ofl = 0;
    const u32 ofoff = WS_OF + ((8 * xcd + 2 * rt + ((lane >> 4) & 1)) * 16 + (lane & 15)) * 64;
    const u32 ldv = sc.ld + lane * 16;
    sfor<0, 18>([&](auto S) {
      constexpr int s = S;
      if constexpr (s == SO) {
        vmwait<0>();
        asm volatile("" : "+v"(ofl));
        if (!__all(ofl == E)) poll_all(gws, ofoff, E);
      }
      BAR();
      sfor<refill_end(s - 1), refill_end(s)>([&](auto Pc) {
        constexpr int pc = Pc;
        *(LAS u32x4 *)((lchar *)(size_t)ldv + ((9 + pc / 4 + 4) % NSLOT) * L_SLOT + (pc & 3) * 1024) = br[pc];
      });
      if constexpr (s == SO - 4) asm volatile("global_load_dword %0, %1, off sc1" : "=v"(ofl) : "v"(gws + ofoff) : "memory");
    });
    STAMP(12);
    asm volatile("s_barrier" ::: "memory");
    STAMP_OUT();
    return;
  }

  const u32 E = early_stats(gws, rx, 8 * b + 2 * w, lane);
  if (tid == 0) *(LAS u32 *)(L + L_E) = E;
  const int rh = w & 1, kg = w >> 1;
  const u32 rb = 8 * xcd + 2 * rt + rh;
  const u32 row = 32 * rb + (lane & 31), col0 = 64 * cc + 32 * kg;
  u32x2 xres[4], b2v[4]; u32x4 sg0;
  auto epi_loads = [&]() {
    for (int g = 0; g < 4; ++g) {
      xres[g] = *(const GAS u32x2 *)(gx + row * XROW + (col0 + 8 * g + 4 * h) * 2);
      b2v[g] = *(const GAS u32x2 *)(gb2 + (col0 + 8 * g + 4 * h) * 2);
    }
    sg0 = ld128_sc1(rws, WS_STAT + row * 16, 0);
  };
  if (att) {
    if (tid < 16) *(LAS u32 *)(L + L_ZERO + 4 * tid) = 0;
    f32x16 acc[2][3]; u32x4 stat[2];
    const u32 vostat = WS_STAT + (256 * rga + 64 * w + (lane & 31)) * 16;
    gemmA_compute<true>(L, w, lane, acc, [&]() { for (int r = 0; r < 2; ++r) stat[r] = ld128_sc1(rws, vostat + 512 * r, 0); } STAMP_PASS);
    RowStat st[2];
    for (int r = 0; r < 2; ++r) {
      st[r] = row_stats(stat_settle(stat[r], rws, vostat + 512 * r, E), h);
      *(LAS float *)(L + L_TOK + w * 256 + r * 128 + 4 * (lane & 31)) = st[r].rstd;
    }
    BAR();
    STAMP(3);
    {
      const u32x4 wk = waug(cs_read(L, 0, lane), 0), wv = waug(cs_read(L, 1, lane), 0), wq = waug(cs_read(L, 2, lane), 0);
      for (int r = 0; r < 2; ++r) { acc[r][0] = mfma(wk, st[r].xaug, acc[r][0]); acc[r][1] = mfma(st[r].xaug, wv, acc[r][1]); acc[r][2] = mfma(wq, st[r].xaug, acc[r][2]); }
    }
    u32x4 wp[2];
    {
      const int blk = (lane >> 4) & 1, q = (lane & 15) >> 2, p = lane & 3;
      const u32 col = (blk && p == 3) ? 24 : 16 * blk + 4 * p;
      for (int s = 0; s < 2; ++s) {
        u32x2 r[2];
        for (int t = 0; t < 2; ++t) {
          const int dp = 16 * s + 8 * t + 4 * h + q;
          const u32 a = (dp >= 24 && dp < 28) ? L_ZERO + 8 * p : L_WPHI + (dp >= 28 ? dp - 4 : dp) * 56 + col * 2;
          r[t] = trd(L + a);
        }
        wp[s] = u32x4{r[0].x, r[0].y, r[1].x, r[1].y};
      }
    }
    u32x4 idf[2];
    for (int s = 0; s < 2; ++s)
      for (int d = 0; d < 4; ++d) {
        const int k0 = 16 * s + 8 * (d >> 1) + 4 * h + 2 * (d & 1), n = lane & 31;
        idf[s][d] = (n == k0 ? 0x3F80u : 0u) | (n == k0 + 1 ? 0x3F800000u : 0u);
      }
    f32x16 kvp[2] = {zero16(), zero16()};
    for (int r = 0; r < 2; ++r) {
      f32x16 P = mfma(wp[0], pack8(acc[r][0], 0), zero16());
      P = mfma(wp[1], pack8(acc[r][0], 1), P);
      u32x4 pf[2][2];
      feature_map(P, st[r].rstd, h, pf);
      f32x16 v = acc[r][1];
      for (int g = 0; g < 4; ++g) {
        const f32x4 rs = *(LAS f32x4 *)(L + L_TOK + w * 256 + r * 128 + (8 * g + 4 * h) * 4);
        for (int e = 0; e < 4; ++e) v[4 * g + e] *= rs[e];
      }
      u32x4 vf[2] = {pack8(v, 0), pack8(v, 1)};
      if ((lane & 31) == 24) { vf[0] = u32x4{0x3F803F80u, 0x3F803F80u, 0x3F803F80u, 0x3F803F80u}; vf[1] = vf[0]; }
      for (int t = 0; t < 2; ++t) {
        f32x16 Z = mfma(pf[t][0], idf[0], zero16());
        Z = mfma(pf[t][1], idf[1], Z);
        kvp[t] = mfma(pack8(Z, 0), vf[0], kvp[t]);
        kvp[t] = mfma(pack8(Z, 1), vf[1], kvp[t]);
      }
    }
    for (int t = 0; t < 2; ++t)
      for (int g = 0; g < 4; ++g)
        *(LAS f32x4 *)(L + w * 8192 + (t * 4 + g) * 1024 + lane * 16) = f32x4{kvp[t][4 * g], kvp[t][4 * g + 1], kvp[t][4 * g + 2], kvp[t][4 * g + 3]};
    STAMP(4);
    BAR();
    u32x4 pq[2][2][2];
    for (int r = 0; r < 2; ++r) {
      f32x16 P = mfma(wp[0], pack8(acc[r][2], 0), zero16());
      P = mfma(wp[1], pack8(acc[r][2], 1), P);
      feature_map(P, st[r].rstd, h, pq[r]);
      for (int t = 0; t < 2; ++t) for (int s = 0; s < 2; ++s) asm volatile("" : "+v"(pq[r][t][s]));
    }
    if (has_tile) epi_loads();
    else { for (int g = 0; g < 4; ++g) { xres[g] = u32x2{0, 0}; b2v[g] = u32x2{0, 0}; } sg0 = u32x4{0, 0, 0, 0}; }
    STAMP(6);
    BAR();
    STAMP(8);
    {
      u32x4 kf[2][2];
      for (int t = 0; t < 2; ++t) for (int s = 0; s < 2; ++s) kf[t][s] = *(LAS u32x4 *)(L + L_KVF + ((t * 2 + s) * 64 + lane) * 16);
      for (int r = 0; r < 2; ++r) {
        f32x16 num = zero16();
        for (int t = 0; t < 2; ++t) for (int s = 0; s < 2; ++s) num = mfma(kf[t][s], pq[r][t][s], num);
        const float rc = __builtin_amdgcn_rcpf(both_halves(num[12]).x + 1e-6f);
        for (int i = 0; i < 16; ++i) num[i] *= rc;
        if (!h) { num[12] = 0.f; num[13] = 0.f; num[14] = 0.f; num[15] = 0.f; }
        const u32 so = WS_O + ((8 * rga + 2 * w + r) * 32 + 2 * head) * 1024;
        st128_sc1(pack8(num, 0), rws, lane * 16, so);
        st128_sc1(pack8(num, 1), rws, lane * 16, so + 1024);
      }
    }
    vmwait<0>();
    if (lane < 2) st_flag(gws, WS_OF + ((8 * rga + 2 * w + lane) * 16 + head) * 64, E);
    STAMP(9);
    if (!has_tile) { STAMP_OUT(); return; }
  } else if (ffn) {
    u32 bias[4];
    for (int t = 0; t < 4; ++t) bias[t] = *(const GAS unsigned short *)(gb1 + (128 * ct + 32 * t + (lane & 31)) * 2);
    f32x16 acc[2][4]; u32x4 stat[2];
    const u32 vostat = WS_STAT + (256 * rga + 64 * w + (lane & 31)) * 16;
    gemmA_compute<false>(L, w, lane, acc, [&]() { for (int r = 0; r < 2; ++r) stat[r] = ld128_sc1(rws, vostat + 512 * r, 0); } STAMP_PASS);
    RowStat st[2];
    for (int r = 0; r < 2; ++r) st[r] = row_stats(stat_settle(stat[r], rws, vostat + 512 * r, E), h);
    BAR();
    STAMP(3);
    epi_loads();
    for (int t = 0; t < 4; ++t) {
      const u32x4 wa = waug(cs_read(L, t, lane), bias[t] | (bias[t] << 16));
      for (int r = 0; r < 2; ++r) {
        f32x16 a = mfma(wa, st[r].xaug, acc[r][t]);
        for (int i = 0; i < 16; i += 2) { f32x2 v = {relu(a[i]), relu(a[i + 1])}; v *= v; a[i] = v.x; a[i + 1] = v.y; }
        const u32 so = WS_H + ((8 * rga + 2 * w + r) * 112 + 8 * ct + 2 * t) * 1024;
        st128_sc1(pack8(a, 0), rws, lane * 16, so);
        st128_sc1(pack8(a, 1), rws, lane * 16, so + 1024);
      }
    }
    STAMP(4);
    vmwait<0>();
    if (lane < 2) st_flag(gws, WS_HF + ((8 * rga + 2 * w + lane) * 14 + ct) * 64, E);
    STAMP(5);
  } else {
    BAR();
    epi_loads();
  }

  {
    constexpr int RD = RING;
    const u32 soh = WS_H + (rb * 112 + 56 * kg) * 1024, soo = WS_O + (rb * 32 + 16 * kg) * 1024;
    u32x4 ring[RD];
    const float sig = asf(stat_settle(sg0, rws, WS_STAT + row * 16, E)[2]);
    const float rs1 = __builtin_amdgcn_rcpf(sig), rs2 = rs1 * rs1;
    f32x2 bx[8], xx[8];
    for (int g = 0; g < 4; ++g) for (int e = 0; e < 2; ++e) {
      bx[2 * g + e] = f32x2{bf2f(b2v[g][e]), bfhi(b2v[g][e])}; xx[2 * g + e] = f32x2{bf2f(xres[g][e]), bfhi(xres[g][e])};
      asm volatile("" : "+v"(bx[2 * g + e]), "+v"(xx[2 * g + e]));
    }
    const int blk = (lane >> 4) & 1, q = (lane & 15) >> 2, p = lane & 3;
    u32 bb0[2], bb[2];
    for (int c2 = 0; c2 < 2; ++c2) bb0[c2] = (u32)(size_t)L + kg * 8192 + (4 * h + q) * 128 + ((c2 ^ kg ^ (q >> 1)) * 64) + 32 * blk + 8 * p;
    f32x16 af[2] = {zero16(), zero16()}, aa[2] = {zero16(), zero16()};
    u32x4 bf[2][2];
    sfor<0, 72>([&](auto I) {
      constexpr int i = I;
      if constexpr ((i & 3) == 0) asm volatile("s_barrier" ::: "memory");
      if constexpr (i == 0) STAMP(11);
      if constexpr (i == 40) STAMP(14);
      if constexpr (i == 0) {
        for (int j = 0; j < RD; ++j) ring[j] = ld128_sc1(rws, lane * 16, soh + j * 1024);
        __builtin_amdgcn_sched_barrier(0);
        for (int c2 = 0; c2 < 2; ++c2) { bb[c2] = bb0[c2] + 4 * L_SLOT; asm volatile("" : "+v"(bb[c2])); bf[0][c2] = trfrag((lchar *)(size_t)bb[c2], 1024); }
      }
      u32x4 a = ring[i % RD];
      if constexpr (i + 1 < 72) {
        constexpr int n = i + 1;
        if constexpr ((n & 3) == 0) for (int c2 = 0; c2 < 2; ++c2) { bb[c2] = bb0[c2] + (((n >> 2) + 4) % NSLOT) * L_SLOT; asm volatile("" : "+v"(bb[c2])); }
        for (int c2 = 0; c2 < 2; ++c2) bf[n & 1][c2] = trfrag((lchar *)(size_t)bb[c2] + (n & 3) * 2048, 1024);
      }
      if constexpr (i < 56) { af[0] = mfma(bf[i & 1][0], a, af[0]); af[1] = mfma(bf[i & 1][1], a, af[1]); asm volatile("" : "+v"(af[0]), "+v"(af[1])); }
      else { aa[0] = mfma(bf[i & 1][0], a, aa[0]); aa[1] = mfma(bf[i & 1][1], a, aa[1]); asm volatile("" : "+v"(aa[0]), "+v"(aa[1])); }
      if constexpr (i + RD < 56) ring[i % RD] = ld128_sc1(rws, lane * 16, soh + (i + RD) * 1024);
      else if constexpr (i + RD < 72) ring[i % RD] = ld128_sc1(rws, lane * 16, soo + (i + RD - 56) * 1024);
      __builtin_amdgcn_sched_barrier(0);
    });
    STAMP(12);
    lchar *X = L + 4 * L_SLOT;
    {
      const f32x16 &sf = af[1], &sa = aa[1];
      for (int g = 0; g < 4; ++g) {
        *(LAS f32x4 *)(X + w * 8192 + g * 1024 + lane * 16) = f32x4{sf[4 * g], sf[4 * g + 1], sf[4 * g + 2], sf[4 * g + 3]};
        *(LAS f32x4 *)(X + w * 8192 + 4096 + g * 1024 + lane * 16) = f32x4{sa[4 * g], sa[4 * g + 1], sa[4 * g + 2], sa[4 * g + 3]};
      }
    }
    BAR();
    const f32x16 &yf = af[0], &ya = aa[0];
    u32x2 o2[4];
    for (int g = 0; g < 4; ++g) {
      const f32x4 pf = *(LAS f32x4 *)(X + (w ^ 2) * 8192 + g * 1024 + lane * 16), pa = *(LAS f32x4 *)(X + (w ^ 2) * 8192 + 4096 + g * 1024 + lane * 16);
      for (int e = 0; e < 2; ++e) {
        const int i = 4 * g + 2 * e;
        const f32x2 y = (f32x2{yf[i], yf[i + 1]} + f32x2{pf[2 * e], pf[2 * e + 1]}) * rs2 + bx[2 * g + e];
        const f32x2 t = y * -LOG2E;
        const f32x2 d = f32x2{__builtin_amdgcn_exp2f(t.x), __builtin_amdgcn_exp2f(t.y)} + 1.0f;
        const f32x2 sg = y * f32x2{__builtin_amdgcn_rcpf(d.x), __builtin_amdgcn_rcpf(d.y)};
        const f32x2 r = (f32x2{ya[i], ya[i + 1]} + f32x2{pa[2 * e], pa[2 * e + 1]}) * sg + xx[2 * g + e];
        o2[g][e] = pk2(r.x, r.y);
      }
    }
    for (int g = 0; g < 4; g += 2) {
      auto rx0 = __builtin_amdgcn_permlane32_swap(o2[g].x, o2[g + 1].x, false, false);
      auto ry0 = __builtin_amdgcn_permlane32_swap(o2[g].y, o2[g + 1].y, false, false);
      *(GAS u32x4 *)(gout + row * XROW + (col0 + 8 * (g + h)) * 2) = u32x4{rx0[0], ry0[0], rx0[1], ry0[1]};
    }
    STAMP(13);
    STAMP(15);
    STAMP_OUT();
  }
}

extern "C" void kernel_launch(void *const *d_in, const int *in_sizes, int n_in, void *d_out, int out_size, void *d_ws, size_t ws_size, hipStream_t stream) {
  fused_hedgehog_ffn<<<256, 512, 0, stream>>>((const char *)d_in[0], (const char *)d_in[1], (const char *)d_in[2], (const char *)d_in[3], (const char *)d_in[5],
                                              (const char *)d_in[6], (const char *)d_in[7], (const char *)d_in[8], (char *)d_out, (char *)d_ws);
}
```

```cpp
#include <hip/hip_runtime.h>
#include <hip/hip_bf16.h>
#include <stdint.h>
#include <type_traits>

typedef unsigned u32;
typedef u32 u32x4 __attribute__((ext_vector_type(4)));
typedef u32 u32x2 __attribute__((ext_vector_type(2)));
typedef float f32x16 __attribute__((ext_vector_type(16)));
typedef float f32x4 __attribute__((ext_vector_type(4)));
typedef float f32x2 __attribute__((ext_vector_type(2)));
typedef short s16x8 __attribute__((ext_vector_type(8)));
typedef short s16x4 __attribute__((ext_vector_type(4)));
typedef __bf16 bf16x2 __attribute__((ext_vector_type(2)));
typedef __bf16 bf16x8 __attribute__((ext_vector_type(8)));
#define LAS __attribute__((address_space(3)))
#define GAS __attribute__((address_space(1)))
#define DI __device__ __forceinline__
typedef __amdgpu_buffer_rsrc_t rsrc_t;
typedef LAS char lchar;

namespace {
constexpr int DM = 448, DF = 1792;
constexpr u32 XROW = DM * 2;
constexpr u32 QKVROW = 3 * DM * 2;
constexpr u32 W1ROW = DF * 2;
constexpr u32 WS_EPOCH = 0;
constexpr u32 WS_STAT = 4096;
constexpr u32 WS_KF = 65536;
constexpr u32 WS_HF = 131072;
constexpr u32 WS_OF = 196608;
constexpr u32 WS_KVP = 262144;
constexpr u32 WS_H = WS_KVP + (1u << 20);
constexpr u32 WS_O = WS_H + 64 * 112 * 1024;
constexpr u32 WS_END = WS_O + 64 * 32 * 1024;
constexpr u32 L_SLOT = 16384, NSLOT = 9;
constexpr u32 A_SLOT = 49152, A_WOFF = 32768;
constexpr u32 L_MISC = NSLOT * L_SLOT;
constexpr u32 L_WPHI = L_MISC;
constexpr u32 L_ZERO = L_MISC + 2048;
constexpr u32 L_E = L_MISC + 2112;
constexpr u32 L_DUMMY = L_MISC + 2304;
constexpr u32 L_CSP = L_MISC + 2560;
constexpr u32 L_TOK = L_CSP + 512;
constexpr u32 L_KVF = L_TOK + 1024;
constexpr u32 L_END = L_KVF + 4096;
constexpr u32 LDS_BYTES = 163840;
static_assert(L_END <= LDS_BYTES, "lds");
constexpr int SPIN_CAP = 1 << 16;
constexpr float LOG2E = 1.4426950408889634f;
#define CS_DOT2 0
#define SGB 1
#define LPRIO 0
#define FIRST_AT -1
#define PFA_ON 0
#define PF_ON 0
#define PF_DELAY 0
#define PF_PACE 2
#define SPREAD 0
#define PIN_C 0
#define RING 12

#define STAMP(i) do {} while (0)
#define STAMP_DECL do {} while (0)
#define STAMP_OUT() do {} while (0)
#define STAMP_ARG
#define STAMP_PASS
template <int B, int E, class F> DI void sfor(F &&f) {
  if constexpr (B < E) { f(std::integral_constant<int, B>{}); sfor<B + 1, E>(f); }
}
DI u32 rfl(u32 v) { return __builtin_amdgcn_readfirstlane(v); }
DI f32x16 mfma(u32x4 a, u32x4 b, f32x16 c) {
  return __builtin_amdgcn_mfma_f32_32x32x16_bf16(__builtin_bit_cast(bf16x8, a), __builtin_bit_cast(bf16x8, b), c, 0, 0, 0);
}
DI u32 pk2(float lo, float hi) { f32x2 p = {lo, hi}; return __builtin_bit_cast(u32, __builtin_convertvector(p, bf16x2)); }
DI float dot2(u32 a, u32 b, float c) { return __builtin_amdgcn_fdot2_f32_bf16(__builtin_bit_cast(bf16x2, a), __builtin_bit_cast(bf16x2, b), c, false); }
DI float bf2f(u32 lo16) { return __builtin_bit_cast(float, lo16 << 16); }
DI float bfhi(u32 v) { return __builtin_bit_cast(float, v & 0xffff0000u); }
DI float asf(u32 v) { return __builtin_bit_cast(float, v); }
DI f32x2 both_halves(float v) {
  const u32 a = __builtin_bit_cast(u32, v);
  auto r = __builtin_amdgcn_permlane32_swap(a, a, false, false);
  return f32x2{asf(r[0]), asf(r[1])};
}
DI float half_lo(float v) { return both_halves(v).x; }
DI u32x4 pack8(const f32x16 &x, int s) { return u32x4{pk2(x[8 * s], x[8 * s + 1]), pk2(x[8 * s + 2], x[8 * s + 3]), pk2(x[8 * s + 4], x[8 * s + 5]), pk2(x[8 * s + 6], x[8 * s + 7])}; }
DI u32x2 trd(lchar *p) {
  typedef short v4i16 __attribute__((ext_vector_type(4)));
  return __builtin_bit_cast(u32x2, __builtin_amdgcn_ds_read_tr16_b64_v4i16((LAS v4i16 *)p));
}
DI u32x4 trfrag(lchar *p, int tstride) { u32x2 a = trd(p), b = trd(p + tstride); return u32x4{a.x, a.y, b.x, b.y}; }
DI float relu(float v) { return __builtin_amdgcn_fmed3f(v, 0.f, __builtin_inff()); }
DI f32x16 zero16() { f32x16 z; for (int i = 0; i < 16; ++i) z[i] = 0.f; return z; }
#define BAR() asm volatile("s_waitcnt lgkmcnt(0)\n\ts_barrier" ::: "memory")
template <int N> DI void vmwait() { asm volatile("s_waitcnt vmcnt(%0)" ::"i"(N) : "memory"); }
DI void dma16(rsrc_t r, u32 voff, u32 soff, u32 ldsaddr) {
  asm volatile("s_mov_b32 m0, %0\n\ts_nop 0\n\tbuffer_load_dwordx4 %1, %2, %3 offen lds" ::"s"(ldsaddr), "v"(voff), "s"(r), "s"(soff) : "memory", "m0");
}
template <int OFF> DI void ald128(u32x4 &d, rsrc_t r, u32 voff, u32 soff) {
  asm volatile("buffer_load_dwordx4 %0, %1, %2, %3 offen offset:%4" : "=v"(d) : "v"(voff), "s"(r), "s"(soff), "i"(OFF) : "memory");
}
DI u32x4 ld128_sc1(rsrc_t r, u32 voff, u32 soff) { return __builtin_amdgcn_raw_buffer_load_b128(r, voff, soff, 16); }
DI void st128_sc1(u32x4 v, rsrc_t r, u32 voff, u32 soff) {
  __builtin_amdgcn_raw_buffer_store_b128(v, r, voff, soff, 16);
  asm volatile("s_nop 1" ::"v"(v) : "memory");
}
typedef GAS u32 gu32;
DI u32 ld_flag(const char *ws, u32 off) { return __hip_atomic_load((gu32 *)(ws + off), __ATOMIC_RELAXED, __HIP_MEMORY_SCOPE_AGENT); }
DI void st_flag(char *ws, u32 off, u32 v) { __hip_atomic_store((gu32 *)(ws + off), v, __ATOMIC_RELAXED, __HIP_MEMORY_SCOPE_AGENT); }
DI void poll_all(const char *ws, u32 off, u32 E) {
#pragma unroll 1
  for (int spin = 0; spin < SPIN_CAP; ++spin) {
    if (__all(ld_flag(ws, off) == E)) break;
    __builtin_amdgcn_s_sleep(1);
  }
  __builtin_amdgcn_fence(__ATOMIC_ACQUIRE, "wavefront");
}

template <u32 LI, u32 SI> DI void dmai(rsrc_t r, u32 voff, u32 sob, u32 ldsb) {
  u32 t;
  asm volatile("s_add_u32 m0, %2, %5\n\ts_add_u32 %0, %1, %6\n\tbuffer_load_dwordx4 %3, %4, %0 offen lds"
               : "=&s"(t) : "s"(sob), "s"(ldsb), "v"(voff), "s"(r), "i"(LI), "i"(SI) : "memory", "m0", "scc");
}
template <int CTRL, int RMASK> DI float dpp_add(float v) {
  return v + asf(__builtin_amdgcn_update_dpp(0u, __builtin_bit_cast(u32, v), CTRL, RMASK, 0xf, true));
}
DI float wave_sum63(float v) {
  v = dpp_add<0x111, 0xf>(v); v = dpp_add<0x112, 0xf>(v); v = dpp_add<0x114, 0xf>(v); v = dpp_add<0x118, 0xf>(v);
  v = dpp_add<0x142, 0xa>(v); v = dpp_add<0x143, 0xc>(v);
  return v;
}
DI void st64_sc1(char *ws, u32 off, u32 lo, u32 hi) {
  __hip_atomic_store((GAS unsigned long long *)(ws + off), (unsigned long long)lo | ((unsigned long long)hi << 32), __ATOMIC_RELAXED, __HIP_MEMORY_SCOPE_AGENT);
}
struct Early { u32 ev; u32x4 xa, xb; };
DI Early early_issue(char *ws, rsrc_t rx, int row0, int lane) {
  Early e;
  e.ev = ld_flag(ws, WS_EPOCH);
  const u32 vo = lane < 56 ? (u32)lane * 16 : 0x7ffffff0u;
  e.xa = __builtin_amdgcn_raw_buffer_load_b128(rx, vo, row0 * XROW, 0); e.xb = __builtin_amdgcn_raw_buffer_load_b128(rx, vo, (row0 + 1) * XROW, 0);
  return e;
}
DI u32 early_finish(const Early &e, rsrc_t rws, int row0, int lane) {
  const u32 E = rfl(e.ev) + 1, ONES = 0x3F803F80u;
  float s[2], q[2];
  for (int r = 0; r < 2; ++r) {
    const u32x4 &x = r ? e.xb : e.xa;
    s[r] = dot2(x[0], ONES, 0.f); q[r] = dot2(x[0], x[0], 0.f);
    for (int d = 1; d < 4; ++d) { s[r] = dot2(x[d], ONES, s[r]); q[r] = dot2(x[d], x[d], q[r]); }
    s[r] = wave_sum63(s[r]); q[r] = wave_sum63(q[r]);
    const float mean = s[r] * (1.0f / DM);
    const float sig = __builtin_amdgcn_sqrtf(fmaxf(q[r] * (1.0f / DM) - mean * mean, 0.f) + 1e-5f);
    const u32 vo = lane == 63 ? WS_STAT + (row0 + r) * 16 : 0x7ffffff0u;
    __builtin_amdgcn_raw_buffer_store_b64(u32x2{__builtin_bit_cast(u32, mean), E}, rws, vo, 0, 16);
    __builtin_amdgcn_raw_buffer_store_b64(u32x2{__builtin_bit_cast(u32, sig), E}, rws, vo + 8, 0, 16);
  }
  return E;
}
DI u32x4 stat_settle(u32x4 g, rsrc_t rws, u32 vostat, u32 E) {
#pragma unroll 1
  for (int spin = 0; spin < SPIN_CAP && !__all(g[1] == E && g[3] == E); ++spin) { __builtin_amdgcn_s_sleep(1); g = ld128_sc1(rws, vostat, 0); }
  return g;
}
struct RowStat { float rstd; u32x4 xaug; };
DI RowStat row_stats(u32x4 g, int h) {
  const float m = -asf(g[0]), sig = asf(g[2]);
  RowStat r; r.rstd = __builtin_amdgcn_rcpf(sig);
  const float ml = m - bfhi(pk2(m, m)), sl = sig - bfhi(pk2(sig, sig));
  const u32 mm = pk2(m, ml), ss = pk2(sig, sl);
  r.xaug = h ? u32x4{0, 0, 0, 0} : u32x4{mm, mm, ss, 0};
  return r;
}
DI u32x4 waug(float cs, u32 bb) {
  const u32 hh = pk2(cs, cs); const float cl = cs - bfhi(hh);
  return u32x4{hh, pk2(cl, cl), bb, 0};
}
DI float cs_read(lchar *L, int t, int lane) { return *(LAS float *)(L + L_CSP + (t * 32 + (lane & 31)) * 4); }

template <bool ATT, class First, class Mid>
DI void gemmA_compute(lchar *L, int w, int lane, f32x16 (&acc)[2][ATT ? 3 : 4], First &&first, Mid &&mid STAMP_ARG) {
  constexpr int T = ATT ? 3 : 4, ROWB = ATT ? 64 : 256;
  const int h = lane >> 5, blk = (lane >> 4) & 1, q = (lane & 15) >> 2, p = lane & 3;
  const u32 lb = (u32)(size_t)L;
  u32 wb0[T], xb0[4], wb[T], xb[4], cb0, cb;
  for (int t = 0; t < T; ++t) wb0[t] = lb + A_WOFF + (ATT ? t * 4096 + (8 * h + q) * 64 : (8 * h + q) * 256 + 64 * (t ^ q)) + 32 * blk + 8 * p;
  for (int u = 0; u < 4; ++u) xb0[u] = lb + w * 8192 + (lane & 31) * 128 + 16 * ((2 * u + h) ^ ((lane >> 1) & 7));
  const int tw = w < T ? w : T - 1;
  cb0 = lb + A_WOFF + (ATT ? tw * 4096 + (8 * h + q) * 64 : (8 * h + q) * 256 + 64 * (tw ^ q)) + 32 * blk + 8 * p;
  f32x2 ca = {0.f, 0.f};
  for (int r = 0; r < 2; ++r) for (int t = 0; t < T; ++t) acc[r][t] = zero16();
  u32x4 xf[2][2], wf[2][T], cf[2];
  sfor<0, 29>([&](auto I) {
    constexpr int i = I, c = i >> 2, u = i & 3;
    if constexpr (i == 0 && FIRST_AT < 0) { first(); STAMP(14); }
    if constexpr (u == 0 && i < 28) {
      BAR();
      if constexpr (c == 0) STAMP(1);
      constexpr u32 so = (c % 3) * A_SLOT;
      for (int t = 0; t < T; ++t) { wb[t] = wb0[t] + so; asm volatile("" : "+v"(wb[t])); }
      for (int j = 0; j < 4; ++j) { xb[j] = xb0[j] + so; asm volatile("" : "+v"(xb[j])); }
      cb = cb0 + so; asm volatile("" : "+v"(cb));
    }
    if constexpr (i < 28) {
      cf[i & 1] = trfrag((lchar *)(size_t)cb + 16 * u * ROWB, 4 * ROWB);
      for (int r = 0; r < 2; ++r) xf[i & 1][r] = *(LAS u32x4 *)((lchar *)(size_t)xb[u] + r * 4096);
      for (int t = 0; t < T; ++t) wf[i & 1][t] = trfrag((lchar *)(size_t)wb[t] + 16 * u * ROWB, 4 * ROWB);
    }
    if constexpr (i == FIRST_AT) first();
    if constexpr (i == 20) mid();
    if constexpr (i > 0) {
      constexpr int j = (i - 1) & 1;
      for (int t = 0; t < T; ++t)
        for (int r = 0; r < 2; ++r) acc[r][t] = (ATT && t == 1) ? mfma(xf[j][r], wf[j][t], acc[r][t]) : mfma(wf[j][t], xf[j][r], acc[r][t]);
      for (int d = 0; d < 4; ++d) ca += f32x2{bf2f(cf[j][d]), bfhi(cf[j][d])};
      asm volatile("" : "+v"(ca));
      for (int m = 0; m < 2 * T; ++m) { __builtin_amdgcn_sched_group_barrier(0x8, 1, 0); __builtin_amdgcn_sched_group_barrier(0x100, 2, 0); __builtin_amdgcn_sched_group_barrier(0x2, 2, 0); }
    }
    __builtin_amdgcn_sched_barrier(0);
  });
  { const f32x2 a = both_halves(ca.x + ca.y); if (lane < 32 && w < T) *(LAS float *)(L + L_CSP + (w * 32 + lane) * 4) = a.x + a.y; }
  STAMP(2);
}
struct StageCtx { rsrc_t rw2, rwo; u32 vob, so2, soo, ld; };
template <int ST> DI void stage_dma(const StageCtx &c) {
  sfor<0, 4>([&](auto I) {
    constexpr u32 i = I, dst = ((ST + 4) % NSLOT) * L_SLOT + i * 1024;
    dmai<dst, (64 * ST + 8 * i) * XROW>(c.rw2, c.vob, c.so2, c.ld);
  });
}
template <int ST> DI void stage_load(const StageCtx &c, u32x4 (&br)[36]) {
  sfor<0, 4>([&](auto I) {
    constexpr u32 i = I;
    if constexpr (ST < 14) br[4 * (ST - 9) + i] = __builtin_amdgcn_raw_buffer_load_b128(c.rw2, c.vob, c.so2 + (64 * ST + 8 * i) * XROW, 0);
    else br[4 * (ST - 9) + i] = __builtin_amdgcn_raw_buffer_load_b128(c.rwo, c.vob, c.soo + (56 * (ST - 14) + (i == 3 ? 20 : 8 * i)) * XROW, 0);
  });
}
DI void touch_w2(const char *gw2, const char *gwo, u32 u, int lane, u32 lb) {
  const bool a = u < 196;
  const rsrc_t rp = __builtin_amdgcn_make_buffer_rsrc((void *)(a ? gw2 : gwo), 0, a ? DF * XROW : DM * XROW, 0x00020000);
  asm volatile("s_mov_b32 m0, %0\n\ts_nop 0\n\tbuffer_load_dword %1, %2, %3 offen lds" ::"s"(lb + L_DUMMY), "v"(lane * 128), "s"(rp), "s"(rfl((a ? u : u - 196) * 8192)) : "memory", "m0");
}
DI void touch_phase_a(const char *gx, const char *gwqkv, const char *gw1, int xcd, int wv, int lane, u32 lb) {
  const rsrc_t rx = __builtin_amdgcn_make_buffer_rsrc((void *)gx, 0, 2048 * XROW, 0x00020000);
  const rsrc_t rq = __builtin_amdgcn_make_buffer_rsrc((void *)gwqkv, 0, DM * QKVROW, 0x00020000);
  const rsrc_t r1 = __builtin_amdgcn_make_buffer_rsrc((void *)gw1, 0, DM * W1ROW, 0x00020000);
#pragma unroll 1
  for (int q = wv; q < 170; q += 16) {
    const int c = 2 + q / 34, r = q % 34;
    if (r < 8) {
      const u32 vo = (512 * (xcd >> 1) + 64 * r + lane) * XROW + 128 * c;
      asm volatile("s_mov_b32 m0, %0\n\ts_nop 0\n\tbuffer_load_dword %1, %2, 0 offen lds" ::"s"(lb + L_DUMMY), "v"(vo), "s"(rx) : "memory", "m0");
    } else if (r < 22) {
      const u32 j = (r - 8) * 64 + lane, vo = (64 * c + ((j & 127) >> 1)) * W1ROW + 256 * (7 * (xcd & 1) + (j >> 7)) + 128 * (j & 1);
      asm volatile("s_mov_b32 m0, %0\n\ts_nop 0\n\tbuffer_load_dword %1, %2, 0 offen lds" ::"s"(lb + L_DUMMY), "v"(vo), "s"(r1) : "memory", "m0");
    } else {
      const u32 j = (r - 22) * 64 + lane, row = (j * 43691u) >> 19, rem = j - 12 * row;
      const u32 vo = (64 * c + row) * QKVROW + 896 * (rem >> 2) + ((448 * (xcd & 1)) & ~127u) + 128 * (rem & 3);
      asm volatile("s_mov_b32 m0, %0\n\ts_nop 0\n\tbuffer_load_dword %1, %2, 0 offen lds" ::"s"(lb + L_DUMMY), "v"(vo), "s"(rq) : "memory", "m0");
    }
  }
}
template <bool ATT>
DI void gemmA_loader(lchar *L, rsrc_t rx, rsrc_t rw, u32 sox, u32 sow, int lw, int lane, const StageCtx &sc, u32x4 (&br)[36] STAMP_ARG) {
  constexpr int NW = ATT ? 3 : 4, N = 8 + NW;
  const u32 lb = (u32)(size_t)L;
  const u32 vox0 = (lane >> 3) * XROW + 16 * ((lane & 7) ^ ((lane >> 4) & 3)), vox1 = vox0 ^ 64;
  const u32 ldx = rfl(lb + lw * 8192);
  const u32 vow = ATT ? (lane >> 2) * QKVROW + ((lane & 3) == 3 ? 40 : 16 * (lane & 3)) : (lane >> 4) * W1ROW + 16 * ((lane & 15) ^ ((lane >> 4) << 2));
  const u32 swb = rfl(sow + 16 * lw * (ATT ? QKVROW : W1ROW)), ldw = rfl(lb + A_WOFF + lw * (ATT ? 1024 : 4096));
  auto issue = [&](auto C) {
    constexpr u32 c = C, so = (c % 3) * A_SLOT;
    sfor<0, NW>([&](auto I) {
      constexpr u32 i = I;
      if constexpr (ATT) dmai<so + i * 4096, 64 * c * QKVROW + (i == 0 ? 896 : i == 1 ? 1792 : 0)>(rw, vow, swb, ldw);
      else dmai<so + i * 1024, (64 * c + 4 * i) * W1ROW>(rw, vow, swb, ldw);
    });
    sfor<0, 8>([&](auto I) { constexpr u32 i = I; dmai<so + i * 1024, 8 * i * XROW + 128 * c>(rx, (i & 1) ? vox1 : vox0, sox, ldx); });
  };
  issue(std::integral_constant<int, 0>{});
  issue(std::integral_constant<int, 1>{});
  sfor<0, 7>([&](auto C) {
    constexpr int c = C;
    vmwait<(c == 6 ? 12 : N)>();
    asm volatile("s_barrier" ::: "memory");
    if constexpr (c == 0) STAMP(1);
    if constexpr (c + 2 < 7) issue(std::integral_constant<int, c + 2>{});
    else { stage_load<9 + 3 * (c - 5)>(sc, br); stage_load<10 + 3 * (c - 5)>(sc, br); stage_load<11 + 3 * (c - 5)>(sc, br); }
  });
  STAMP(2);
}

DI void feature_map(const f32x16 &P, float rstd, int h, u32x4 (&pf)[2][2]) {
  const float c = rstd * LOG2E;
  float mx = fmaxf(fmaxf(P[0], P[1]), P[2]), mn = fminf(fminf(P[0], P[1]), P[2]);
  for (int i = 3; i < 15; i += 2) { mx = fmaxf(fmaxf(mx, P[i]), P[i + 1]); mn = fminf(fminf(mn, P[i]), P[i + 1]); }
  mx = fmaxf(mx, P[15]); mn = fminf(mn, P[15]);
  { f32x2 a = both_halves(mx), b = both_halves(mn); mx = fmaxf(a.x, a.y); mn = fminf(b.x, b.y); }
  const float mxc = -mx * c, mnc = mn * c;
  f32x16 ep, en;
  for (int i = 0; i < 16; i += 2) {
    const f32x2 p2 = {P[i], P[i + 1]}, ap = p2 * c + mxc, an = mnc - p2 * c;
    ep[i] = __builtin_amdgcn_exp2f(ap.x); ep[i + 1] = __builtin_amdgcn_exp2f(ap.y);
    en[i] = __builtin_amdgcn_exp2f(an.x); en[i + 1] = __builtin_amdgcn_exp2f(an.y);
  }
  f32x2 sp2 = {ep[0], ep[1]}, sn2 = {en[0], en[1]};
  for (int i = 2; i < 12; i += 2) { sp2 += f32x2{ep[i], ep[i + 1]}; sn2 += f32x2{en[i], en[i + 1]}; }
  const float keep = h ? 0.f : 1.f;
  sp2 += (f32x2{ep[12], ep[13]} + f32x2{ep[14], ep[15]}) * keep;
  sn2 += (f32x2{en[12], en[13]} + f32x2{en[14], en[15]}) * keep;
  const f32x2 a = both_halves(sp2.x + sp2.y), b = both_halves(sn2.x + sn2.y);
  const float rp = __builtin_amdgcn_rcpf(a.x + a.y), rn = __builtin_amdgcn_rcpf(b.x + b.y);
  for (int i = 0; i < 16; i += 2) {
    const f32x2 u = f32x2{ep[i], ep[i + 1]} * rp, v = f32x2{en[i], en[i + 1]} * rn;
    ep[i] = u.x; ep[i + 1] = u.y; en[i] = v.x; en[i + 1] = v.y;
  }
  pf[0][0] = pack8(ep, 0); pf[0][1] = pack8(ep, 1); pf[1][0] = pack8(en, 0); pf[1][1] = pack8(en, 1);
}

constexpr int refill_end(int s) { return s <= 0 ? 0 : (12 * s + 4) / 5 > 36 ? 36 : (12 * s + 4) / 5; }
constexpr bool refill_ok() { for (int s = 1; s <= 17; ++s) for (int p = refill_end(s - 1); p < refill_end(s); ++p) if (s < 1 + p / 4 || s > 7 + p / 4) return false; return refill_end(15) == 36; }
static_assert(refill_ok(), "refill schedule");
}

__global__ void __launch_bounds__(512, 2) fused_hedgehog_ffn(const char *__restrict__ gx, const char *__restrict__ gwqkv, const char *__restrict__ gwphi, const char *__restrict__ gwo,
                                                              const char *__restrict__ gw1, const char *__restrict__ gb1, const char *__restrict__ gw2, const char *__restrict__ gb2,
                                                              char *__restrict__ gout, char *__restrict__ gws) {
  __shared__ __attribute__((aligned(1024))) char smem[LDS_BYTES];
  lchar *L = (lchar *)smem;
  asm volatile("" ::"s"(gx), "s"(gwqkv), "s"(gwphi), "s"(gwo), "s"(gw1), "s"(gb1), "s"(gw2), "s"(gb2), "s"(gout), "s"(gws));
  const int tid = threadIdx.x, lane = tid & 63, w = rfl(tid >> 6);
  const int b = blockIdx.x, xcd = b & 7, slot = b >> 3;
  const int h = lane >> 5;
  STAMP_DECL; STAMP(0);
  const rsrc_t rx = __builtin_amdgcn_make_buffer_rsrc((void *)gx, 0, 2048 * XROW, 0x00020000);
  const rsrc_t rws = __builtin_amdgcn_make_buffer_rsrc((void *)gws, 0, WS_END, 0x00020000);
  const int tc = slot >= 16 ? slot - 16 : slot + 16;
  const bool has_tile = tc < 28;
  const u32 rt = tc / 7, cc = tc % 7;
  const bool att = slot < 16, ffn = !att && slot < 30;
  const int head = 8 * (xcd & 1) + (slot & 7);
  const int jf = slot - 16, ct = 7 * (xcd & 1) + (jf >= 7 ? jf - 7 : jf);
  const int rga = 2 * (xcd >> 1) + (att ? (slot >> 3) : (jf >= 7));

  if (w >= 4) {
    const int lw = w - 4;
    StageCtx sc;
    sc.rw2 = __builtin_amdgcn_make_buffer_rsrc((void *)gw2, 0, DF * XROW, 0x00020000);
    sc.rwo = __builtin_amdgcn_make_buffer_rsrc((void *)gwo, 0, DM * XROW, 0x00020000);
    sc.vob = (lane >> 3) * XROW + 16 * ((lane & 7) ^ (((lane >> 4) & 1) << 2));
    sc.so2 = rfl((896 * (lw >> 1) + 32 * (lw & 1)) * XROW + 128 * cc);
    sc.soo = rfl(28 * (8 * (lw >> 1) + (lw & 1)) * XROW + 128 * cc);
    sc.ld = rfl((u32)(size_t)L + (lw >> 1) * 8192 + (lw & 1) * 4096);
    u32 E;
    u32x4 br[36];
    const u32 hfoff = WS_HF + ((8 * xcd + 2 * rt) * 14 + (lane < 28 ? lane : 0)) * 64;
    if (att) {
      const rsrc_t rw = __builtin_amdgcn_make_buffer_rsrc((void *)gwqkv, 0, DM * QKVROW, 0x00020000);
      if (lw < 2) {
        const rsrc_t rp = __builtin_amdgcn_make_buffer_rsrc((void *)gwphi, 0, 16 * 28 * 28 * 2, 0x00020000);
        dma16(rp, lane * 16 + lw * 1024, head * 1568, (u32)(size_t)L + L_WPHI + lw * 1024);
      }
      gemmA_loader<true>(L, rx, rw, rfl((256 * rga + 64 * lw) * XROW), rfl(56 * head), lw, lane, sc, br STAMP_PASS);
      BAR();
      E = rfl(*(LAS u32 *)(L + L_E));
      if (has_tile) sfor<0, 5>([&](auto S) { stage_dma<S>(sc); });
      sfor<15, 18>([&](auto S) { stage_load<S>(sc, br); });
      BAR();
      __builtin_amdgcn_s_setprio(3);
      STAMP(4);
      for (int t = 0; t < 2; ++t) {
        f32x4 s = *(LAS f32x4 *)(L + (t * 4 + lw) * 1024 + lane * 16);
        for (int ww = 1; ww < 4; ++ww) s += *(LAS f32x4 *)(L + ww * 8192 + (t * 4 + lw) * 1024 + lane * 16);
        st128_sc1(__builtin_bit_cast(u32x4, s), rws, lane * 16, WS_KVP + (head * 8 + rga) * 8192 + (t * 4 + lw) * 1024);
      }
      vmwait<0>();
      if (lane == 0) st_flag(gws, WS_KF + ((head * 8 + rga) * 4 + lw) * 64, E);
      STAMP(5);
      poll_all(gws, WS_KF + ((head * 8 + (lane & 7)) * 4 + lw) * 64, E);
      STAMP(7);
      u32 hfl;
      asm volatile("global_load_dword %0, %1, off sc1" : "=v"(hfl) : "v"(gws + hfoff) : "memory");
      u32x4 part[2][8];
      for (int t = 0; t < 2; ++t) for (int r = 0; r < 8; ++r) part[t][r] = ld128_sc1(rws, lane * 16, WS_KVP + (head * 8 + r) * 8192 + (t * 4 + lw) * 1024);
      for (int t = 0; t < 2; ++t) {
        f32x4 s = __builtin_bit_cast(f32x4, part[t][0]);
        for (int r = 1; r < 8; ++r) s += __builtin_bit_cast(f32x4, part[t][r]);
        u32x2 pk = {pk2(s.x, s.y), pk2(s.z, s.w)};
        if (lw == 3 && h) pk = u32x2{0, 0};
        *(LAS u32x2 *)(L + L_KVF + ((t * 2 + (lw >> 1)) * 64 + lane) * 16 + (lw & 1) * 8) = pk;
      }
      asm volatile("" : "+v"(hfl));
      BAR();
      __builtin_amdgcn_s_setprio(0);
      STAMP(8);
      if (!has_tile) {
        if (b == 8 * 12 && lw == 0) {
#pragma unroll 1
          for (int spin = 0; spin < SPIN_CAP; ++spin) {
            bool ok = true;
            for (int i = 0; i < 32; ++i) ok &= ld_flag(gws, WS_STAT + (64 * i + lane) * 16 + 4) == E;
            if (__all(ok)) break;
            __builtin_amdgcn_s_sleep(8);
          }
          if (lane == 0) st_flag(gws, WS_EPOCH, E);
        }
        STAMP_OUT();
        return;
      }
      sfor<5, 9>([&](auto S) { stage_dma<S>(sc); });
      if (!__all(hfl == E)) poll_all(gws, hfoff, E);
    } else if (ffn) {
      const rsrc_t rw = __builtin_amdgcn_make_buffer_rsrc((void *)gw1, 0, DM * W1ROW, 0x00020000);
      gemmA_loader<false>(L, rx, rw, rfl((256 * rga + 64 * lw) * XROW), rfl(256 * ct), lw, lane, sc, br STAMP_PASS);
      BAR();
      E = rfl(*(LAS u32 *)(L + L_E));
      sfor<0, 9>([&](auto S) { stage_dma<S>(sc); });
      sfor<15, 18>([&](auto S) { stage_load<S>(sc, br); });
      poll_all(gws, hfoff, E);
    } else {
      if (PFA_ON) touch_phase_a(gx, gwqkv, gw1, xcd, (slot - 30) * 8 + w, lane, (u32)(size_t)L);
      sfor<0, 9>([&](auto S) { stage_dma<S>(sc); });
      sfor<9, 18>([&](auto S) { stage_load<S>(sc, br); });
      BAR();
      E = rfl(*(LAS u32 *)(L + L_E));
      poll_all(gws, hfoff, E);
    }
    STAMP(10);
    constexpr int SO = (56 - RING) / 4;
    u32 ofl = 0;
    const u32 ofoff = WS_OF + ((8 * xcd + 2 * rt + ((lane >> 4) & 1)) * 16 + (lane & 15)) * 64;
    const u32 ldv = sc.ld + lane * 16;
    sfor<0, 18>([&](auto S) {
      constexpr int s = S;
      if constexpr (s == SO) {
        vmwait<0>();
        asm volatile("" : "+v"(ofl));
        if (!__all(ofl == E)) poll_all(gws, ofoff, E);
      }
      if constexpr (s == 4) vmwait<0>();
      BAR();
      sfor<refill_end(s - 1), refill_end(s)>([&](auto Pc) {
        constexpr int pc = Pc;
        *(LAS u32x4 *)((lchar *)(size_t)ldv + ((9 + pc / 4 + 4) % NSLOT) * L_SLOT + (pc & 3) * 1024) = br[pc];
      });
      if constexpr (s == SO - 4) asm volatile("global_load_dword %0, %1, off sc1" : "=v"(ofl) : "v"(gws + ofoff) : "memory");
    });
    STAMP(12);
    asm volatile("s_barrier" ::: "memory");
    STAMP_OUT();
    return;
  }

  const Early early = early_issue(gws, rx, 8 * b + 2 * w, lane);
  u32 E;
  auto first = [&]() { E = early_finish(early, rws, 8 * b + 2 * w, lane); if (tid == 0) *(LAS u32 *)(L + L_E) = E; };
  const int rh = w & 1, kg = w >> 1;
  const u32 rb = 8 * xcd + 2 * rt + rh;
  const u32 row = 32 * rb + (lane & 31), col0 = 64 * cc + 32 * kg;
  u32x2 xres[4], b2v[4]; u32x4 sg0;
  auto epi_loads = [&]() {
    for (int g = 0; g < 4; ++g) {
      xres[g] = *(const GAS u32x2 *)(gx + row * XROW + (col0 + 8 * g + 4 * h) * 2);
      b2v[g] = *(const GAS u32x2 *)(gb2 + (col0 + 8 * g + 4 * h) * 2);
    }
    sg0 = ld128_sc1(rws, WS_STAT + row * 16, 0);
  };
  if (att) {
    if (tid < 16) *(LAS u32 *)(L + L_ZERO + 4 * tid) = 0;
    u32x4 idf[2];
    for (int s = 0; s < 2; ++s)
      for (int d = 0; d < 4; ++d) {
        const int k0 = 16 * s + 8 * (d >> 1) + 4 * h + 2 * (d & 1), n = lane & 31;
        idf[s][d] = (n == k0 ? 0x3F80u : 0u) | (n == k0 + 1 ? 0x3F800000u : 0u);
      }
    for (int s = 0; s < 2; ++s) asm volatile("" : "+v"(idf[s]));
    f32x16 acc[2][3]; u32x4 stat[2];
    const u32 vostat = WS_STAT + (256 * rga + 64 * w + (lane & 31)) * 16;
    gemmA_compute<true>(L, w, lane, acc, first, [&]() { for (int r = 0; r < 2; ++r) stat[r] = ld128_sc1(rws, vostat + 512 * r, 0); } STAMP_PASS);
    RowStat st[2];
    for (int r = 0; r < 2; ++r) {
      st[r] = row_stats(stat_settle(stat[r], rws, vostat + 512 * r, E), h);
      *(LAS float *)(L + L_TOK + w * 256 + r * 128 + 4 * (lane & 31)) = st[r].rstd;
    }
    BAR();
    STAMP(3);
    {
      const u32x4 wk = waug(cs_read(L, 0, lane), 0), wv = waug(cs_read(L, 1, lane), 0), wq = waug(cs_read(L, 2, lane), 0);
      for (int r = 0; r < 2; ++r) { acc[r][0] = mfma(wk, st[r].xaug, acc[r][0]); acc[r][1] = mfma(st[r].xaug, wv, acc[r][1]); acc[r][2] = mfma(wq, st[r].xaug, acc[r][2]); }
    }
    u32x4 wp[2];
    {
      const int blk = (lane >> 4) & 1, q = (lane & 15) >> 2, p = lane & 3;
      const u32 col = (blk && p == 3) ? 24 : 16 * blk + 4 * p;
      for (int s = 0; s < 2; ++s) {
        u32x2 r[2];
        for (int t = 0; t < 2; ++t) {
          const int dp = 16 * s + 8 * t + 4 * h + q;
          const u32 a = (dp >= 24 && dp < 28) ? L_ZERO + 8 * p : L_WPHI + (dp >= 28 ? dp - 4 : dp) * 56 + col * 2;
          r[t] = trd(L + a);
        }
        wp[s] = u32x4{r[0].x, r[0].y, r[1].x, r[1].y};
      }
    }
    f32x16 kvp[2] = {zero16(), zero16()};
    {
      f32x16 P[2];
      for (int r = 0; r < 2; ++r) P[r] = mfma(wp[0], pack8(acc[r][0], 0), zero16());
      for (int r = 0; r < 2; ++r) P[r] = mfma(wp[1], pack8(acc[r][0], 1), P[r]);
      u32x4 vf[2][2];
      for (int r = 0; r < 2; ++r) {
        f32x16 v = acc[r][1];
        for (int g = 0; g < 4; ++g) {
          const f32x4 rs = *(LAS f32x4 *)(L + L_TOK + w * 256 + r * 128 + (8 * g + 4 * h) * 4);
          for (int e = 0; e < 4; e += 2) { const f32x2 t2 = f32x2{v[4 * g + e], v[4 * g + e + 1]} * f32x2{rs[e], rs[e + 1]}; v[4 * g + e] = t2.x; v[4 * g + e + 1] = t2.y; }
        }
        for (int s = 0; s < 2; ++s) { vf[r][s] = pack8(v, s); if ((lane & 31) == 24) vf[r][s] = u32x4{0x3F803F80u, 0x3F803F80u, 0x3F803F80u, 0x3F803F80u}; }
      }
      u32x4 pf[2][2][2];
      for (int r = 0; r < 2; ++r) feature_map(P[r], st[r].rstd, h, pf[r]);
      f32x16 Z[2][2];
      for (int r = 0; r < 2; ++r) for (int t = 0; t < 2; ++t) Z[r][t] = mfma(pf[r][t][0], idf[0], zero16());
      for (int r = 0; r < 2; ++r) for (int t = 0; t < 2; ++t) Z[r][t] = mfma(pf[r][t][1], idf[1], Z[r][t]);
      for (int r = 0; r < 2; ++r) for (int s = 0; s < 2; ++s) for (int t = 0; t < 2; ++t) kvp[t] = mfma(pack8(Z[r][t], s), vf[r][s], kvp[t]);
    }
    for (int t = 0; t < 2; ++t)
      for (int g = 0; g < 4; ++g)
        *(LAS f32x4 *)(L + w * 8192 + (t * 4 + g) * 1024 + lane * 16) = f32x4{kvp[t][4 * g], kvp[t][4 * g + 1], kvp[t][4 * g + 2], kvp[t][4 * g + 3]};
    STAMP(4);
    BAR();
    u32x4 pq[2][2][2];
    {
      f32x16 P[2];
      for (int r = 0; r < 2; ++r) P[r] = mfma(wp[0], pack8(acc[r][2], 0), zero16());
      for (int r = 0; r < 2; ++r) P[r] = mfma(wp[1], pack8(acc[r][2], 1), P[r]);
      for (int r = 0; r < 2; ++r) {
        feature_map(P[r], st[r].rstd, h, pq[r]);
        for (int t = 0; t < 2; ++t) for (int s = 0; s < 2; ++s) asm volatile("" : "+v"(pq[r][t][s]));
      }
    }
    if (has_tile) epi_loads();
    else { for (int g = 0; g < 4; ++g) { xres[g] = u32x2{0, 0}; b2v[g] = u32x2{0, 0}; } sg0 = u32x4{0, 0, 0, 0}; }
    STAMP(6);
    BAR();
    STAMP(8);
    {
      u32x4 kf[2][2];
      for (int t = 0; t < 2; ++t) for (int s = 0; s < 2; ++s) kf[t][s] = *(LAS u32x4 *)(L + L_KVF + ((t * 2 + s) * 64 + lane) * 16);
      for (int r = 0; r < 2; ++r) {
        f32x16 num = zero16();
        for (int t = 0; t < 2; ++t) for (int s = 0; s < 2; ++s) num = mfma(kf[t][s], pq[r][t][s], num);
        const float rc = __builtin_amdgcn_rcpf(both_halves(num[12]).x + 1e-6f);
        for (int i = 0; i < 16; ++i) num[i] *= rc;
        if (!h) { num[12] = 0.f; num[13] = 0.f; num[14] = 0.f; num[15] = 0.f; }
        const u32 so = WS_O + ((8 * rga + 2 * w + r) * 32 + 2 * head) * 1024;
        st128_sc1(pack8(num, 0), rws, lane * 16, so);
        st128_sc1(pack8(num, 1), rws, lane * 16, so + 1024);
      }
    }
    vmwait<0>();
    if (lane < 2) st_flag(gws, WS_OF + ((8 * rga + 2 * w + lane) * 16 + head) * 64, E);
    STAMP(9);
    if (!has_tile) { STAMP_OUT(); return; }
  } else if (ffn) {
    u32 bias[4];
    for (int t = 0; t < 4; ++t) bias[t] = *(const GAS unsigned short *)(gb1 + (128 * ct + 32 * t + (lane & 31)) * 2);
    f32x16 acc[2][4]; u32x4 stat[2];
    const u32 vostat = WS_STAT + (256 * rga + 64 * w + (lane & 31)) * 16;
    gemmA_compute<false>(L, w, lane, acc, first, [&]() { for (int r = 0; r < 2; ++r) stat[r] = ld128_sc1(rws, vostat + 512 * r, 0); } STAMP_PASS);
    RowStat st[2];
    for (int r = 0; r < 2; ++r) st[r] = row_stats(stat_settle(stat[r], rws, vostat + 512 * r, E), h);
    BAR();
    STAMP(3);
    epi_loads();
    for (int t = 0; t < 4; ++t) {
      const u32x4 wa = waug(cs_read(L, t, lane), bias[t] | (bias[t] << 16));
      for (int r = 0; r < 2; ++r) {
        f32x16 a = mfma(wa, st[r].xaug, acc[r][t]);
        for (int i = 0; i < 16; i += 2) { f32x2 v = {relu(a[i]), relu(a[i + 1])}; v *= v; a[i] = v.x; a[i + 1] = v.y; }
        const u32 so = WS_H + ((8 * rga + 2 * w + r) * 112 + 8 * ct + 2 * t) * 1024;
        st128_sc1(pack8(a, 0), rws, lane * 16, so);
        st128_sc1(pack8(a, 1), rws, lane * 16, so + 1024);
      }
    }
    STAMP(4);
    vmwait<0>();
    if (lane < 2) st_flag(gws, WS_HF + ((8 * rga + 2 * w + lane) * 14 + ct) * 64, E);
    STAMP(5);
  } else {
    first();
    if (PFA_ON) touch_phase_a(gx, gwqkv, gw1, xcd, (slot - 30) * 8 + w, lane, (u32)(size_t)L);
    BAR();
    if (PF_ON) {
      __builtin_amdgcn_s_sleep(PF_DELAY);
#pragma unroll 1
      for (u32 u = (slot - 30) * 4 + w; u < 245; u += 8) { touch_w2(gw2, gwo, u, lane, (u32)(size_t)L); __builtin_amdgcn_s_sleep(PF_PACE); }
    }
    epi_loads();
  }

  {
    constexpr int RD = RING;
    const u32 soh = WS_H + (rb * 112 + 56 * kg) * 1024, soo = WS_O + (rb * 32 + 16 * kg) * 1024;
    u32x4 ring[RD];
    const float sig = asf(stat_settle(sg0, rws, WS_STAT + row * 16, E)[2]);
    const float rs1 = __builtin_amdgcn_rcpf(sig), rs2 = rs1 * rs1;
    f32x2 bx[8], xx[8];
    for (int g = 0; g < 4; ++g) for (int e = 0; e < 2; ++e) {
      bx[2 * g + e] = f32x2{bf2f(b2v[g][e]), bfhi(b2v[g][e])}; xx[2 * g + e] = f32x2{bf2f(xres[g][e]), bfhi(xres[g][e])};
      asm volatile("" : "+v"(bx[2 * g + e]), "+v"(xx[2 * g + e]));
    }
    const int blk = (lane >> 4) & 1, q = (lane & 15) >> 2, p = lane & 3;
    u32 bb0[2], bb[2];
    for (int c2 = 0; c2 < 2; ++c2) bb0[c2] = (u32)(size_t)L + kg * 8192 + (4 * h + q) * 128 + ((c2 ^ kg ^ (q >> 1)) * 64) + 32 * blk + 8 * p;
    f32x16 af[2] = {zero16(), zero16()}, aa[2] = {zero16(), zero16()};
    u32x4 bf[2][2];
    sfor<0, 72>([&](auto I) {
      constexpr int i = I;
      if constexpr ((i & 3) == 0) asm volatile("s_barrier" ::: "memory");
      if constexpr (i == 0) STAMP(11);
      if constexpr (i == 40) STAMP(14);
      if constexpr (i == 0) {
        for (int j = 0; j < RD; ++j) ring[j] = ld128_sc1(rws, lane * 16, soh + j * 1024);
        __builtin_amdgcn_sched_barrier(0);
        for (int c2 = 0; c2 < 2; ++c2) { bb[c2] = bb0[c2] + 4 * L_SLOT; asm volatile("" : "+v"(bb[c2])); bf[0][c2] = trfrag((lchar *)(size_t)bb[c2], 1024); }
      }
      u32x4 a = ring[i % RD];
      if constexpr (i + 1 < 72) {
        constexpr int n = i + 1;
        if constexpr ((n & 3) == 0) for (int c2 = 0; c2 < 2; ++c2) { bb[c2] = bb0[c2] + (((n >> 2) + 4) % NSLOT) * L_SLOT; asm volatile("" : "+v"(bb[c2])); }
        for (int c2 = 0; c2 < 2; ++c2) bf[n & 1][c2] = trfrag((lchar *)(size_t)bb[c2] + (n & 3) * 2048, 1024);
      }
      if constexpr (i < 56) { af[0] = mfma(bf[i & 1][0], a, af[0]); af[1] = mfma(bf[i & 1][1], a, af[1]); asm volatile("" : "+v"(af[0]), "+v"(af[1])); }
      else { aa[0] = mfma(bf[i & 1][0], a, aa[0]); aa[1] = mfma(bf[i & 1][1], a, aa[1]); asm volatile("" : "+v"(aa[0]), "+v"(aa[1])); }
      if constexpr (i + RD < 56) ring[i % RD] = ld128_sc1(rws, lane * 16, soh + (i + RD) * 1024);
      else if constexpr (i + RD < 72) ring[i % RD] = ld128_sc1(rws, lane * 16, soo + (i + RD - 56) * 1024);
      __builtin_amdgcn_sched_barrier(0);
    });
    STAMP(12);
    lchar *X = L + 4 * L_SLOT;
    {
      const f32x16 &sf = af[1], &sa = aa[1];
      for (int g = 0; g < 4; ++g) {
        *(LAS f32x4 *)(X + w * 8192 + g * 1024 + lane * 16) = f32x4{sf[4 * g], sf[4 * g + 1], sf[4 * g + 2], sf[4 * g + 3]};
        *(LAS f32x4 *)(X + w * 8192 + 4096 + g * 1024 + lane * 16) = f32x4{sa[4 * g], sa[4 * g + 1], sa[4 * g + 2], sa[4 * g + 3]};
      }
    }
    BAR();
    const f32x16 &yf = af[0], &ya = aa[0];
    u32x2 o2[4];
    for (int g = 0; g < 4; ++g) {
      const f32x4 pf = *(LAS f32x4 *)(X + (w ^ 2) * 8192 + g * 1024 + lane * 16), pa = *(LAS f32x4 *)(X + (w ^ 2) * 8192 + 4096 + g * 1024 + lane * 16);
      for (int e = 0; e < 2; ++e) {
        const int i = 4 * g + 2 * e;
        const f32x2 y = (f32x2{yf[i], yf[i + 1]} + f32x2{pf[2 * e], pf[2 * e + 1]}) * rs2 + bx[2 * g + e];
        const f32x2 t = y * -LOG2E;
        const f32x2 d = f32x2{__builtin_amdgcn_exp2f(t.x), __builtin_amdgcn_exp2f(t.y)} + 1.0f;
        const f32x2 sg = y * f32x2{__builtin_amdgcn_rcpf(d.x), __builtin_amdgcn_rcpf(d.y)};
        const f32x2 r = (f32x2{ya[i], ya[i + 1]} + f32x2{pa[2 * e], pa[2 * e + 1]}) * sg + xx[2 * g + e];
        o2[g][e] = pk2(r.x, r.y);
      }
    }
    for (int g = 0; g < 4; g += 2) {
      auto rx0 = __builtin_amdgcn_permlane32_swap(o2[g].x, o2[g + 1].x, false, false);
      auto ry0 = __builtin_amdgcn_permlane32_swap(o2[g].y, o2[g + 1].y, false, false);
      *(GAS u32x4 *)(gout + row * XROW + (col0 + 8 * (g + h)) * 2) = u32x4{rx0[0], ry0[0], rx0[1], ry0[1]};
    }
    STAMP(13);
    STAMP(15);
    STAMP_OUT();
  }
}

extern "C" void kernel_launch(void *const *d_in, const int *in_sizes, int n_in, void *d_out, int out_size, void *d_ws, size_t ws_size, hipStream_t stream) {
  fused_hedgehog_ffn<<<256, 512, 0, stream>>>((const char *)d_in[0], (const char *)d_in[1], (const char *)d_in[2], (const char *)d_in[3], (const char *)d_in[5],
                                              (const char *)d_in[6], (const char *)d_in[7], (const char *)d_in[8], (char *)d_out, (char *)d_ws);
}
```

```cpp
#include <hip/hip_runtime.h>
#include <hip/hip_bf16.h>
#include <stdint.h>
#include <type_traits>

typedef unsigned u32;
typedef u32 u32x4 __attribute__((ext_vector_type(4)));
typedef u32 u32x2 __attribute__((ext_vector_type(2)));
typedef float f32x16 __attribute__((ext_vector_type(16)));
typedef float f32x4 __attribute__((ext_vector_type(4)));
typedef float f32x2 __attribute__((ext_vector_type(2)));
typedef short s16x8 __attribute__((ext_vector_type(8)));
typedef short s16x4 __attribute__((ext_vector_type(4)));
typedef __bf16 bf16x2 __attribute__((ext_vector_type(2)));
typedef __bf16 bf16x8 __attribute__((ext_vector_type(8)));
#define LAS __attribute__((address_space(3)))
#define GAS __attribute__((address_space(1)))
#define DI __device__ __forceinline__
typedef __amdgpu_buffer_rsrc_t rsrc_t;
typedef LAS char lchar;

namespace {
constexpr int DM = 448, DF = 1792;
constexpr u32 XROW = DM * 2;
constexpr u32 QKVROW = 3 * DM * 2;
constexpr u32 W1ROW = DF * 2;
constexpr u32 WS_EPOCH = 0;
constexpr u32 WS_STAT = 4096;
constexpr u32 WS_KF = 65536;
constexpr u32 WS_HF = 131072;
constexpr u32 WS_OF = 196608;
constexpr u32 WS_KVP = 262144;
constexpr u32 WS_H = WS_KVP + (1u << 20);
constexpr u32 WS_O = WS_H + 64 * 112 * 1024;
constexpr u32 WS_END = WS_O + 64 * 32 * 1024;
constexpr u32 L_SLOT = 16384, NSLOT = 9;
constexpr u32 A_SLOT = 49152, A_WOFF = 32768;
constexpr u32 L_MISC = NSLOT * L_SLOT;
constexpr u32 L_WPHI = L_MISC;
constexpr u32 L_ZERO = L_MISC + 2048;
constexpr u32 L_E = L_MISC + 2112;
constexpr u32 L_CSP = L_MISC + 2560;
constexpr u32 L_TOK = L_CSP + 512;
constexpr u32 L_KVF = L_TOK + 1024;
constexpr u32 L_END = L_KVF + 4096;
constexpr u32 LDS_BYTES = 163840;
static_assert(L_END <= LDS_BYTES, "lds");
constexpr int SPIN_CAP = 1 << 16;
constexpr float LOG2E = 1.4426950408889634f;
#define CS_DOT2 0
#define SGB 1
#define LPRIO 0
#define FIRST_AT -1
#define OUT_AUX 0
#define SPREAD 0
#define PIN_C 0
#define RING 12

#define STAMP(i) do {} while (0)
#define STAMP_DECL do {} while (0)
#define STAMP_OUT() do {} while (0)
#define STAMP_ARG
#define STAMP_PASS
template <int B, int E, class F> DI void sfor(F &&f) {
  if constexpr (B < E) { f(std::integral_constant<int, B>{}); sfor<B + 1, E>(f); }
}
DI u32 rfl(u32 v) { return __builtin_amdgcn_readfirstlane(v); }
DI f32x16 mfma(u32x4 a, u32x4 b, f32x16 c) {
  return __builtin_amdgcn_mfma_f32_32x32x16_bf16(__builtin_bit_cast(bf16x8, a), __builtin_bit_cast(bf16x8, b), c, 0, 0, 0);
}
DI u32 pk2(float lo, float hi) { f32x2 p = {lo, hi}; return __builtin_bit_cast(u32, __builtin_convertvector(p, bf16x2)); }
DI float dot2(u32 a, u32 b, float c) { return __builtin_amdgcn_fdot2_f32_bf16(__builtin_bit_cast(bf16x2, a), __builtin_bit_cast(bf16x2, b), c, false); }
DI float bf2f(u32 lo16) { return __builtin_bit_cast(float, lo16 << 16); }
DI float bfhi(u32 v) { return __builtin_bit_cast(float, v & 0xffff0000u); }
DI float asf(u32 v) { return __builtin_bit_cast(float, v); }
DI f32x2 both_halves(float v) {
  const u32 a = __builtin_bit_cast(u32, v);
  auto r = __builtin_amdgcn_permlane32_swap(a, a, false, false);
  return f32x2{asf(r[0]), asf(r[1])};
}
DI float half_lo(float v) { return both_halves(v).x; }
DI u32x4 pack8(const f32x16 &x, int s) { return u32x4{pk2(x[8 * s], x[8 * s + 1]), pk2(x[8 * s + 2], x[8 * s + 3]), pk2(x[8 * s + 4], x[8 * s + 5]), pk2(x[8 * s + 6], x[8 * s + 7])}; }
DI u32x2 trd(lchar *p) {
  typedef short v4i16 __attribute__((ext_vector_type(4)));
  return __builtin_bit_cast(u32x2, __builtin_amdgcn_ds_read_tr16_b64_v4i16((LAS v4i16 *)p));
}
DI u32x4 trfrag(lchar *p, int tstride) { u32x2 a = trd(p), b = trd(p + tstride); return u32x4{a.x, a.y, b.x, b.y}; }
DI float relu(float v) { return __builtin_amdgcn_fmed3f(v, 0.f, __builtin_inff()); }
DI f32x16 zero16() { f32x16 z; for (int i = 0; i < 16; ++i) z[i] = 0.f; return z; }
#define BAR() asm volatile("s_waitcnt lgkmcnt(0)\n\ts_barrier" ::: "memory")
template <int N> DI void vmwait() { asm volatile("s_waitcnt vmcnt(%0)" ::"i"(N) : "memory"); }
DI void dma16(rsrc_t r, u32 voff, u32 soff, u32 ldsaddr) {
  asm volatile("s_mov_b32 m0, %0\n\ts_nop 0\n\tbuffer_load_dwordx4 %1, %2, %3 offen lds" ::"s"(ldsaddr), "v"(voff), "s"(r), "s"(soff) : "memory", "m0");
}
template <int OFF> DI void ald128(u32x4 &d, rsrc_t r, u32 voff, u32 soff) {
  asm volatile("buffer_load_dwordx4 %0, %1, %2, %3 offen offset:%4" : "=v"(d) : "v"(voff), "s"(r), "s"(soff), "i"(OFF) : "memory");
}
DI u32x4 ld128_sc1(rsrc_t r, u32 voff, u32 soff) { return __builtin_amdgcn_raw_buffer_load_b128(r, voff, soff, 16); }
DI void st128_sc1(u32x4 v, rsrc_t r, u32 voff, u32 soff) {
  __builtin_amdgcn_raw_buffer_store_b128(v, r, voff, soff, 16);
  asm volatile("s_nop 1" ::"v"(v) : "memory");
}
typedef GAS u32 gu32;
DI u32 ld_flag(const char *ws, u32 off) { return __hip_atomic_load((gu32 *)(ws + off), __ATOMIC_RELAXED, __HIP_MEMORY_SCOPE_AGENT); }
DI void st_flag(char *ws, u32 off, u32 v) { __hip_atomic_store((gu32 *)(ws + off), v, __ATOMIC_RELAXED, __HIP_MEMORY_SCOPE_AGENT); }
DI void poll_all(const char *ws, u32 off, u32 E) {
#pragma unroll 1
  for (int spin = 0; spin < SPIN_CAP; ++spin) {
    if (__all(ld_flag(ws, off) == E)) break;
    __builtin_amdgcn_s_sleep(1);
  }
  __builtin_amdgcn_fence(__ATOMIC_ACQUIRE, "wavefront");
}

template <u32 LI, u32 SI> DI void dmai(rsrc_t r, u32 voff, u32 sob, u32 ldsb) {
  u32 t;
  asm volatile("s_add_u32 m0, %2, %5\n\ts_add_u32 %0, %1, %6\n\tbuffer_load_dwordx4 %3, %4, %0 offen lds"
               : "=&s"(t) : "s"(sob), "s"(ldsb), "v"(voff), "s"(r), "i"(LI), "i"(SI) : "memory", "m0", "scc");
}
template <int CTRL, int RMASK> DI float dpp_add(float v) {
  return v + asf(__builtin_amdgcn_update_dpp(0u, __builtin_bit_cast(u32, v), CTRL, RMASK, 0xf, true));
}
DI float wave_sum63(float v) {
  v = dpp_add<0x111, 0xf>(v); v = dpp_add<0x112, 0xf>(v); v = dpp_add<0x114, 0xf>(v); v = dpp_add<0x118, 0xf>(v);
  v = dpp_add<0x142, 0xa>(v); v = dpp_add<0x143, 0xc>(v);
  return v;
}
DI void st64_sc1(char *ws, u32 off, u32 lo, u32 hi) {
  __hip_atomic_store((GAS unsigned long long *)(ws + off), (unsigned long long)lo | ((unsigned long long)hi << 32), __ATOMIC_RELAXED, __HIP_MEMORY_SCOPE_AGENT);
}
struct Early { u32 ev; u32x4 xa, xb; };
DI Early early_issue(char *ws, rsrc_t rx, int row0, int lane) {
  Early e;
  e.ev = ld_flag(ws, WS_EPOCH);
  const u32 vo = lane < 56 ? (u32)lane * 16 : 0x7ffffff0u;
  e.xa = __builtin_amdgcn_raw_buffer_load_b128(rx, vo, row0 * XROW, 0); e.xb = __builtin_amdgcn_raw_buffer_load_b128(rx, vo, (row0 + 1) * XROW, 0);
  return e;
}
DI u32 early_finish(const Early &e, rsrc_t rws, int row0, int lane) {
  const u32 E = rfl(e.ev) + 1, ONES = 0x3F803F80u;
  float s[2], q[2];
  for (int r = 0; r < 2; ++r) {
    const u32x4 &x = r ? e.xb : e.xa;
    s[r] = dot2(x[0], ONES, 0.f); q[r] = dot2(x[0], x[0], 0.f);
    for (int d = 1; d < 4; ++d) { s[r] = dot2(x[d], ONES, s[r]); q[r] = dot2(x[d], x[d], q[r]); }
    s[r] = wave_sum63(s[r]); q[r] = wave_sum63(q[r]);
    const float mean = s[r] * (1.0f / DM);
    const float sig = __builtin_amdgcn_sqrtf(fmaxf(q[r] * (1.0f / DM) - mean * mean, 0.f) + 1e-5f);
    const u32 vo = lane == 63 ? WS_STAT + (row0 + r) * 16 : 0x7ffffff0u;
    __builtin_amdgcn_raw_buffer_store_b64(u32x2{__builtin_bit_cast(u32, mean), E}, rws, vo, 0, 16);
    __builtin_amdgcn_raw_buffer_store_b64(u32x2{__builtin_bit_cast(u32, sig), E}, rws, vo + 8, 0, 16);
  }
  return E;
}
DI u32x4 stat_settle(u32x4 g, rsrc_t rws, u32 vostat, u32 E) {
#pragma unroll 1
  for (int spin = 0; spin < SPIN_CAP && !__all(g[1] == E && g[3] == E); ++spin) { __builtin_amdgcn_s_sleep(1); g = ld128_sc1(rws, vostat, 0); }
  return g;
}
struct RowStat { float rstd; u32x4 xaug; };
DI RowStat row_stats(u32x4 g, int h) {
  const float m = -asf(g[0]), sig = asf(g[2]);
  RowStat r; r.rstd = __builtin_amdgcn_rcpf(sig);
  const float ml = m - bfhi(pk2(m, m)), sl = sig - bfhi(pk2(sig, sig));
  const u32 mm = pk2(m, ml), ss = pk2(sig, sl);
  r.xaug = h ? u32x4{0, 0, 0, 0} : u32x4{mm, mm, ss, 0};
  return r;
}
DI u32x4 waug(float cs, u32 bb) {
  const u32 hh = pk2(cs, cs); const float cl = cs - bfhi(hh);
  return u32x4{hh, pk2(cl, cl), bb, 0};
}
DI float cs_read(lchar *L, int t, int lane) { return *(LAS float *)(L + L_CSP + (t * 32 + (lane & 31)) * 4); }

template <bool ATT, class First, class Mid>
DI void gemmA_compute(lchar *L, int w, int lane, f32x16 (&acc)[2][ATT ? 3 : 4], First &&first, Mid &&mid STAMP_ARG) {
  constexpr int T = ATT ? 3 : 4, ROWB = ATT ? 64 : 256;
  const int h = lane >> 5, blk = (lane >> 4) & 1, q = (lane & 15) >> 2, p = lane & 3;
  const u32 lb = (u32)(size_t)L;
  u32 wb0[T], xb0[4], wb[T], xb[4], cb0, cb;
  for (int t = 0; t < T; ++t) wb0[t] = lb + A_WOFF + (ATT ? t * 4096 + (8 * h + q) * 64 : (8 * h + q) * 256 + 64 * (t ^ q)) + 32 * blk + 8 * p;
  for (int u = 0; u < 4; ++u) xb0[u] = lb + w * 8192 + (lane & 31) * 128 + 16 * ((2 * u + h) ^ ((lane >> 1) & 7));
  const int tw = w < T ? w : T - 1;
  cb0 = lb + A_WOFF + (ATT ? tw * 4096 + (8 * h + q) * 64 : (8 * h + q) * 256 + 64 * (tw ^ q)) + 32 * blk + 8 * p;
  f32x2 ca = {0.f, 0.f};
  for (int r = 0; r < 2; ++r) for (int t = 0; t < T; ++t) acc[r][t] = zero16();
  u32x4 xf[2][2], wf[2][T], cf[2];
  sfor<0, 29>([&](auto I) {
    constexpr int i = I, c = i >> 2, u = i & 3;
    if constexpr (i == 0 && FIRST_AT < 0) { first(); STAMP(14); }
    if constexpr (u == 0 && i < 28) {
      BAR();
      if constexpr (c == 0) STAMP(1);
      constexpr u32 so = (c % 3) * A_SLOT;
      for (int t = 0; t < T; ++t) { wb[t] = wb0[t] + so; asm volatile("" : "+v"(wb[t])); }
      for (int j = 0; j < 4; ++j) { xb[j] = xb0[j] + so; asm volatile("" : "+v"(xb[j])); }
      cb = cb0 + so; asm volatile("" : "+v"(cb));
    }
    if constexpr (i < 28) {
      cf[i & 1] = trfrag((lchar *)(size_t)cb + 16 * u * ROWB, 4 * ROWB);
      for (int r = 0; r < 2; ++r) xf[i & 1][r] = *(LAS u32x4 *)((lchar *)(size_t)xb[u] + r * 4096);
      for (int t = 0; t < T; ++t) wf[i & 1][t] = trfrag((lchar *)(size_t)wb[t] + 16 * u * ROWB, 4 * ROWB);
    }
    if constexpr (i == FIRST_AT) first();
    if constexpr (i == 20) mid();
    if constexpr (i > 0) {
      constexpr int j = (i - 1) & 1;
      for (int t = 0; t < T; ++t)
        for (int r = 0; r < 2; ++r) acc[r][t] = (ATT && t == 1) ? mfma(xf[j][r], wf[j][t], acc[r][t]) : mfma(wf[j][t], xf[j][r], acc[r][t]);
      for (int d = 0; d < 4; ++d) ca += f32x2{bf2f(cf[j][d]), bfhi(cf[j][d])};
      asm volatile("" : "+v"(ca));
      for (int m = 0; m < 2 * T; ++m) { __builtin_amdgcn_sched_group_barrier(0x8, 1, 0); __builtin_amdgcn_sched_group_barrier(0x100, 2, 0); __builtin_amdgcn_sched_group_barrier(0x2, 2, 0); }
    }
    __builtin_amdgcn_sched_barrier(0);
  });
  { const f32x2 a = both_halves(ca.x + ca.y); if (lane < 32 && w < T) *(LAS float *)(L + L_CSP + (w * 32 + lane) * 4) = a.x + a.y; }
  STAMP(2);
}
struct StageCtx { rsrc_t rw2, rwo; u32 vob, so2, soo, ld; };
template <int ST> DI void stage_dma(const StageCtx &c) {
  sfor<0, 4>([&](auto I) {
    constexpr u32 i = I, dst = ((ST + 4) % NSLOT) * L_SLOT + i * 1024;
    dmai<dst, (64 * ST + 8 * i) * XROW>(c.rw2, c.vob, c.so2, c.ld);
  });
}
template <int ST> DI void stage_load(const StageCtx &c, u32x4 (&br)[36]) {
  sfor<0, 4>([&](auto I) {
    constexpr u32 i = I;
    if constexpr (ST < 14) br[4 * (ST - 9) + i] = __builtin_amdgcn_raw_buffer_load_b128(c.rw2, c.vob, c.so2 + (64 * ST + 8 * i) * XROW, 0);
    else br[4 * (ST - 9) + i] = __builtin_amdgcn_raw_buffer_load_b128(c.rwo, c.vob, c.soo + (56 * (ST - 14) + (i == 3 ? 20 : 8 * i)) * XROW, 0);
  });
}
template <bool ATT>
DI void gemmA_loader(lchar *L, rsrc_t rx, rsrc_t rw, u32 sox, u32 sow, int lw, int lane, const StageCtx &sc, u32x4 (&br)[36] STAMP_ARG) {
  constexpr int NW = ATT ? 3 : 4, N = 8 + NW;
  const u32 lb = (u32)(size_t)L;
  const u32 vox0 = (lane >> 3) * XROW + 16 * ((lane & 7) ^ ((lane >> 4) & 3)), vox1 = vox0 ^ 64;
  const u32 ldx = rfl(lb + lw * 8192);
  const u32 vow = ATT ? (lane >> 2) * QKVROW + ((lane & 3) == 3 ? 40 : 16 * (lane & 3)) : (lane >> 4) * W1ROW + 16 * ((lane & 15) ^ ((lane >> 4) << 2));
  const u32 swb = rfl(sow + 16 * lw * (ATT ? QKVROW : W1ROW)), ldw = rfl(lb + A_WOFF + lw * (ATT ? 1024 : 4096));
  auto issue = [&](auto C) {
    constexpr u32 c = C, so = (c % 3) * A_SLOT;
    sfor<0, NW>([&](auto I) {
      constexpr u32 i = I;
      if constexpr (ATT) dmai<so + i * 4096, 64 * c * QKVROW + (i == 0 ? 896 : i == 1 ? 1792 : 0)>(rw, vow, swb, ldw);
      else dmai<so + i * 1024, (64 * c + 4 * i) * W1ROW>(rw, vow, swb, ldw);
    });
    sfor<0, 8>([&](auto I) { constexpr u32 i = I; dmai<so + i * 1024, 8 * i * XROW + 128 * c>(rx, (i & 1) ? vox1 : vox0, sox, ldx); });
  };
  issue(std::integral_constant<int, 0>{});
  issue(std::integral_constant<int, 1>{});
  sfor<0, 7>([&](auto C) {
    constexpr int c = C;
    vmwait<(c == 6 ? 12 : N)>();
    asm volatile("s_barrier" ::: "memory");
    if constexpr (c == 0) STAMP(1);
    if constexpr (c + 2 < 7) issue(std::integral_constant<int, c + 2>{});
    else { stage_load<9 + 3 * (c - 5)>(sc, br); stage_load<10 + 3 * (c - 5)>(sc, br); stage_load<11 + 3 * (c - 5)>(sc, br); }
  });
  STAMP(2);
}

DI void feature_map(const f32x16 &P, float rstd, int h, u32x4 (&pf)[2][2]) {
  const float c = rstd * LOG2E;
  float mx = fmaxf(fmaxf(P[0], P[1]), P[2]), mn = fminf(fminf(P[0], P[1]), P[2]);
  for (int i = 3; i < 15; i += 2) { mx = fmaxf(fmaxf(mx, P[i]), P[i + 1]); mn = fminf(fminf(mn, P[i]), P[i + 1]); }
  mx = fmaxf(mx, P[15]); mn = fminf(mn, P[15]);
  { f32x2 a = both_halves(mx), b = both_halves(mn); mx = fmaxf(a.x, a.y); mn = fminf(b.x, b.y); }
  const float mxc = -mx * c, mnc = mn * c;
  f32x16 ep, en;
  for (int i = 0; i < 16; i += 2) {
    const f32x2 p2 = {P[i], P[i + 1]}, ap = p2 * c + mxc, an = mnc - p2 * c;
    ep[i] = __builtin_amdgcn_exp2f(ap.x); ep[i + 1] = __builtin_amdgcn_exp2f(ap.y);
    en[i] = __builtin_amdgcn_exp2f(an.x); en[i + 1] = __builtin_amdgcn_exp2f(an.y);
  }
  f32x2 sp2 = {ep[0], ep[1]}, sn2 = {en[0], en[1]};
  for (int i = 2; i < 12; i += 2) { sp2 += f32x2{ep[i], ep[i + 1]}; sn2 += f32x2{en[i], en[i + 1]}; }
  const float keep = h ? 0.f : 1.f;
  sp2 += (f32x2{ep[12], ep[13]} + f32x2{ep[14], ep[15]}) * keep;
  sn2 += (f32x2{en[12], en[13]} + f32x2{en[14], en[15]}) * keep;
  const f32x2 a = both_halves(sp2.x + sp2.y), b = both_halves(sn2.x + sn2.y);
  const float rp = __builtin_amdgcn_rcpf(a.x + a.y), rn = __builtin_amdgcn_rcpf(b.x + b.y);
  for (int i = 0; i < 16; i += 2) {
    const f32x2 u = f32x2{ep[i], ep[i + 1]} * rp, v = f32x2{en[i], en[i + 1]} * rn;
    ep[i] = u.x; ep[i + 1] = u.y; en[i] = v.x; en[i + 1] = v.y;
  }
  pf[0][0] = pack8(ep, 0); pf[0][1] = pack8(ep, 1); pf[1][0] = pack8(en, 0); pf[1][1] = pack8(en, 1);
}

constexpr int refill_end(int s) { return s <= 0 ? 0 : (12 * s + 4) / 5 > 36 ? 36 : (12 * s + 4) / 5; }
constexpr bool refill_ok() { for (int s = 1; s <= 17; ++s) for (int p = refill_end(s - 1); p < refill_end(s); ++p) if (s < 1 + p / 4 || s > 7 + p / 4) return false; return refill_end(15) == 36; }
static_assert(refill_ok(), "refill schedule");
}

__global__ void __launch_bounds__(512, 2) fused_hedgehog_ffn(const char *__restrict__ gx, const char *__restrict__ gwqkv, const char *__restrict__ gwphi, const char *__restrict__ gwo,
                                                              const char *__restrict__ gw1, const char *__restrict__ gb1, const char *__restrict__ gw2, const char *__restrict__ gb2,
                                                              char *__restrict__ gout, char *__restrict__ gws) {
  __shared__ __attribute__((aligned(1024))) char smem[LDS_BYTES];
  lchar *L = (lchar *)smem;
  asm volatile("" ::"s"(gx), "s"(gwqkv), "s"(gwphi), "s"(gwo), "s"(gw1), "s"(gb1), "s"(gw2), "s"(gb2), "s"(gout), "s"(gws));
  const int tid = threadIdx.x, lane = tid & 63, w = rfl(tid >> 6);
  const int b = blockIdx.x, xcd = b & 7, slot = b >> 3;
  const int h = lane >> 5;
  STAMP_DECL; STAMP(0);
  const rsrc_t rx = __builtin_amdgcn_make_buffer_rsrc((void *)gx, 0, 2048 * XROW, 0x00020000);
  const rsrc_t rws = __builtin_amdgcn_make_buffer_rsrc((void *)gws, 0, WS_END, 0x00020000);
  const int tc = slot >= 16 ? slot - 16 : slot + 16;
  const bool has_tile = tc < 28;
  const u32 rt = tc / 7, cc = tc % 7;
  const bool att = slot < 16, ffn = !att && slot < 30;
  const int head = 8 * (xcd & 1) + (slot & 7);
  const int jf = slot - 16, ct = 7 * (xcd & 1) + (jf >= 7 ? jf - 7 : jf);
  const int rga = 2 * (xcd >> 1) + (att ? (slot >> 3) : (jf >= 7));

  if (w >= 4) {
    const int lw = w - 4;
    StageCtx sc;
    sc.rw2 = __builtin_amdgcn_make_buffer_rsrc((void *)gw2, 0, DF * XROW, 0x00020000);
    sc.rwo = __builtin_amdgcn_make_buffer_rsrc((void *)gwo, 0, DM * XROW, 0x00020000);
    sc.vob = (lane >> 3) * XROW + 16 * ((lane & 7) ^ (((lane >> 4) & 1) << 2));
    sc.so2 = rfl((896 * (lw >> 1) + 32 * (lw & 1)) * XROW + 128 * cc);
    sc.soo = rfl(28 * (8 * (lw >> 1) + (lw & 1)) * XROW + 128 * cc);
    sc.ld = rfl((u32)(size_t)L + (lw >> 1) * 8192 + (lw & 1) * 4096);
    u32 E;
    u32x4 br[36];
    const u32 hfoff = WS_HF + ((8 * xcd + 2 * rt) * 14 + (lane < 28 ? lane : 0)) * 64;
    if (att) {
      const rsrc_t rw = __builtin_amdgcn_make_buffer_rsrc((void *)gwqkv, 0, DM * QKVROW, 0x00020000);
      if (lw < 2) {
        const rsrc_t rp = __builtin_amdgcn_make_buffer_rsrc((void *)gwphi, 0, 16 * 28 * 28 * 2, 0x00020000);
        dma16(rp, lane * 16 + lw * 1024, head * 1568, (u32)(size_t)L + L_WPHI + lw * 1024);
      }
      gemmA_loader<true>(L, rx, rw, rfl((256 * rga + 64 * lw) * XROW), rfl(56 * head), lw, lane, sc, br STAMP_PASS);
      BAR();
      E = rfl(*(LAS u32 *)(L + L_E));
      if (has_tile) sfor<0, 5>([&](auto S) { stage_dma<S>(sc); });
      sfor<15, 18>([&](auto S) { stage_load<S>(sc, br); });
      BAR();
      __builtin_amdgcn_s_setprio(3);
      STAMP(4);
      for (int t = 0; t < 2; ++t) {
        f32x4 s = *(LAS f32x4 *)(L + (t * 4 + lw) * 1024 + lane * 16);
        for (int ww = 1; ww < 4; ++ww) s += *(LAS f32x4 *)(L + ww * 8192 + (t * 4 + lw) * 1024 + lane * 16);
        st128_sc1(__builtin_bit_cast(u32x4, s), rws, lane * 16, WS_KVP + (head * 8 + rga) * 8192 + (t * 4 + lw) * 1024);
      }
      vmwait<0>();
      if (lane == 0) st_flag(gws, WS_KF + ((head * 8 + rga) * 4 + lw) * 64, E);
      STAMP(5);
      poll_all(gws, WS_KF + ((head * 8 + (lane & 7)) * 4 + lw) * 64, E);
      STAMP(7);
      u32 hfl;
      asm volatile("global_load_dword %0, %1, off sc1" : "=v"(hfl) : "v"(gws + hfoff) : "memory");
      u32x4 part[2][8];
      for (int t = 0; t < 2; ++t) for (int r = 0; r < 8; ++r) part[t][r] = ld128_sc1(rws, lane * 16, WS_KVP + (head * 8 + r) * 8192 + (t * 4 + lw) * 1024);
      for (int t = 0; t < 2; ++t) {
        f32x4 s = __builtin_bit_cast(f32x4, part[t][0]);
        for (int r = 1; r < 8; ++r) s += __builtin_bit_cast(f32x4, part[t][r]);
        u32x2 pk = {pk2(s.x, s.y), pk2(s.z, s.w)};
        if (lw == 3 && h) pk = u32x2{0, 0};
        *(LAS u32x2 *)(L + L_KVF + ((t * 2 + (lw >> 1)) * 64 + lane) * 16 + (lw & 1) * 8) = pk;
      }
      asm volatile("" : "+v"(hfl));
      BAR();
      __builtin_amdgcn_s_setprio(0);
      STAMP(8);
      if (!has_tile) {
        if (b == 8 * 12 && lw == 0) {
#pragma unroll 1
          for (int spin = 0; spin < SPIN_CAP; ++spin) {
            bool ok = true;
            for (int i = 0; i < 32; ++i) ok &= ld_flag(gws, WS_STAT + (64 * i + lane) * 16 + 4) == E;
            if (__all(ok)) break;
            __builtin_amdgcn_s_sleep(8);
          }
          if (lane == 0) st_flag(gws, WS_EPOCH, E);
        }
        STAMP_OUT();
        return;
      }
      sfor<5, 9>([&](auto S) { stage_dma<S>(sc); });
      if (!__all(hfl == E)) poll_all(gws, hfoff, E);
    } else if (ffn) {
      const rsrc_t rw = __builtin_amdgcn_make_buffer_rsrc((void *)gw1, 0, DM * W1ROW, 0x00020000);
      gemmA_loader<false>(L, rx, rw, rfl((256 * rga + 64 * lw) * XROW), rfl(256 * ct), lw, lane, sc, br STAMP_PASS);
      BAR();
      E = rfl(*(LAS u32 *)(L + L_E));
      sfor<0, 9>([&](auto S) { stage_dma<S>(sc); });
      sfor<15, 18>([&](auto S) { stage_load<S>(sc, br); });
      poll_all(gws, hfoff, E);
    } else {
      sfor<0, 9>([&](auto S) { stage_dma<S>(sc); });
      sfor<9, 18>([&](auto S) { stage_load<S>(sc, br); });
      BAR();
      E = rfl(*(LAS u32 *)(L + L_E));
      poll_all(gws, hfoff, E);
    }
    STAMP(10);
    constexpr int SO = (56 - RING) / 4;
    u32 ofl = 0;
    const u32 ofoff = WS_OF + ((8 * xcd + 2 * rt + ((lane >> 4) & 1)) * 16 + (lane & 15)) * 64;
    const u32 ldv = sc.ld + lane * 16;
    sfor<0, 18>([&](auto S) {
      constexpr int s = S;
      if constexpr (s == SO) {
        vmwait<0>();
        asm volatile("" : "+v"(ofl));
        if (!__all(ofl == E)) poll_all(gws, ofoff, E);
      }
      if constexpr (s == 4) vmwait<0>();
      BAR();
      sfor<refill_end(s - 1), refill_end(s)>([&](auto Pc) {
        constexpr int pc = Pc;
        *(LAS u32x4 *)((lchar *)(size_t)ldv + ((9 + pc / 4 + 4) % NSLOT) * L_SLOT + (pc & 3) * 1024) = br[pc];
      });
      if constexpr (s == SO - 4) asm volatile("global_load_dword %0, %1, off sc1" : "=v"(ofl) : "v"(gws + ofoff) : "memory");
    });
    STAMP(12);
    asm volatile("s_barrier" ::: "memory");
    STAMP_OUT();
    return;
  }

  const Early early = early_issue(gws, rx, 8 * b + 2 * w, lane);
  u32 E;
  auto first = [&]() { E = early_finish(early, rws, 8 * b + 2 * w, lane); if (tid == 0) *(LAS u32 *)(L + L_E) = E; };
  const int rh = w & 1, kg = w >> 1;
  const u32 rb = 8 * xcd + 2 * rt + rh;
  const u32 row = 32 * rb + (lane & 31), col0 = 64 * cc + 32 * kg;
  u32x2 xres[4], b2v[4]; u32x4 sg0;
  auto epi_loads = [&]() {
    for (int g = 0; g < 4; ++g) {
      xres[g] = *(const GAS u32x2 *)(gx + row * XROW + (col0 + 8 * g + 4 * h) * 2);
      b2v[g] = *(const GAS u32x2 *)(gb2 + (col0 + 8 * g + 4 * h) * 2);
    }
    sg0 = ld128_sc1(rws, WS_STAT + row * 16, 0);
  };
  if (att) {
    if (tid < 16) *(LAS u32 *)(L + L_ZERO + 4 * tid) = 0;
    u32x4 idf[2];
    for (int s = 0; s < 2; ++s)
      for (int d = 0; d < 4; ++d) {
        const int k0 = 16 * s + 8 * (d >> 1) + 4 * h + 2 * (d & 1), n = lane & 31;
        idf[s][d] = (n == k0 ? 0x3F80u : 0u) | (n == k0 + 1 ? 0x3F800000u : 0u);
      }
    for (int s = 0; s < 2; ++s) asm volatile("" : "+v"(idf[s]));
    f32x16 acc[2][3]; u32x4 stat[2];
    const u32 vostat = WS_STAT + (256 * rga + 64 * w + (lane & 31)) * 16;
    gemmA_compute<true>(L, w, lane, acc, first, [&]() { for (int r = 0; r < 2; ++r) stat[r] = ld128_sc1(rws, vostat + 512 * r, 0); } STAMP_PASS);
    RowStat st[2];
    for (int r = 0; r < 2; ++r) {
      st[r] = row_stats(stat_settle(stat[r], rws, vostat + 512 * r, E), h);
      *(LAS float *)(L + L_TOK + w * 256 + r * 128 + 4 * (lane & 31)) = st[r].rstd;
    }
    BAR();
    STAMP(3);
    {
      const u32x4 wk = waug(cs_read(L, 0, lane), 0), wv = waug(cs_read(L, 1, lane), 0), wq = waug(cs_read(L, 2, lane), 0);
      for (int r = 0; r < 2; ++r) { acc[r][0] = mfma(wk, st[r].xaug, acc[r][0]); acc[r][1] = mfma(st[r].xaug, wv, acc[r][1]); acc[r][2] = mfma(wq, st[r].xaug, acc[r][2]); }
    }
    u32x4 wp[2];
    {
      const int blk = (lane >> 4) & 1, q = (lane & 15) >> 2, p = lane & 3;
      const u32 col = (blk && p == 3) ? 24 : 16 * blk + 4 * p;
      for (int s = 0; s < 2; ++s) {
        u32x2 r[2];
        for (int t = 0; t < 2; ++t) {
          const int dp = 16 * s + 8 * t + 4 * h + q;
          const u32 a = (dp >= 24 && dp < 28) ? L_ZERO + 8 * p : L_WPHI + (dp >= 28 ? dp - 4 : dp) * 56 + col * 2;
          r[t] = trd(L + a);
        }
        wp[s] = u32x4{r[0].x, r[0].y, r[1].x, r[1].y};
      }
    }
    f32x16 kvp[2] = {zero16(), zero16()};
    {
      f32x16 P[2];
      for (int r = 0; r < 2; ++r) P[r] = mfma(wp[0], pack8(acc[r][0], 0), zero16());
      for (int r = 0; r < 2; ++r) P[r] = mfma(wp[1], pack8(acc[r][0], 1), P[r]);
      u32x4 vf[2][2];
      for (int r = 0; r < 2; ++r) {
        f32x16 v = acc[r][1];
        for (int g = 0; g < 4; ++g) {
          const f32x4 rs = *(LAS f32x4 *)(L + L_TOK + w * 256 + r * 128 + (8 * g + 4 * h) * 4);
          for (int e = 0; e < 4; e += 2) { const f32x2 t2 = f32x2{v[4 * g + e], v[4 * g + e + 1]} * f32x2{rs[e], rs[e + 1]}; v[4 * g + e] = t2.x; v[4 * g + e + 1] = t2.y; }
        }
        for (int s = 0; s < 2; ++s) { vf[r][s] = pack8(v, s); if ((lane & 31) == 24) vf[r][s] = u32x4{0x3F803F80u, 0x3F803F80u, 0x3F803F80u, 0x3F803F80u}; }
      }
      u32x4 pf[2][2][2];
      for (int r = 0; r < 2; ++r) feature_map(P[r], st[r].rstd, h, pf[r]);
      f32x16 Z[2][2];
      for (int r = 0; r < 2; ++r) for (int t = 0; t < 2; ++t) Z[r][t] = mfma(pf[r][t][0], idf[0], zero16());
      for (int r = 0; r < 2; ++r) for (int t = 0; t < 2; ++t) Z[r][t] = mfma(pf[r][t][1], idf[1], Z[r][t]);
      for (int r = 0; r < 2; ++r) for (int s = 0; s < 2; ++s) for (int t = 0; t < 2; ++t) kvp[t] = mfma(pack8(Z[r][t], s), vf[r][s], kvp[t]);
    }
    for (int t = 0; t < 2; ++t)
      for (int g = 0; g < 4; ++g)
        *(LAS f32x4 *)(L + w * 8192 + (t * 4 + g) * 1024 + lane * 16) = f32x4{kvp[t][4 * g], kvp[t][4 * g + 1], kvp[t][4 * g + 2], kvp[t][4 * g + 3]};
    STAMP(4);
    BAR();
    u32x4 pq[2][2][2];
    {
      f32x16 P[2];
      for (int r = 0; r < 2; ++r) P[r] = mfma(wp[0], pack8(acc[r][2], 0), zero16());
      for (int r = 0; r < 2; ++r) P[r] = mfma(wp[1], pack8(acc[r][2], 1), P[r]);
      for (int r = 0; r < 2; ++r) {
        feature_map(P[r], st[r].rstd, h, pq[r]);
        for (int t = 0; t < 2; ++t) for (int s = 0; s < 2; ++s) asm volatile("" : "+v"(pq[r][t][s]));
      }
    }
    if (has_tile) epi_loads();
    else { for (int g = 0; g < 4; ++g) { xres[g] = u32x2{0, 0}; b2v[g] = u32x2{0, 0}; } sg0 = u32x4{0, 0, 0, 0}; }
    STAMP(6);
    BAR();
    STAMP(8);
    {
      u32x4 kf[2][2];
      for (int t = 0; t < 2; ++t) for (int s = 0; s < 2; ++s) kf[t][s] = *(LAS u32x4 *)(L + L_KVF + ((t * 2 + s) * 64 + lane) * 16);
      for (int r = 0; r < 2; ++r) {
        f32x16 num = zero16();
        for (int t = 0; t < 2; ++t) for (int s = 0; s < 2; ++s) num = mfma(kf[t][s], pq[r][t][s], num);
        const float rc = __builtin_amdgcn_rcpf(both_halves(num[12]).x + 1e-6f);
        for (int i = 0; i < 16; ++i) num[i] *= rc;
        if (!h) { num[12] = 0.f; num[13] = 0.f; num[14] = 0.f; num[15] = 0.f; }
        const u32 so = WS_O + ((8 * rga + 2 * w + r) * 32 + 2 * head) * 1024;
        st128_sc1(pack8(num, 0), rws, lane * 16, so);
        st128_sc1(pack8(num, 1), rws, lane * 16, so + 1024);
      }
    }
    vmwait<0>();
    if (lane < 2) st_flag(gws, WS_OF + ((8 * rga + 2 * w + lane) * 16 + head) * 64, E);
    STAMP(9);
    if (!has_tile) { STAMP_OUT(); return; }
  } else if (ffn) {
    u32 bias[4];
    for (int t = 0; t < 4; ++t) bias[t] = *(const GAS unsigned short *)(gb1 + (128 * ct + 32 * t + (lane & 31)) * 2);
    f32x16 acc[2][4]; u32x4 stat[2];
    const u32 vostat = WS_STAT + (256 * rga + 64 * w + (lane & 31)) * 16;
    gemmA_compute<false>(L, w, lane, acc, first, [&]() { for (int r = 0; r < 2; ++r) stat[r] = ld128_sc1(rws, vostat + 512 * r, 0); } STAMP_PASS);
    RowStat st[2];
    for (int r = 0; r < 2; ++r) st[r] = row_stats(stat_settle(stat[r], rws, vostat + 512 * r, E), h);
    BAR();
    STAMP(3);
    epi_loads();
    for (int t = 0; t < 4; ++t) {
      const u32x4 wa = waug(cs_read(L, t, lane), bias[t] | (bias[t] << 16));
      for (int r = 0; r < 2; ++r) {
        f32x16 a = mfma(wa, st[r].xaug, acc[r][t]);
        for (int i = 0; i < 16; i += 2) { f32x2 v = {relu(a[i]), relu(a[i + 1])}; v *= v; a[i] = v.x; a[i + 1] = v.y; }
        const u32 so = WS_H + ((8 * rga + 2 * w + r) * 112 + 8 * ct + 2 * t) * 1024;
        st128_sc1(pack8(a, 0), rws, lane * 16, so);
        st128_sc1(pack8(a, 1), rws, lane * 16, so + 1024);
      }
    }
    STAMP(4);
    vmwait<0>();
    if (lane < 2) st_flag(gws, WS_HF + ((8 * rga + 2 * w + lane) * 14 + ct) * 64, E);
    STAMP(5);
  } else {
    first();
    BAR();
    epi_loads();
  }

  {
    constexpr int RD = RING;
    const u32 soh = WS_H + (rb * 112 + 56 * kg) * 1024, soo = WS_O + (rb * 32 + 16 * kg) * 1024;
    u32x4 ring[RD];
    const float sig = asf(stat_settle(sg0, rws, WS_STAT + row * 16, E)[2]);
    const float rs1 = __builtin_amdgcn_rcpf(sig), rs2 = rs1 * rs1;
    f32x2 bx[8], xx[8];
    for (int g = 0; g < 4; ++g) for (int e = 0; e < 2; ++e) {
      bx[2 * g + e] = f32x2{bf2f(b2v[g][e]), bfhi(b2v[g][e])}; xx[2 * g + e] = f32x2{bf2f(xres[g][e]), bfhi(xres[g][e])};
      asm volatile("" : "+v"(bx[2 * g + e]), "+v"(xx[2 * g + e]));
    }
    const int blk = (lane >> 4) & 1, q = (lane & 15) >> 2, p = lane & 3;
    u32 bb0[2], bb[2];
    for (int c2 = 0; c2 < 2; ++c2) bb0[c2] = (u32)(size_t)L + kg * 8192 + (4 * h + q) * 128 + ((c2 ^ kg ^ (q >> 1)) * 64) + 32 * blk + 8 * p;
    f32x16 af[2] = {zero16(), zero16()}, aa[2] = {zero16(), zero16()};
    u32x4 bf[2][2];
    sfor<0, 72>([&](auto I) {
      constexpr int i = I;
      if constexpr ((i & 3) == 0) asm volatile("s_barrier" ::: "memory");
      if constexpr (i == 0) STAMP(11);
      if constexpr (i == 40) STAMP(14);
      if constexpr (i == 0) {
        for (int j = 0; j < RD; ++j) ring[j] = ld128_sc1(rws, lane * 16, soh + j * 1024);
        __builtin_amdgcn_sched_barrier(0);
        for (int c2 = 0; c2 < 2; ++c2) { bb[c2] = bb0[c2] + 4 * L_SLOT; asm volatile("" : "+v"(bb[c2])); bf[0][c2] = trfrag((lchar *)(size_t)bb[c2], 1024); }
      }
      u32x4 a = ring[i % RD];
      if constexpr (i + 1 < 72) {
        constexpr int n = i + 1;
        if constexpr ((n & 3) == 0) for (int c2 = 0; c2 < 2; ++c2) { bb[c2] = bb0[c2] + (((n >> 2) + 4) % NSLOT) * L_SLOT; asm volatile("" : "+v"(bb[c2])); }
        for (int c2 = 0; c2 < 2; ++c2) bf[n & 1][c2] = trfrag((lchar *)(size_t)bb[c2] + (n & 3) * 2048, 1024);
      }
      if constexpr (i < 56) { af[0] = mfma(bf[i & 1][0], a, af[0]); af[1] = mfma(bf[i & 1][1], a, af[1]); asm volatile("" : "+v"(af[0]), "+v"(af[1])); }
      else { aa[0] = mfma(bf[i & 1][0], a, aa[0]); aa[1] = mfma(bf[i & 1][1], a, aa[1]); asm volatile("" : "+v"(aa[0]), "+v"(aa[1])); }
      if constexpr (i + RD < 56) ring[i % RD] = ld128_sc1(rws, lane * 16, soh + (i + RD) * 1024);
      else if constexpr (i + RD < 72) ring[i % RD] = ld128_sc1(rws, lane * 16, soo + (i + RD - 56) * 1024);
      __builtin_amdgcn_sched_barrier(0);
    });
    STAMP(12);
    lchar *X = L + 4 * L_SLOT;
    {
      const f32x16 &sf = af[1], &sa = aa[1];
      for (int g = 0; g < 4; ++g) {
        *(LAS f32x4 *)(X + w * 8192 + g * 1024 + lane * 16) = f32x4{sf[4 * g], sf[4 * g + 1], sf[4 * g + 2], sf[4 * g + 3]};
        *(LAS f32x4 *)(X + w * 8192 + 4096 + g * 1024 + lane * 16) = f32x4{sa[4 * g], sa[4 * g + 1], sa[4 * g + 2], sa[4 * g + 3]};
      }
    }
    BAR();
    const f32x16 &yf = af[0], &ya = aa[0];
    u32x2 o2[4];
    for (int g = 0; g < 4; ++g) {
      const f32x4 pf = *(LAS f32x4 *)(X + (w ^ 2) * 8192 + g * 1024 + lane * 16), pa = *(LAS f32x4 *)(X + (w ^ 2) * 8192 + 4096 + g * 1024 + lane * 16);
      for (int e = 0; e < 2; ++e) {
        const int i = 4 * g + 2 * e;
        const f32x2 y = (f32x2{yf[i], yf[i + 1]} + f32x2{pf[2 * e], pf[2 * e + 1]}) * rs2 + bx[2 * g + e];
        const f32x2 t = y * -LOG2E;
        const f32x2 d = f32x2{__builtin_amdgcn_exp2f(t.x), __builtin_amdgcn_exp2f(t.y)} + 1.0f;
        const f32x2 sg = y * f32x2{__builtin_amdgcn_rcpf(d.x), __builtin_amdgcn_rcpf(d.y)};
        const f32x2 r = (f32x2{ya[i], ya[i + 1]} + f32x2{pa[2 * e], pa[2 * e + 1]}) * sg + xx[2 * g + e];
        o2[g][e] = pk2(r.x, r.y);
      }
    }
    const rsrc_t rout = __builtin_amdgcn_make_buffer_rsrc((void *)gout, 0, 2048 * XROW, 0x00020000);
    for (int g = 0; g < 4; g += 2) {
      auto rx0 = __builtin_amdgcn_permlane32_swap(o2[g].x, o2[g + 1].x, false, false);
      auto ry0 = __builtin_amdgcn_permlane32_swap(o2[g].y, o2[g + 1].y, false, false);
      __builtin_amdgcn_raw_buffer_store_b128(u32x4{rx0[0], ry0[0], rx0[1], ry0[1]}, rout, row * XROW + (col0 + 8 * (g + h)) * 2, 0, OUT_AUX);
    }
    STAMP(13);
    STAMP(15);
    STAMP_OUT();
  }
}

extern "C" void kernel_launch(void *const *d_in, const int *in_sizes, int n_in, void *d_out, int out_size, void *d_ws, size_t ws_size, hipStream_t stream) {
  fused_hedgehog_ffn<<<256, 512, 0, stream>>>((const char *)d_in[0], (const char *)d_in[1], (const char *)d_in[2], (const char *)d_in[3], (const char *)d_in[5],
                                              (const char *)d_in[6], (const char *)d_in[7], (const char *)d_in[8], (char *)d_out, (char *)d_ws);
}
```

```cpp
#include <hip/hip_runtime.h>
#include <hip/hip_bf16.h>
#include <stdint.h>
#include <type_traits>

typedef unsigned u32;
typedef u32 u32x4 __attribute__((ext_vector_type(4)));
typedef u32 u32x2 __attribute__((ext_vector_type(2)));
typedef float f32x16 __attribute__((ext_vector_type(16)));
typedef float f32x4 __attribute__((ext_vector_type(4)));
typedef float f32x2 __attribute__((ext_vector_type(2)));
typedef short s16x8 __attribute__((ext_vector_type(8)));
typedef short s16x4 __attribute__((ext_vector_type(4)));
typedef __bf16 bf16x2 __attribute__((ext_vector_type(2)));
typedef __bf16 bf16x8 __attribute__((ext_vector_type(8)));
#define LAS __attribute__((address_space(3)))
#define GAS __attribute__((address_space(1)))
#define DI __device__ __forceinline__
typedef __amdgpu_buffer_rsrc_t rsrc_t;
typedef LAS char lchar;

namespace {
constexpr int DM = 448, DF = 1792;
constexpr u32 XROW = DM * 2;
constexpr u32 QKVROW = 3 * DM * 2;
constexpr u32 W1ROW = DF * 2;
constexpr u32 WS_EPOCH = 0;
constexpr u32 WS_STAT = 4096;
constexpr u32 WS_KF = 65536;
constexpr u32 WS_HF = 131072;
constexpr u32 WS_OF = 196608;
constexpr u32 WS_KVP = 262144;
constexpr u32 WS_H = WS_KVP + (1u << 20);
constexpr u32 WS_O = WS_H + 64 * 112 * 1024;
constexpr u32 WS_END = WS_O + 64 * 32 * 1024;
constexpr u32 L_SLOT = 16384, NSLOT = 9;
constexpr u32 A_SLOT = 49152, A_WOFF = 32768;
constexpr u32 L_MISC = NSLOT * L_SLOT;
constexpr u32 L_WPHI = L_MISC;
constexpr u32 L_ZERO = L_MISC + 2048;
constexpr u32 L_E = L_MISC + 2112;
constexpr u32 L_DUMMY = L_MISC + 2304;
constexpr u32 L_CSP = L_MISC + 2560;
constexpr u32 L_TOK = L_CSP + 512;
constexpr u32 L_KVF = L_TOK + 1024;
constexpr u32 L_END = L_KVF + 4096;
constexpr u32 LDS_BYTES = 163840;
static_assert(L_END <= LDS_BYTES, "lds");
constexpr int SPIN_CAP = 1 << 16;
constexpr float LOG2E = 1.4426950408889634f;
#define CS_DOT2 0
#define SGB 1
#define LPRIO 0
#define FIRST_AT -1
#define CODE_PF 0
#define OUT_AUX 0
#define SPREAD 0
#define PIN_C 0
#define RING 12

#define STAMP(i) do {} while (0)
#define STAMP_DECL do {} while (0)
#define STAMP_OUT() do {} while (0)
#define STAMP_ARG
#define STAMP_PASS
template <int B, int E, class F> DI void sfor(F &&f) {
  if constexpr (B < E) { f(std::integral_constant<int, B>{}); sfor<B + 1, E>(f); }
}
DI u32 rfl(u32 v) { return __builtin_amdgcn_readfirstlane(v); }
DI f32x16 mfma(u32x4 a, u32x4 b, f32x16 c) {
  return __builtin_amdgcn_mfma_f32_32x32x16_bf16(__builtin_bit_cast(bf16x8, a), __builtin_bit_cast(bf16x8, b), c, 0, 0, 0);
}
DI u32 pk2(float lo, float hi) { f32x2 p = {lo, hi}; return __builtin_bit_cast(u32, __builtin_convertvector(p, bf16x2)); }
DI float dot2(u32 a, u32 b, float c) { return __builtin_amdgcn_fdot2_f32_bf16(__builtin_bit_cast(bf16x2, a), __builtin_bit_cast(bf16x2, b), c, false); }
DI float bf2f(u32 lo16) { return __builtin_bit_cast(float, lo16 << 16); }
DI float bfhi(u32 v) { return __builtin_bit_cast(float, v & 0xffff0000u); }
DI float asf(u32 v) { return __builtin_bit_cast(float, v); }
DI f32x2 both_halves(float v) {
  const u32 a = __builtin_bit_cast(u32, v);
  auto r = __builtin_amdgcn_permlane32_swap(a, a, false, false);
  return f32x2{asf(r[0]), asf(r[1])};
}
DI float half_lo(float v) { return both_halves(v).x; }
DI u32x4 pack8(const f32x16 &x, int s) { return u32x4{pk2(x[8 * s], x[8 * s + 1]), pk2(x[8 * s + 2], x[8 * s + 3]), pk2(x[8 * s + 4], x[8 * s + 5]), pk2(x[8 * s + 6], x[8 * s + 7])}; }
DI u32x2 trd(lchar *p) {
  typedef short v4i16 __attribute__((ext_vector_type(4)));
  return __builtin_bit_cast(u32x2, __builtin_amdgcn_ds_read_tr16_b64_v4i16((LAS v4i16 *)p));
}
DI u32x4 trfrag(lchar *p, int tstride) { u32x2 a = trd(p), b = trd(p + tstride); return u32x4{a.x, a.y, b.x, b.y}; }
DI float relu(float v) { return __builtin_amdgcn_fmed3f(v, 0.f, __builtin_inff()); }
DI f32x16 zero16() { f32x16 z; for (int i = 0; i < 16; ++i) z[i] = 0.f; return z; }
#define BAR() asm volatile("s_waitcnt lgkmcnt(0)\n\ts_barrier" ::: "memory")
template <int N> DI void vmwait() { asm volatile("s_waitcnt vmcnt(%0)" ::"i"(N) : "memory"); }
DI void dma16(rsrc_t r, u32 voff, u32 soff, u32 ldsaddr) {
  asm volatile("s_mov_b32 m0, %0\n\ts_nop 0\n\tbuffer_load_dwordx4 %1, %2, %3 offen lds" ::"s"(ldsaddr), "v"(voff), "s"(r), "s"(soff) : "memory", "m0");
}
template <int OFF> DI void ald128(u32x4 &d, rsrc_t r, u32 voff, u32 soff) {
  asm volatile("buffer_load_dwordx4 %0, %1, %2, %3 offen offset:%4" : "=v"(d) : "v"(voff), "s"(r), "s"(soff), "i"(OFF) : "memory");
}
DI u32x4 ld128_sc1(rsrc_t r, u32 voff, u32 soff) { return __builtin_amdgcn_raw_buffer_load_b128(r, voff, soff, 16); }
DI void st128_sc1(u32x4 v, rsrc_t r, u32 voff, u32 soff) {
  __builtin_amdgcn_raw_buffer_store_b128(v, r, voff, soff, 16);
  asm volatile("s_nop 1" ::"v"(v) : "memory");
}
typedef GAS u32 gu32;
DI u32 ld_flag(const char *ws, u32 off) { return __hip_atomic_load((gu32 *)(ws + off), __ATOMIC_RELAXED, __HIP_MEMORY_SCOPE_AGENT); }
DI void st_flag(char *ws, u32 off, u32 v) { __hip_atomic_store((gu32 *)(ws + off), v, __ATOMIC_RELAXED, __HIP_MEMORY_SCOPE_AGENT); }
DI void poll_all(const char *ws, u32 off, u32 E) {
#pragma unroll 1
  for (int spin = 0; spin < SPIN_CAP; ++spin) {
    if (__all(ld_flag(ws, off) == E)) break;
    __builtin_amdgcn_s_sleep(1);
  }
  __builtin_amdgcn_fence(__ATOMIC_ACQUIRE, "wavefront");
}

template <u32 LI, u32 SI> DI void dmai(rsrc_t r, u32 voff, u32 sob, u32 ldsb) {
  u32 t;
  asm volatile("s_add_u32 m0, %2, %5\n\ts_add_u32 %0, %1, %6\n\tbuffer_load_dwordx4 %3, %4, %0 offen lds"
               : "=&s"(t) : "s"(sob), "s"(ldsb), "v"(voff), "s"(r), "i"(LI), "i"(SI) : "memory", "m0", "scc");
}
template <int CTRL, int RMASK> DI float dpp_add(float v) {
  return v + asf(__builtin_amdgcn_update_dpp(0u, __builtin_bit_cast(u32, v), CTRL, RMASK, 0xf, true));
}
DI float wave_sum63(float v) {
  v = dpp_add<0x111, 0xf>(v); v = dpp_add<0x112, 0xf>(v); v = dpp_add<0x114, 0xf>(v); v = dpp_add<0x118, 0xf>(v);
  v = dpp_add<0x142, 0xa>(v); v = dpp_add<0x143, 0xc>(v);
  return v;
}
DI void st64_sc1(char *ws, u32 off, u32 lo, u32 hi) {
  __hip_atomic_store((GAS unsigned long long *)(ws + off), (unsigned long long)lo | ((unsigned long long)hi << 32), __ATOMIC_RELAXED, __HIP_MEMORY_SCOPE_AGENT);
}
struct Early { u32 ev; u32x4 xa, xb; };
DI Early early_issue(char *ws, rsrc_t rx, int row0, int lane) {
  Early e;
  e.ev = ld_flag(ws, WS_EPOCH);
  const u32 vo = lane < 56 ? (u32)lane * 16 : 0x7ffffff0u;
  e.xa = __builtin_amdgcn_raw_buffer_load_b128(rx, vo, row0 * XROW, 0); e.xb = __builtin_amdgcn_raw_buffer_load_b128(rx, vo, (row0 + 1) * XROW, 0);
  return e;
}
DI u32 early_finish(const Early &e, rsrc_t rws, int row0, int lane) {
  const u32 E = rfl(e.ev) + 1, ONES = 0x3F803F80u;
  float s[2], q[2];
  for (int r = 0; r < 2; ++r) {
    const u32x4 &x = r ? e.xb : e.xa;
    s[r] = dot2(x[0], ONES, 0.f); q[r] = dot2(x[0], x[0], 0.f);
    for (int d = 1; d < 4; ++d) { s[r] = dot2(x[d], ONES, s[r]); q[r] = dot2(x[d], x[d], q[r]); }
    s[r] = wave_sum63(s[r]); q[r] = wave_sum63(q[r]);
    const float mean = s[r] * (1.0f / DM);
    const float sig = __builtin_amdgcn_sqrtf(fmaxf(q[r] * (1.0f / DM) - mean * mean, 0.f) + 1e-5f);
    const u32 vo = lane == 63 ? WS_STAT + (row0 + r) * 16 : 0x7ffffff0u;
    __builtin_amdgcn_raw_buffer_store_b64(u32x2{__builtin_bit_cast(u32, mean), E}, rws, vo, 0, 16);
    __builtin_amdgcn_raw_buffer_store_b64(u32x2{__builtin_bit_cast(u32, sig), E}, rws, vo + 8, 0, 16);
  }
  return E;
}
DI u32x4 stat_settle(u32x4 g, rsrc_t rws, u32 vostat, u32 E) {
#pragma unroll 1
  for (int spin = 0; spin < SPIN_CAP && !__all(g[1] == E && g[3] == E); ++spin) { __builtin_amdgcn_s_sleep(1); g = ld128_sc1(rws, vostat, 0); }
  return g;
}
struct RowStat { float rstd; u32x4 xaug; };
DI RowStat row_stats(u32x4 g, int h) {
  const float m = -asf(g[0]), sig = asf(g[2]);
  RowStat r; r.rstd = __builtin_amdgcn_rcpf(sig);
  const float ml = m - bfhi(pk2(m, m)), sl = sig - bfhi(pk2(sig, sig));
  const u32 mm = pk2(m, ml), ss = pk2(sig, sl);
  r.xaug = h ? u32x4{0, 0, 0, 0} : u32x4{mm, mm, ss, 0};
  return r;
}
DI u32x4 waug(float cs, u32 bb) {
  const u32 hh = pk2(cs, cs); const float cl = cs - bfhi(hh);
  return u32x4{hh, pk2(cl, cl), bb, 0};
}
DI float cs_read(lchar *L, int t, int lane) { return *(LAS float *)(L + L_CSP + (t * 32 + (lane & 31)) * 4); }

template <bool ATT, class First, class Mid>
DI void gemmA_compute(lchar *L, int w, int lane, f32x16 (&acc)[2][ATT ? 3 : 4], First &&first, Mid &&mid STAMP_ARG) {
  constexpr int T = ATT ? 3 : 4, ROWB = ATT ? 64 : 256;
  const int h = lane >> 5, blk = (lane >> 4) & 1, q = (lane & 15) >> 2, p = lane & 3;
  const u32 lb = (u32)(size_t)L;
  u32 wb0[T], xb0[4], wb[T], xb[4], cb0, cb;
  for (int t = 0; t < T; ++t) wb0[t] = lb + A_WOFF + (ATT ? t * 4096 + (8 * h + q) * 64 : (8 * h + q) * 256 + 64 * (t ^ q)) + 32 * blk + 8 * p;
  for (int u = 0; u < 4; ++u) xb0[u] = lb + w * 8192 + (lane & 31) * 128 + 16 * ((2 * u + h) ^ ((lane >> 1) & 7));
  const int tw = w < T ? w : T - 1;
  cb0 = lb + A_WOFF + (ATT ? tw * 4096 + (8 * h + q) * 64 : (8 * h + q) * 256 + 64 * (tw ^ q)) + 32 * blk + 8 * p;
  f32x2 ca = {0.f, 0.f};
  for (int r = 0; r < 2; ++r) for (int t = 0; t < T; ++t) acc[r][t] = zero16();
  u32x4 xf[2][2], wf[2][T], cf[2];
  sfor<0, 29>([&](auto I) {
    constexpr int i = I, c = i >> 2, u = i & 3;
    if constexpr (i == 0 && FIRST_AT < 0) { first(); STAMP(14); }
    if constexpr (u == 0 && i < 28) {
      BAR();
      if constexpr (c == 0) STAMP(1);
      constexpr u32 so = (c % 3) * A_SLOT;
      for (int t = 0; t < T; ++t) { wb[t] = wb0[t] + so; asm volatile("" : "+v"(wb[t])); }
      for (int j = 0; j < 4; ++j) { xb[j] = xb0[j] + so; asm volatile("" : "+v"(xb[j])); }
      cb = cb0 + so; asm volatile("" : "+v"(cb));
    }
    if constexpr (i < 28) {
      cf[i & 1] = trfrag((lchar *)(size_t)cb + 16 * u * ROWB, 4 * ROWB);
      for (int r = 0; r < 2; ++r) xf[i & 1][r] = *(LAS u32x4 *)((lchar *)(size_t)xb[u] + r * 4096);
      for (int t = 0; t < T; ++t) wf[i & 1][t] = trfrag((lchar *)(size_t)wb[t] + 16 * u * ROWB, 4 * ROWB);
    }
    if constexpr (i == FIRST_AT) first();
    if constexpr (i == 20) mid();
    if constexpr (i > 0) {
      constexpr int j = (i - 1) & 1;
      for (int t = 0; t < T; ++t)
        for (int r = 0; r < 2; ++r) acc[r][t] = (ATT && t == 1) ? mfma(xf[j][r], wf[j][t], acc[r][t]) : mfma(wf[j][t], xf[j][r], acc[r][t]);
      for (int d = 0; d < 4; ++d) ca += f32x2{bf2f(cf[j][d]), bfhi(cf[j][d])};
      asm volatile("" : "+v"(ca));
      for (int m = 0; m < 2 * T; ++m) { __builtin_amdgcn_sched_group_barrier(0x8, 1, 0); __builtin_amdgcn_sched_group_barrier(0x100, 2, 0); __builtin_amdgcn_sched_group_barrier(0x2, 2, 0); }
    }
    __builtin_amdgcn_sched_barrier(0);
  });
  { const f32x2 a = both_halves(ca.x + ca.y); if (lane < 32 && w < T) *(LAS float *)(L + L_CSP + (w * 32 + lane) * 4) = a.x + a.y; }
  STAMP(2);
}
struct StageCtx { rsrc_t rw2, rwo; u32 vob, so2, soo, ld; };
template <int ST> DI void stage_dma(const StageCtx &c) {
  sfor<0, 4>([&](auto I) {
    constexpr u32 i = I, dst = ((ST + 4) % NSLOT) * L_SLOT + i * 1024;
    dmai<dst, (64 * ST + 8 * i) * XROW>(c.rw2, c.vob, c.so2, c.ld);
  });
}
template <int ST> DI void stage_load(const StageCtx &c, u32x4 (&br)[36]) {
  sfor<0, 4>([&](auto I) {
    constexpr u32 i = I;
    if constexpr (ST < 14) br[4 * (ST - 9) + i] = __builtin_amdgcn_raw_buffer_load_b128(c.rw2, c.vob, c.so2 + (64 * ST + 8 * i) * XROW, 0);
    else br[4 * (ST - 9) + i] = __builtin_amdgcn_raw_buffer_load_b128(c.rwo, c.vob, c.soo + (56 * (ST - 14) + (i == 3 ? 20 : 8 * i)) * XROW, 0);
  });
}
template <bool ATT>
DI void gemmA_loader(lchar *L, rsrc_t rx, rsrc_t rw, u32 sox, u32 sow, int lw, int lane, const StageCtx &sc, u32x4 (&br)[36] STAMP_ARG) {
  constexpr int NW = ATT ? 3 : 4, N = 8 + NW;
  const u32 lb = (u32)(size_t)L;
  const u32 vox0 = (lane >> 3) * XROW + 16 * ((lane & 7) ^ ((lane >> 4) & 3)), vox1 = vox0 ^ 64;
  const u32 ldx = rfl(lb + lw * 8192);
  const u32 vow = ATT ? (lane >> 2) * QKVROW + ((lane & 3) == 3 ? 40 : 16 * (lane & 3)) : (lane >> 4) * W1ROW + 16 * ((lane & 15) ^ ((lane >> 4) << 2));
  const u32 swb = rfl(sow + 16 * lw * (ATT ? QKVROW : W1ROW)), ldw = rfl(lb + A_WOFF + lw * (ATT ? 1024 : 4096));
  auto issue = [&](auto C) {
    constexpr u32 c = C, so = (c % 3) * A_SLOT;
    sfor<0, NW>([&](auto I) {
      constexpr u32 i = I;
      if constexpr (ATT) dmai<so + i * 4096, 64 * c * QKVROW + (i == 0 ? 896 : i == 1 ? 1792 : 0)>(rw, vow, swb, ldw);
      else dmai<so + i * 1024, (64 * c + 4 * i) * W1ROW>(rw, vow, swb, ldw);
    });
    sfor<0, 8>([&](auto I) { constexpr u32 i = I; dmai<so + i * 1024, 8 * i * XROW + 128 * c>(rx, (i & 1) ? vox1 : vox0, sox, ldx); });
  };
  issue(std::integral_constant<int, 0>{});
  issue(std::integral_constant<int, 1>{});
  sfor<0, 7>([&](auto C) {
    constexpr int c = C;
    vmwait<(c == 6 ? 12 : N)>();
    asm volatile("s_barrier" ::: "memory");
    if constexpr (c == 0) STAMP(1);
    if constexpr (c + 2 < 7) issue(std::integral_constant<int, c + 2>{});
    else { stage_load<9 + 3 * (c - 5)>(sc, br); stage_load<10 + 3 * (c - 5)>(sc, br); stage_load<11 + 3 * (c - 5)>(sc, br); }
  });
  STAMP(2);
}

DI void feature_map(const f32x16 &P, float rstd, int h, u32x4 (&pf)[2][2]) {
  const float c = rstd * LOG2E;
  float mx = fmaxf(fmaxf(P[0], P[1]), P[2]), mn = fminf(fminf(P[0], P[1]), P[2]);
  for (int i = 3; i < 15; i += 2) { mx = fmaxf(fmaxf(mx, P[i]), P[i + 1]); mn = fminf(fminf(mn, P[i]), P[i + 1]); }
  mx = fmaxf(mx, P[15]); mn = fminf(mn, P[15]);
  { f32x2 a = both_halves(mx), b = both_halves(mn); mx = fmaxf(a.x, a.y); mn = fminf(b.x, b.y); }
  const float mxc = -mx * c, mnc = mn * c;
  f32x16 ep, en;
  for (int i = 0; i < 16; i += 2) {
    const f32x2 p2 = {P[i], P[i + 1]}, ap = p2 * c + mxc, an = mnc - p2 * c;
    ep[i] = __builtin_amdgcn_exp2f(ap.x); ep[i + 1] = __builtin_amdgcn_exp2f(ap.y);
    en[i] = __builtin_amdgcn_exp2f(an.x); en[i + 1] = __builtin_amdgcn_exp2f(an.y);
  }
  f32x2 sp2 = {ep[0], ep[1]}, sn2 = {en[0], en[1]};
  for (int i = 2; i < 12; i += 2) { sp2 += f32x2{ep[i], ep[i + 1]}; sn2 += f32x2{en[i], en[i + 1]}; }
  const float keep = h ? 0.f : 1.f;
  sp2 += (f32x2{ep[12], ep[13]} + f32x2{ep[14], ep[15]}) * keep;
  sn2 += (f32x2{en[12], en[13]} + f32x2{en[14], en[15]}) * keep;
  const f32x2 a = both_halves(sp2.x + sp2.y), b = both_halves(sn2.x + sn2.y);
  const float rp = __builtin_amdgcn_rcpf(a.x + a.y), rn = __builtin_amdgcn_rcpf(b.x + b.y);
  for (int i = 0; i < 16; i += 2) {
    const f32x2 u = f32x2{ep[i], ep[i + 1]} * rp, v = f32x2{en[i], en[i + 1]} * rn;
    ep[i] = u.x; ep[i + 1] = u.y; en[i] = v.x; en[i + 1] = v.y;
  }
  pf[0][0] = pack8(ep, 0); pf[0][1] = pack8(ep, 1); pf[1][0] = pack8(en, 0); pf[1][1] = pack8(en, 1);
}

constexpr int refill_end(int s) { return s <= 0 ? 0 : (12 * s + 4) / 5 > 36 ? 36 : (12 * s + 4) / 5; }
constexpr bool refill_ok() { for (int s = 1; s <= 17; ++s) for (int p = refill_end(s - 1); p < refill_end(s); ++p) if (s < 1 + p / 4 || s > 7 + p / 4) return false; return refill_end(15) == 36; }
static_assert(refill_ok(), "refill schedule");
}

__global__ void __launch_bounds__(512, 2) fused_hedgehog_ffn(const char *__restrict__ gx, const char *__restrict__ gwqkv, const char *__restrict__ gwphi, const char *__restrict__ gwo,
                                                              const char *__restrict__ gw1, const char *__restrict__ gb1, const char *__restrict__ gw2, const char *__restrict__ gb2,
                                                              char *__restrict__ gout, char *__restrict__ gws) {
  __shared__ __attribute__((aligned(1024))) char smem[LDS_BYTES];
  lchar *L = (lchar *)smem;
  unsigned long long pc0;
  asm volatile("s_getpc_b64 %0" : "=s"(pc0));
  asm volatile("" ::"s"(gx), "s"(gwqkv), "s"(gwphi), "s"(gwo), "s"(gw1), "s"(gb1), "s"(gw2), "s"(gb2), "s"(gout), "s"(gws));
  const int tid = threadIdx.x, lane = tid & 63, w = rfl(tid >> 6);
  const int b = blockIdx.x, xcd = b & 7, slot = b >> 3;
  const int h = lane >> 5;
  STAMP_DECL; STAMP(0);
  const rsrc_t rx = __builtin_amdgcn_make_buffer_rsrc((void *)gx, 0, 2048 * XROW, 0x00020000);
  const rsrc_t rws = __builtin_amdgcn_make_buffer_rsrc((void *)gws, 0, WS_END, 0x00020000);
  const int tc = slot >= 16 ? slot - 16 : slot + 16;
  const bool has_tile = tc < 28;
  const u32 rt = tc / 7, cc = tc % 7;
  const bool att = slot < 16, ffn = !att && slot < 30;
  const int head = 8 * (xcd & 1) + (slot & 7);
  const int jf = slot - 16, ct = 7 * (xcd & 1) + (jf >= 7 ? jf - 7 : jf);
  const int rga = 2 * (xcd >> 1) + (att ? (slot >> 3) : (jf >= 7));

  if (w >= 4) {
    const int lw = w - 4;
    StageCtx sc;
    sc.rw2 = __builtin_amdgcn_make_buffer_rsrc((void *)gw2, 0, DF * XROW, 0x00020000);
    sc.rwo = __builtin_amdgcn_make_buffer_rsrc((void *)gwo, 0, DM * XROW, 0x00020000);
    sc.vob = (lane >> 3) * XROW + 16 * ((lane & 7) ^ (((lane >> 4) & 1) << 2));
    sc.so2 = rfl((896 * (lw >> 1) + 32 * (lw & 1)) * XROW + 128 * cc);
    sc.soo = rfl(28 * (8 * (lw >> 1) + (lw & 1)) * XROW + 128 * cc);
    sc.ld = rfl((u32)(size_t)L + (lw >> 1) * 8192 + (lw & 1) * 4096);
    u32 E;
    u32x4 br[36];
    const u32 hfoff = WS_HF + ((8 * xcd + 2 * rt) * 14 + (lane < 28 ? lane : 0)) * 64;
    if (att) {
      const rsrc_t rw = __builtin_amdgcn_make_buffer_rsrc((void *)gwqkv, 0, DM * QKVROW, 0x00020000);
      if (lw < 2) {
        const rsrc_t rp = __builtin_amdgcn_make_buffer_rsrc((void *)gwphi, 0, 16 * 28 * 28 * 2, 0x00020000);
        dma16(rp, lane * 16 + lw * 1024, head * 1568, (u32)(size_t)L + L_WPHI + lw * 1024);
      }
      gemmA_loader<true>(L, rx, rw, rfl((256 * rga + 64 * lw) * XROW), rfl(56 * head), lw, lane, sc, br STAMP_PASS);
      BAR();
      E = rfl(*(LAS u32 *)(L + L_E));
      if (has_tile) sfor<0, 5>([&](auto S) { stage_dma<S>(sc); });
      sfor<15, 18>([&](auto S) { stage_load<S>(sc, br); });
      BAR();
      __builtin_amdgcn_s_setprio(3);
      STAMP(4);
      for (int t = 0; t < 2; ++t) {
        f32x4 s = *(LAS f32x4 *)(L + (t * 4 + lw) * 1024 + lane * 16);
        for (int ww = 1; ww < 4; ++ww) s += *(LAS f32x4 *)(L + ww * 8192 + (t * 4 + lw) * 1024 + lane * 16);
        st128_sc1(__builtin_bit_cast(u32x4, s), rws, lane * 16, WS_KVP + (head * 8 + rga) * 8192 + (t * 4 + lw) * 1024);
      }
      vmwait<0>();
      if (lane == 0) st_flag(gws, WS_KF + ((head * 8 + rga) * 4 + lw) * 64, E);
      STAMP(5);
      poll_all(gws, WS_KF + ((head * 8 + (lane & 7)) * 4 + lw) * 64, E);
      STAMP(7);
      u32 hfl;
      asm volatile("global_load_dword %0, %1, off sc1" : "=v"(hfl) : "v"(gws + hfoff) : "memory");
      u32x4 part[2][8];
      for (int t = 0; t < 2; ++t) for (int r = 0; r < 8; ++r) part[t][r] = ld128_sc1(rws, lane * 16, WS_KVP + (head * 8 + r) * 8192 + (t * 4 + lw) * 1024);
      for (int t = 0; t < 2; ++t) {
        f32x4 s = __builtin_bit_cast(f32x4, part[t][0]);
        for (int r = 1; r < 8; ++r) s += __builtin_bit_cast(f32x4, part[t][r]);
        u32x2 pk = {pk2(s.x, s.y), pk2(s.z, s.w)};
        if (lw == 3 && h) pk = u32x2{0, 0};
        *(LAS u32x2 *)(L + L_KVF + ((t * 2 + (lw >> 1)) * 64 + lane) * 16 + (lw & 1) * 8) = pk;
      }
      asm volatile("" : "+v"(hfl));
      BAR();
      __builtin_amdgcn_s_setprio(0);
      STAMP(8);
      if (!has_tile) {
        if (b == 8 * 12 && lw == 0) {
#pragma unroll 1
          for (int spin = 0; spin < SPIN_CAP; ++spin) {
            bool ok = true;
            for (int i = 0; i < 32; ++i) ok &= ld_flag(gws, WS_STAT + (64 * i + lane) * 16 + 4) == E;
            if (__all(ok)) break;
            __builtin_amdgcn_s_sleep(8);
          }
          if (lane == 0) st_flag(gws, WS_EPOCH, E);
        }
        STAMP_OUT();
        return;
      }
      sfor<5, 9>([&](auto S) { stage_dma<S>(sc); });
      if (!__all(hfl == E)) poll_all(gws, hfoff, E);
    } else if (ffn) {
      const rsrc_t rw = __builtin_amdgcn_make_buffer_rsrc((void *)gw1, 0, DM * W1ROW, 0x00020000);
      gemmA_loader<false>(L, rx, rw, rfl((256 * rga + 64 * lw) * XROW), rfl(256 * ct), lw, lane, sc, br STAMP_PASS);
      BAR();
      E = rfl(*(LAS u32 *)(L + L_E));
      sfor<0, 9>([&](auto S) { stage_dma<S>(sc); });
      sfor<15, 18>([&](auto S) { stage_load<S>(sc, br); });
      poll_all(gws, hfoff, E);
    } else {
      sfor<0, 9>([&](auto S) { stage_dma<S>(sc); });
      sfor<9, 18>([&](auto S) { stage_load<S>(sc, br); });
      BAR();
      E = rfl(*(LAS u32 *)(L + L_E));
      poll_all(gws, hfoff, E);
    }
    STAMP(10);
    constexpr int SO = (56 - RING) / 4;
    u32 ofl = 0;
    const u32 ofoff = WS_OF + ((8 * xcd + 2 * rt + ((lane >> 4) & 1)) * 16 + (lane & 15)) * 64;
    const u32 ldv = sc.ld + lane * 16;
    sfor<0, 18>([&](auto S) {
      constexpr int s = S;
      if constexpr (s == SO) {
        vmwait<0>();
        asm volatile("" : "+v"(ofl));
        if (!__all(ofl == E)) poll_all(gws, ofoff, E);
      }
      if constexpr (s == 4) vmwait<0>();
      BAR();
      sfor<refill_end(s - 1), refill_end(s)>([&](auto Pc) {
        constexpr int pc = Pc;
        *(LAS u32x4 *)((lchar *)(size_t)ldv + ((9 + pc / 4 + 4) % NSLOT) * L_SLOT + (pc & 3) * 1024) = br[pc];
      });
      if constexpr (s == SO - 4) asm volatile("global_load_dword %0, %1, off sc1" : "=v"(ofl) : "v"(gws + ofoff) : "memory");
    });
    STAMP(12);
    asm volatile("s_barrier" ::: "memory");
    STAMP_OUT();
    return;
  }

  const Early early = early_issue(gws, rx, 8 * b + 2 * w, lane);
  u32 E;
  auto first = [&]() { E = early_finish(early, rws, 8 * b + 2 * w, lane); if (tid == 0) *(LAS u32 *)(L + L_E) = E; };
  const int rh = w & 1, kg = w >> 1;
  const u32 rb = 8 * xcd + 2 * rt + rh;
  const u32 row = 32 * rb + (lane & 31), col0 = 64 * cc + 32 * kg;
  u32x2 xres[4], b2v[4]; u32x4 sg0;
  auto epi_loads = [&]() {
    for (int g = 0; g < 4; ++g) {
      xres[g] = *(const GAS u32x2 *)(gx + row * XROW + (col0 + 8 * g + 4 * h) * 2);
      b2v[g] = *(const GAS u32x2 *)(gb2 + (col0 + 8 * g + 4 * h) * 2);
    }
    sg0 = ld128_sc1(rws, WS_STAT + row * 16, 0);
  };
  if (att) {
    if (tid < 16) *(LAS u32 *)(L + L_ZERO + 4 * tid) = 0;
    u32x4 idf[2];
    for (int s = 0; s < 2; ++s)
      for (int d = 0; d < 4; ++d) {
        const int k0 = 16 * s + 8 * (d >> 1) + 4 * h + 2 * (d & 1), n = lane & 31;
        idf[s][d] = (n == k0 ? 0x3F80u : 0u) | (n == k0 + 1 ? 0x3F800000u : 0u);
      }
    for (int s = 0; s < 2; ++s) asm volatile("" : "+v"(idf[s]));
    f32x16 acc[2][3]; u32x4 stat[2];
    const u32 vostat = WS_STAT + (256 * rga + 64 * w + (lane & 31)) * 16;
    gemmA_compute<true>(L, w, lane, acc, first, [&]() { for (int r = 0; r < 2; ++r) stat[r] = ld128_sc1(rws, vostat + 512 * r, 0); } STAMP_PASS);
    RowStat st[2];
    for (int r = 0; r < 2; ++r) {
      st[r] = row_stats(stat_settle(stat[r], rws, vostat + 512 * r, E), h);
      *(LAS float *)(L + L_TOK + w * 256 + r * 128 + 4 * (lane & 31)) = st[r].rstd;
    }
    BAR();
    STAMP(3);
    {
      const u32x4 wk = waug(cs_read(L, 0, lane), 0), wv = waug(cs_read(L, 1, lane), 0), wq = waug(cs_read(L, 2, lane), 0);
      for (int r = 0; r < 2; ++r) { acc[r][0] = mfma(wk, st[r].xaug, acc[r][0]); acc[r][1] = mfma(st[r].xaug, wv, acc[r][1]); acc[r][2] = mfma(wq, st[r].xaug, acc[r][2]); }
    }
    u32x4 wp[2];
    {
      const int blk = (lane >> 4) & 1, q = (lane & 15) >> 2, p = lane & 3;
      const u32 col = (blk && p == 3) ? 24 : 16 * blk + 4 * p;
      for (int s = 0; s < 2; ++s) {
        u32x2 r[2];
        for (int t = 0; t < 2; ++t) {
          const int dp = 16 * s + 8 * t + 4 * h + q;
          const u32 a = (dp >= 24 && dp < 28) ? L_ZERO + 8 * p : L_WPHI + (dp >= 28 ? dp - 4 : dp) * 56 + col * 2;
          r[t] = trd(L + a);
        }
        wp[s] = u32x4{r[0].x, r[0].y, r[1].x, r[1].y};
      }
    }
    f32x16 kvp[2] = {zero16(), zero16()};
    {
      f32x16 P[2];
      for (int r = 0; r < 2; ++r) P[r] = mfma(wp[0], pack8(acc[r][0], 0), zero16());
      for (int r = 0; r < 2; ++r) P[r] = mfma(wp[1], pack8(acc[r][0], 1), P[r]);
      u32x4 vf[2][2];
      for (int r = 0; r < 2; ++r) {
        f32x16 v = acc[r][1];
        for (int g = 0; g < 4; ++g) {
          const f32x4 rs = *(LAS f32x4 *)(L + L_TOK + w * 256 + r * 128 + (8 * g + 4 * h) * 4);
          for (int e = 0; e < 4; e += 2) { const f32x2 t2 = f32x2{v[4 * g + e], v[4 * g + e + 1]} * f32x2{rs[e], rs[e + 1]}; v[4 * g + e] = t2.x; v[4 * g + e + 1] = t2.y; }
        }
        for (int s = 0; s < 2; ++s) { vf[r][s] = pack8(v, s); if ((lane & 31) == 24) vf[r][s] = u32x4{0x3F803F80u, 0x3F803F80u, 0x3F803F80u, 0x3F803F80u}; }
      }
      u32x4 pf[2][2][2];
      for (int r = 0; r < 2; ++r) feature_map(P[r], st[r].rstd, h, pf[r]);
      f32x16 Z[2][2];
      for (int r = 0; r < 2; ++r) for (int t = 0; t < 2; ++t) Z[r][t] = mfma(pf[r][t][0], idf[0], zero16());
      for (int r = 0; r < 2; ++r) for (int t = 0; t < 2; ++t) Z[r][t] = mfma(pf[r][t][1], idf[1], Z[r][t]);
      for (int r = 0; r < 2; ++r) for (int s = 0; s < 2; ++s) for (int t = 0; t < 2; ++t) kvp[t] = mfma(pack8(Z[r][t], s), vf[r][s], kvp[t]);
    }
    for (int t = 0; t < 2; ++t)
      for (int g = 0; g < 4; ++g)
        *(LAS f32x4 *)(L + w * 8192 + (t * 4 + g) * 1024 + lane * 16) = f32x4{kvp[t][4 * g], kvp[t][4 * g + 1], kvp[t][4 * g + 2], kvp[t][4 * g + 3]};
    STAMP(4);
    BAR();
    u32x4 pq[2][2][2];
    {
      f32x16 P[2];
      for (int r = 0; r < 2; ++r) P[r] = mfma(wp[0], pack8(acc[r][2], 0), zero16());
      for (int r = 0; r < 2; ++r) P[r] = mfma(wp[1], pack8(acc[r][2], 1), P[r]);
      for (int r = 0; r < 2; ++r) {
        feature_map(P[r], st[r].rstd, h, pq[r]);
        for (int t = 0; t < 2; ++t) for (int s = 0; s < 2; ++s) asm volatile("" : "+v"(pq[r][t][s]));
      }
    }
    if (has_tile) epi_loads();
    else { for (int g = 0; g < 4; ++g) { xres[g] = u32x2{0, 0}; b2v[g] = u32x2{0, 0}; } sg0 = u32x4{0, 0, 0, 0}; }
    STAMP(6);
    BAR();
    STAMP(8);
    {
      u32x4 kf[2][2];
      for (int t = 0; t < 2; ++t) for (int s = 0; s < 2; ++s) kf[t][s] = *(LAS u32x4 *)(L + L_KVF + ((t * 2 + s) * 64 + lane) * 16);
      for (int r = 0; r < 2; ++r) {
        f32x16 num = zero16();
        for (int t = 0; t < 2; ++t) for (int s = 0; s < 2; ++s) num = mfma(kf[t][s], pq[r][t][s], num);
        const float rc = __builtin_amdgcn_rcpf(both_halves(num[12]).x + 1e-6f);
        for (int i = 0; i < 16; ++i) num[i] *= rc;
        if (!h) { num[12] = 0.f; num[13] = 0.f; num[14] = 0.f; num[15] = 0.f; }
        const u32 so = WS_O + ((8 * rga + 2 * w + r) * 32 + 2 * head) * 1024;
        st128_sc1(pack8(num, 0), rws, lane * 16, so);
        st128_sc1(pack8(num, 1), rws, lane * 16, so + 1024);
      }
    }
    vmwait<0>();
    if (lane < 2) st_flag(gws, WS_OF + ((8 * rga + 2 * w + lane) * 16 + head) * 64, E);
    STAMP(9);
    if (!has_tile) { STAMP_OUT(); return; }
  } else if (ffn) {
    u32 bias[4];
    for (int t = 0; t < 4; ++t) bias[t] = *(const GAS unsigned short *)(gb1 + (128 * ct + 32 * t + (lane & 31)) * 2);
    f32x16 acc[2][4]; u32x4 stat[2];
    const u32 vostat = WS_STAT + (256 * rga + 64 * w + (lane & 31)) * 16;
    gemmA_compute<false>(L, w, lane, acc, first, [&]() { for (int r = 0; r < 2; ++r) stat[r] = ld128_sc1(rws, vostat + 512 * r, 0); } STAMP_PASS);
    RowStat st[2];
    for (int r = 0; r < 2; ++r) st[r] = row_stats(stat_settle(stat[r], rws, vostat + 512 * r, E), h);
    BAR();
    STAMP(3);
    epi_loads();
    for (int t = 0; t < 4; ++t) {
      const u32x4 wa = waug(cs_read(L, t, lane), bias[t] | (bias[t] << 16));
      for (int r = 0; r < 2; ++r) {
        f32x16 a = mfma(wa, st[r].xaug, acc[r][t]);
        for (int i = 0; i < 16; i += 2) { f32x2 v = {relu(a[i]), relu(a[i + 1])}; v *= v; a[i] = v.x; a[i + 1] = v.y; }
        const u32 so = WS_H + ((8 * rga + 2 * w + r) * 112 + 8 * ct + 2 * t) * 1024;
        st128_sc1(pack8(a, 0), rws, lane * 16, so);
        st128_sc1(pack8(a, 1), rws, lane * 16, so + 1024);
      }
    }
    STAMP(4);
    vmwait<0>();
    if (lane < 2) st_flag(gws, WS_HF + ((8 * rga + 2 * w + lane) * 14 + ct) * 64, E);
    STAMP(5);
  } else {
    if (CODE_PF) {
      const rsrc_t rc = __builtin_amdgcn_make_buffer_rsrc((void *)(pc0 & ~127ull), 0, CODE_PF, 0x00020000);
      asm volatile("s_mov_b32 m0, %0\n\ts_nop 0\n\tbuffer_load_dword %1, %2, %3 offen lds" ::"s"((u32)(size_t)L + L_DUMMY), "v"(lane * 128), "s"(rc), "s"(rfl(((slot - 30) * 4 + w) * 8192)) : "memory", "m0");
    }
    first();
    BAR();
    epi_loads();
  }

  {
    constexpr int RD = RING;
    const u32 soh = WS_H + (rb * 112 + 56 * kg) * 1024, soo = WS_O + (rb * 32 + 16 * kg) * 1024;
    u32x4 ring[RD];
    const float sig = asf(stat_settle(sg0, rws, WS_STAT + row * 16, E)[2]);
    const float rs1 = __builtin_amdgcn_rcpf(sig), rs2 = rs1 * rs1;
    f32x2 bx[8], xx[8];
    for (int g = 0; g < 4; ++g) for (int e = 0; e < 2; ++e) {
      bx[2 * g + e] = f32x2{bf2f(b2v[g][e]), bfhi(b2v[g][e])}; xx[2 * g + e] = f32x2{bf2f(xres[g][e]), bfhi(xres[g][e])};
      asm volatile("" : "+v"(bx[2 * g + e]), "+v"(xx[2 * g + e]));
    }
    const int blk = (lane >> 4) & 1, q = (lane & 15) >> 2, p = lane & 3;
    u32 bb0[2], bb[2];
    for (int c2 = 0; c2 < 2; ++c2) bb0[c2] = (u32)(size_t)L + kg * 8192 + (4 * h + q) * 128 + ((c2 ^ kg ^ (q >> 1)) * 64) + 32 * blk + 8 * p;
    f32x16 af[2] = {zero16(), zero16()}, aa[2] = {zero16(), zero16()};
    u32x4 bf[2][2];
    sfor<0, 72>([&](auto I) {
      constexpr int i = I;
      if constexpr ((i & 3) == 0) asm volatile("s_barrier" ::: "memory");
      if constexpr (i == 0) STAMP(11);
      if constexpr (i == 40) STAMP(14);
      if constexpr (i == 0) {
        for (int j = 0; j < RD; ++j) ring[j] = ld128_sc1(rws, lane * 16, soh + j * 1024);
        __builtin_amdgcn_sched_barrier(0);
        for (int c2 = 0; c2 < 2; ++c2) { bb[c2] = bb0[c2] + 4 * L_SLOT; asm volatile("" : "+v"(bb[c2])); bf[0][c2] = trfrag((lchar *)(size_t)bb[c2], 1024); }
      }
      u32x4 a = ring[i % RD];
      if constexpr (i + 1 < 72) {
        constexpr int n = i + 1;
        if constexpr ((n & 3) == 0) for (int c2 = 0; c2 < 2; ++c2) { bb[c2] = bb0[c2] + (((n >> 2) + 4) % NSLOT) * L_SLOT; asm volatile("" : "+v"(bb[c2])); }
        for (int c2 = 0; c2 < 2; ++c2) bf[n & 1][c2] = trfrag((lchar *)(size_t)bb[c2] + (n & 3) * 2048, 1024);
      }
      if constexpr (i < 56) { af[0] = mfma(bf[i & 1][0], a, af[0]); af[1] = mfma(bf[i & 1][1], a, af[1]); asm volatile("" : "+v"(af[0]), "+v"(af[1])); }
      else { aa[0] = mfma(bf[i & 1][0], a, aa[0]); aa[1] = mfma(bf[i & 1][1], a, aa[1]); asm volatile("" : "+v"(aa[0]), "+v"(aa[1])); }
      if constexpr (i + RD < 56) ring[i % RD] = ld128_sc1(rws, lane * 16, soh + (i + RD) * 1024);
      else if constexpr (i + RD < 72) ring[i % RD] = ld128_sc1(rws, lane * 16, soo + (i + RD - 56) * 1024);
      __builtin_amdgcn_sched_barrier(0);
    });
    STAMP(12);
    lchar *X = L + 4 * L_SLOT;
    {
      const f32x16 &sf = af[1], &sa = aa[1];
      for (int g = 0; g < 4; ++g) {
        *(LAS f32x4 *)(X + w * 8192 + g * 1024 + lane * 16) = f32x4{sf[4 * g], sf[4 * g + 1], sf[4 * g + 2], sf[4 * g + 3]};
        *(LAS f32x4 *)(X + w * 8192 + 4096 + g * 1024 + lane * 16) = f32x4{sa[4 * g], sa[4 * g + 1], sa[4 * g + 2], sa[4 * g + 3]};
      }
    }
    BAR();
    const f32x16 &yf = af[0], &ya = aa[0];
    u32x2 o2[4];
    for (int g = 0; g < 4; ++g) {
      const f32x4 pf = *(LAS f32x4 *)(X + (w ^ 2) * 8192 + g * 1024 + lane * 16), pa = *(LAS f32x4 *)(X + (w ^ 2) * 8192 + 4096 + g * 1024 + lane * 16);
      for (int e = 0; e < 2; ++e) {
        const int i = 4 * g + 2 * e;
        const f32x2 y = (f32x2{yf[i], yf[i + 1]} + f32x2{pf[2 * e], pf[2 * e + 1]}) * rs2 + bx[2 * g + e];
        const f32x2 t = y * -LOG2E;
        const f32x2 d = f32x2{__builtin_amdgcn_exp2f(t.x), __builtin_amdgcn_exp2f(t.y)} + 1.0f;
        const f32x2 sg = y * f32x2{__builtin_amdgcn_rcpf(d.x), __builtin_amdgcn_rcpf(d.y)};
        const f32x2 r = (f32x2{ya[i], ya[i + 1]} + f32x2{pa[2 * e], pa[2 * e + 1]}) * sg + xx[2 * g + e];
        o2[g][e] = pk2(r.x, r.y);
      }
    }
    const rsrc_t rout = __builtin_amdgcn_make_buffer_rsrc((void *)gout, 0, 2048 * XROW, 0x00020000);
    for (int g = 0; g < 4; g += 2) {
      auto rx0 = __builtin_amdgcn_permlane32_swap(o2[g].x, o2[g + 1].x, false, false);
      auto ry0 = __builtin_amdgcn_permlane32_swap(o2[g].y, o2[g + 1].y, false, false);
      __builtin_amdgcn_raw_buffer_store_b128(u32x4{rx0[0], ry0[0], rx0[1], ry0[1]}, rout, row * XROW + (col0 + 8 * (g + h)) * 2, 0, OUT_AUX);
    }
    STAMP(13);
    STAMP(15);
    STAMP_OUT();
  }
}

extern "C" void kernel_launch(void *const *d_in, const int *in_sizes, int n_in, void *d_out, int out_size, void *d_ws, size_t ws_size, hipStream_t stream) {
  fused_hedgehog_ffn<<<256, 512, 0, stream>>>((const char *)d_in[0], (const char *)d_in[1], (const char *)d_in[2], (const char *)d_in[3], (const char *)d_in[5],
                                              (const char *)d_in[6], (const char *)d_in[7], (const char *)d_in[8], (char *)d_out, (char *)d_ws);
}
```

```cpp
#include <hip/hip_runtime.h>
#include <hip/hip_bf16.h>
#include <stdint.h>
#include <type_traits>

typedef unsigned u32;
typedef u32 u32x4 __attribute__((ext_vector_type(4)));
typedef u32 u32x2 __attribute__((ext_vector_type(2)));
typedef float f32x16 __attribute__((ext_vector_type(16)));
typedef float f32x4 __attribute__((ext_vector_type(4)));
typedef float f32x2 __attribute__((ext_vector_type(2)));
typedef short s16x8 __attribute__((ext_vector_type(8)));
typedef short s16x4 __attribute__((ext_vector_type(4)));
typedef __bf16 bf16x2 __attribute__((ext_vector_type(2)));
typedef __bf16 bf16x8 __attribute__((ext_vector_type(8)));
#define LAS __attribute__((address_space(3)))
#define GAS __attribute__((address_space(1)))
#define DI __device__ __forceinline__
typedef __amdgpu_buffer_rsrc_t rsrc_t;
typedef LAS char lchar;

namespace {
constexpr int DM = 448, DF = 1792;
constexpr u32 XROW = DM * 2;
constexpr u32 QKVROW = 3 * DM * 2;
constexpr u32 W1ROW = DF * 2;
constexpr u32 WS_EPOCH = 0;
constexpr u32 WS_STAT = 4096;
constexpr u32 WS_KF = 65536;
constexpr u32 WS_HF = 131072;
constexpr u32 WS_OF = 196608;
constexpr u32 WS_KVP = 262144;
constexpr u32 WS_H = WS_KVP + (1u << 20);
constexpr u32 WS_O = WS_H + 64 * 112 * 1024;
constexpr u32 WS_END = WS_O + 64 * 32 * 1024;
constexpr u32 L_SLOT = 16384, NSLOT = 9;
constexpr u32 A_SLOT = 49152, A_WOFF = 32768;
constexpr u32 L_MISC = NSLOT * L_SLOT;
constexpr u32 L_WPHI = L_MISC;
constexpr u32 L_ZERO = L_MISC + 2048;
constexpr u32 L_E = L_MISC + 2112;
constexpr u32 L_DUMMY = L_MISC + 2304;
constexpr u32 L_CSP = L_MISC + 2560;
constexpr u32 L_TOK = L_CSP + 512;
constexpr u32 L_KVF = L_TOK + 1024;
constexpr u32 L_END = L_KVF + 4096;
constexpr u32 LDS_BYTES = 163840;
static_assert(L_END <= LDS_BYTES, "lds");
constexpr int SPIN_CAP = 1 << 16;
constexpr float LOG2E = 1.4426950408889634f;
#define CS_DOT2 0
#define SGB 1
#define LPRIO 0
#define FIRST_AT -1
#define TOUCH_A 1
#define OUT_AUX 0
#define SPREAD 0
#define PIN_C 0
#define RING 12

#define STAMP(i) do {} while (0)
#define STAMP_DECL do {} while (0)
#define STAMP_OUT() do {} while (0)
#define STAMP_ARG
#define STAMP_PASS
template <int B, int E, class F> DI void sfor(F &&f) {
  if constexpr (B < E) { f(std::integral_constant<int, B>{}); sfor<B + 1, E>(f); }
}
DI u32 rfl(u32 v) { return __builtin_amdgcn_readfirstlane(v); }
DI f32x16 mfma(u32x4 a, u32x4 b, f32x16 c) {
  return __builtin_amdgcn_mfma_f32_32x32x16_bf16(__builtin_bit_cast(bf16x8, a), __builtin_bit_cast(bf16x8, b), c, 0, 0, 0);
}
DI u32 pk2(float lo, float hi) { f32x2 p = {lo, hi}; return __builtin_bit_cast(u32, __builtin_convertvector(p, bf16x2)); }
DI float dot2(u32 a, u32 b, float c) { return __builtin_amdgcn_fdot2_f32_bf16(__builtin_bit_cast(bf16x2, a), __builtin_bit_cast(bf16x2, b), c, false); }
DI float bf2f(u32 lo16) { return __builtin_bit_cast(float, lo16 << 16); }
DI float bfhi(u32 v) { return __builtin_bit_cast(float, v & 0xffff0000u); }
DI float asf(u32 v) { return __builtin_bit_cast(float, v); }
DI f32x2 both_halves(float v) {
  const u32 a = __builtin_bit_cast(u32, v);
  auto r = __builtin_amdgcn_permlane32_swap(a, a, false, false);
  return f32x2{asf(r[0]), asf(r[1])};
}
DI float half_lo(float v) { return both_halves(v).x; }
DI u32x4 pack8(const f32x16 &x, int s) { return u32x4{pk2(x[8 * s], x[8 * s + 1]), pk2(x[8 * s + 2], x[8 * s + 3]), pk2(x[8 * s + 4], x[8 * s + 5]), pk2(x[8 * s + 6], x[8 * s + 7])}; }
DI u32x2 trd(lchar *p) {
  typedef short v4i16 __attribute__((ext_vector_type(4)));
  return __builtin_bit_cast(u32x2, __builtin_amdgcn_ds_read_tr16_b64_v4i16((LAS v4i16 *)p));
}
DI u32x4 trfrag(lchar *p, int tstride) { u32x2 a = trd(p), b = trd(p + tstride); return u32x4{a.x, a.y, b.x, b.y}; }
DI float relu(float v) { return __builtin_amdgcn_fmed3f(v, 0.f, __builtin_inff()); }
DI f32x16 zero16() { f32x16 z; for (int i = 0; i < 16; ++i) z[i] = 0.f; return z; }
#define BAR() asm volatile("s_waitcnt lgkmcnt(0)\n\ts_barrier" ::: "memory")
template <int N> DI void vmwait() { asm volatile("s_waitcnt vmcnt(%0)" ::"i"(N) : "memory"); }
DI void dma16(rsrc_t r, u32 voff, u32 soff, u32 ldsaddr) {
  asm volatile("s_mov_b32 m0, %0\n\ts_nop 0\n\tbuffer_load_dwordx4 %1, %2, %3 offen lds" ::"s"(ldsaddr), "v"(voff), "s"(r), "s"(soff) : "memory", "m0");
}
template <int OFF> DI void ald128(u32x4 &d, rsrc_t r, u32 voff, u32 soff) {
  asm volatile("buffer_load_dwordx4 %0, %1, %2, %3 offen offset:%4" : "=v"(d) : "v"(voff), "s"(r), "s"(soff), "i"(OFF) : "memory");
}
DI u32x4 ld128_sc1(rsrc_t r, u32 voff, u32 soff) { return __builtin_amdgcn_raw_buffer_load_b128(r, voff, soff, 16); }
DI void st128_sc1(u32x4 v, rsrc_t r, u32 voff, u32 soff) {
  __builtin_amdgcn_raw_buffer_store_b128(v, r, voff, soff, 16);
  asm volatile("s_nop 1" ::"v"(v) : "memory");
}
typedef GAS u32 gu32;
DI u32 ld_flag(const char *ws, u32 off) { return __hip_atomic_load((gu32 *)(ws + off), __ATOMIC_RELAXED, __HIP_MEMORY_SCOPE_AGENT); }
DI void st_flag(char *ws, u32 off, u32 v) { __hip_atomic_store((gu32 *)(ws + off), v, __ATOMIC_RELAXED, __HIP_MEMORY_SCOPE_AGENT); }
DI void poll_all(const char *ws, u32 off, u32 E) {
#pragma unroll 1
  for (int spin = 0; spin < SPIN_CAP; ++spin) {
    if (__all(ld_flag(ws, off) == E)) break;
    __builtin_amdgcn_s_sleep(1);
  }
  __builtin_amdgcn_fence(__ATOMIC_ACQUIRE, "wavefront");
}

template <u32 LI, u32 SI> DI void dmai(rsrc_t r, u32 voff, u32 sob, u32 ldsb) {
  u32 t;
  asm volatile("s_add_u32 m0, %2, %5\n\ts_add_u32 %0, %1, %6\n\tbuffer_load_dwordx4 %3, %4, %0 offen lds"
               : "=&s"(t) : "s"(sob), "s"(ldsb), "v"(voff), "s"(r), "i"(LI), "i"(SI) : "memory", "m0", "scc");
}
template <int CTRL, int RMASK> DI float dpp_add(float v) {
  return v + asf(__builtin_amdgcn_update_dpp(0u, __builtin_bit_cast(u32, v), CTRL, RMASK, 0xf, true));
}
DI float wave_sum63(float v) {
  v = dpp_add<0x111, 0xf>(v); v = dpp_add<0x112, 0xf>(v); v = dpp_add<0x114, 0xf>(v); v = dpp_add<0x118, 0xf>(v);
  v = dpp_add<0x142, 0xa>(v); v = dpp_add<0x143, 0xc>(v);
  return v;
}
DI void st64_sc1(char *ws, u32 off, u32 lo, u32 hi) {
  __hip_atomic_store((GAS unsigned long long *)(ws + off), (unsigned long long)lo | ((unsigned long long)hi << 32), __ATOMIC_RELAXED, __HIP_MEMORY_SCOPE_AGENT);
}
struct Early { u32 ev; u32x4 xa, xb; };
DI Early early_issue(char *ws, rsrc_t rx, int row0, int lane) {
  Early e;
  e.ev = ld_flag(ws, WS_EPOCH);
  const u32 vo = lane < 56 ? (u32)lane * 16 : 0x7ffffff0u;
  e.xa = __builtin_amdgcn_raw_buffer_load_b128(rx, vo, row0 * XROW, 0); e.xb = __builtin_amdgcn_raw_buffer_load_b128(rx, vo, (row0 + 1) * XROW, 0);
  return e;
}
DI u32 early_finish(const Early &e, rsrc_t rws, int row0, int lane) {
  const u32 E = rfl(e.ev) + 1, ONES = 0x3F803F80u;
  float s[2], q[2];
  for (int r = 0; r < 2; ++r) {
    const u32x4 &x = r ? e.xb : e.xa;
    s[r] = dot2(x[0], ONES, 0.f); q[r] = dot2(x[0], x[0], 0.f);
    for (int d = 1; d < 4; ++d) { s[r] = dot2(x[d], ONES, s[r]); q[r] = dot2(x[d], x[d], q[r]); }
    s[r] = wave_sum63(s[r]); q[r] = wave_sum63(q[r]);
    const float mean = s[r] * (1.0f / DM);
    const float sig = __builtin_amdgcn_sqrtf(fmaxf(q[r] * (1.0f / DM) - mean * mean, 0.f) + 1e-5f);
    const u32 vo = lane == 63 ? WS_STAT + (row0 + r) * 16 : 0x7ffffff0u;
    __builtin_amdgcn_raw_buffer_store_b64(u32x2{__builtin_bit_cast(u32, mean), E}, rws, vo, 0, 16);
    __builtin_amdgcn_raw_buffer_store_b64(u32x2{__builtin_bit_cast(u32, sig), E}, rws, vo + 8, 0, 16);
  }
  return E;
}
DI u32x4 stat_settle(u32x4 g, rsrc_t rws, u32 vostat, u32 E) {
#pragma unroll 1
  for (int spin = 0; spin < SPIN_CAP && !__all(g[1] == E && g[3] == E); ++spin) { __builtin_amdgcn_s_sleep(1); g = ld128_sc1(rws, vostat, 0); }
  return g;
}
struct RowStat { float rstd; u32x4 xaug; };
DI RowStat row_stats(u32x4 g, int h) {
  const float m = -asf(g[0]), sig = asf(g[2]);
  RowStat r; r.rstd = __builtin_amdgcn_rcpf(sig);
  const float ml = m - bfhi(pk2(m, m)), sl = sig - bfhi(pk2(sig, sig));
  const u32 mm = pk2(m, ml), ss = pk2(sig, sl);
  r.xaug = h ? u32x4{0, 0, 0, 0} : u32x4{mm, mm, ss, 0};
  return r;
}
DI u32x4 waug(float cs, u32 bb) {
  const u32 hh = pk2(cs, cs); const float cl = cs - bfhi(hh);
  return u32x4{hh, pk2(cl, cl), bb, 0};
}
DI float cs_read(lchar *L, int t, int lane) { return *(LAS float *)(L + L_CSP + (t * 32 + (lane & 31)) * 4); }

DI void touch(rsrc_t r, u32 voff, u32 lb) {
  asm volatile("s_mov_b32 m0, %0\n\ts_nop 0\n\tbuffer_load_dword %1, %2, 0 offen lds" ::"s"(lb + L_DUMMY), "v"(voff), "s"(r) : "memory", "m0");
}
template <bool ATT, class First, class Mid>
DI void gemmA_compute(lchar *L, int w, int lane, f32x16 (&acc)[2][ATT ? 3 : 4], First &&first, Mid &&mid STAMP_ARG) {
  constexpr int T = ATT ? 3 : 4, ROWB = ATT ? 64 : 256;
  const int h = lane >> 5, blk = (lane >> 4) & 1, q = (lane & 15) >> 2, p = lane & 3;
  const u32 lb = (u32)(size_t)L;
  u32 wb0[T], xb0[4], wb[T], xb[4], cb0, cb;
  for (int t = 0; t < T; ++t) wb0[t] = lb + A_WOFF + (ATT ? t * 4096 + (8 * h + q) * 64 : (8 * h + q) * 256 + 64 * (t ^ q)) + 32 * blk + 8 * p;
  for (int u = 0; u < 4; ++u) xb0[u] = lb + w * 8192 + (lane & 31) * 128 + 16 * ((2 * u + h) ^ ((lane >> 1) & 7));
  const int tw = w < T ? w : T - 1;
  cb0 = lb + A_WOFF + (ATT ? tw * 4096 + (8 * h + q) * 64 : (8 * h + q) * 256 + 64 * (tw ^ q)) + 32 * blk + 8 * p;
  f32x2 ca = {0.f, 0.f};
  for (int r = 0; r < 2; ++r) for (int t = 0; t < T; ++t) acc[r][t] = zero16();
  u32x4 xf[2][2], wf[2][T], cf[2];
  sfor<0, 29>([&](auto I) {
    constexpr int i = I, c = i >> 2, u = i & 3;
    if constexpr (i == 0 && FIRST_AT < 0) { first(); STAMP(14); }
    if constexpr (u == 0 && i < 28) {
      BAR();
      if constexpr (c == 0) STAMP(1);
      constexpr u32 so = (c % 3) * A_SLOT;
      for (int t = 0; t < T; ++t) { wb[t] = wb0[t] + so; asm volatile("" : "+v"(wb[t])); }
      for (int j = 0; j < 4; ++j) { xb[j] = xb0[j] + so; asm volatile("" : "+v"(xb[j])); }
      cb = cb0 + so; asm volatile("" : "+v"(cb));
    }
    if constexpr (i < 28) {
      cf[i & 1] = trfrag((lchar *)(size_t)cb + 16 * u * ROWB, 4 * ROWB);
      for (int r = 0; r < 2; ++r) xf[i & 1][r] = *(LAS u32x4 *)((lchar *)(size_t)xb[u] + r * 4096);
      for (int t = 0; t < T; ++t) wf[i & 1][t] = trfrag((lchar *)(size_t)wb[t] + 16 * u * ROWB, 4 * ROWB);
    }
    if constexpr (i == FIRST_AT) first();
    if constexpr (i == 20) mid();
    if constexpr (i > 0) {
      constexpr int j = (i - 1) & 1;
      for (int t = 0; t < T; ++t)
        for (int r = 0; r < 2; ++r) acc[r][t] = (ATT && t == 1) ? mfma(xf[j][r], wf[j][t], acc[r][t]) : mfma(wf[j][t], xf[j][r], acc[r][t]);
      for (int d = 0; d < 4; ++d) ca += f32x2{bf2f(cf[j][d]), bfhi(cf[j][d])};
      asm volatile("" : "+v"(ca));
      for (int m = 0; m < 2 * T; ++m) { __builtin_amdgcn_sched_group_barrier(0x8, 1, 0); __builtin_amdgcn_sched_group_barrier(0x100, 2, 0); __builtin_amdgcn_sched_group_barrier(0x2, 2, 0); }
    }
    __builtin_amdgcn_sched_barrier(0);
  });
  { const f32x2 a = both_halves(ca.x + ca.y); if (lane < 32 && w < T) *(LAS float *)(L + L_CSP + (w * 32 + lane) * 4) = a.x + a.y; }
  STAMP(2);
}
struct StageCtx { rsrc_t rw2, rwo; u32 vob, so2, soo, ld; };
template <int ST> DI void stage_dma(const StageCtx &c) {
  sfor<0, 4>([&](auto I) {
    constexpr u32 i = I, dst = ((ST + 4) % NSLOT) * L_SLOT + i * 1024;
    dmai<dst, (64 * ST + 8 * i) * XROW>(c.rw2, c.vob, c.so2, c.ld);
  });
}
template <int ST> DI void stage_load(const StageCtx &c, u32x4 (&br)[36]) {
  sfor<0, 4>([&](auto I) {
    constexpr u32 i = I;
    if constexpr (ST < 14) br[4 * (ST - 9) + i] = __builtin_amdgcn_raw_buffer_load_b128(c.rw2, c.vob, c.so2 + (64 * ST + 8 * i) * XROW, 0);
    else br[4 * (ST - 9) + i] = __builtin_amdgcn_raw_buffer_load_b128(c.rwo, c.vob, c.soo + (56 * (ST - 14) + (i == 3 ? 20 : 8 * i)) * XROW, 0);
  });
}
template <bool ATT>
DI void gemmA_loader(lchar *L, rsrc_t rx, rsrc_t rw, u32 sox, u32 sow, int lw, int lane, const StageCtx &sc, u32x4 (&br)[36] STAMP_ARG) {
  constexpr int NW = ATT ? 3 : 4, N = 8 + NW;
  const u32 lb = (u32)(size_t)L;
  const u32 vox0 = (lane >> 3) * XROW + 16 * ((lane & 7) ^ ((lane >> 4) & 3)), vox1 = vox0 ^ 64;
  const u32 ldx = rfl(lb + lw * 8192);
  const u32 vow = ATT ? (lane >> 2) * QKVROW + ((lane & 3) == 3 ? 40 : 16 * (lane & 3)) : (lane >> 4) * W1ROW + 16 * ((lane & 15) ^ ((lane >> 4) << 2));
  const u32 swb = rfl(sow + 16 * lw * (ATT ? QKVROW : W1ROW)), ldw = rfl(lb + A_WOFF + lw * (ATT ? 1024 : 4096));
  auto issue = [&](auto C) {
    constexpr u32 c = C, so = (c % 3) * A_SLOT;
    sfor<0, NW>([&](auto I) {
      constexpr u32 i = I;
      if constexpr (ATT) dmai<so + i * 4096, 64 * c * QKVROW + (i == 0 ? 896 : i == 1 ? 1792 : 0)>(rw, vow, swb, ldw);
      else dmai<so + i * 1024, (64 * c + 4 * i) * W1ROW>(rw, vow, swb, ldw);
    });
    sfor<0, 8>([&](auto I) { constexpr u32 i = I; dmai<so + i * 1024, 8 * i * XROW + 128 * c>(rx, (i & 1) ? vox1 : vox0, sox, ldx); });
  };
  issue(std::integral_constant<int, 0>{});
  issue(std::integral_constant<int, 1>{});
  sfor<0, 7>([&](auto C) {
    constexpr int c = C;
    vmwait<(c == 6 ? 12 : N)>();
    asm volatile("s_barrier" ::: "memory");
    if constexpr (c == 0) STAMP(1);
    if constexpr (c + 2 < 7) issue(std::integral_constant<int, c + 2>{});
    else { stage_load<9 + 3 * (c - 5)>(sc, br); stage_load<10 + 3 * (c - 5)>(sc, br); stage_load<11 + 3 * (c - 5)>(sc, br); }
  });
  STAMP(2);
}

DI void feature_map(const f32x16 &P, float rstd, int h, u32x4 (&pf)[2][2]) {
  const float c = rstd * LOG2E;
  float mx = fmaxf(fmaxf(P[0], P[1]), P[2]), mn = fminf(fminf(P[0], P[1]), P[2]);
  for (int i = 3; i < 15; i += 2) { mx = fmaxf(fmaxf(mx, P[i]), P[i + 1]); mn = fminf(fminf(mn, P[i]), P[i + 1]); }
  mx = fmaxf(mx, P[15]); mn = fminf(mn, P[15]);
  { f32x2 a = both_halves(mx), b = both_halves(mn); mx = fmaxf(a.x, a.y); mn = fminf(b.x, b.y); }
  const float mxc = -mx * c, mnc = mn * c;
  f32x16 ep, en;
  for (int i = 0; i < 16; i += 2) {
    const f32x2 p2 = {P[i], P[i + 1]}, ap = p2 * c + mxc, an = mnc - p2 * c;
    ep[i] = __builtin_amdgcn_exp2f(ap.x); ep[i + 1] = __builtin_amdgcn_exp2f(ap.y);
    en[i] = __builtin_amdgcn_exp2f(an.x); en[i + 1] = __builtin_amdgcn_exp2f(an.y);
  }
  f32x2 sp2 = {ep[0], ep[1]}, sn2 = {en[0], en[1]};
  for (int i = 2; i < 12; i += 2) { sp2 += f32x2{ep[i], ep[i + 1]}; sn2 += f32x2{en[i], en[i + 1]}; }
  const float keep = h ? 0.f : 1.f;
  sp2 += (f32x2{ep[12], ep[13]} + f32x2{ep[14], ep[15]}) * keep;
  sn2 += (f32x2{en[12], en[13]} + f32x2{en[14], en[15]}) * keep;
  const f32x2 a = both_halves(sp2.x + sp2.y), b = both_halves(sn2.x + sn2.y);
  const float rp = __builtin_amdgcn_rcpf(a.x + a.y), rn = __builtin_amdgcn_rcpf(b.x + b.y);
  for (int i = 0; i < 16; i += 2) {
    const f32x2 u = f32x2{ep[i], ep[i + 1]} * rp, v = f32x2{en[i], en[i + 1]} * rn;
    ep[i] = u.x; ep[i + 1] = u.y; en[i] = v.x; en[i + 1] = v.y;
  }
  pf[0][0] = pack8(ep, 0); pf[0][1] = pack8(ep, 1); pf[1][0] = pack8(en, 0); pf[1][1] = pack8(en, 1);
}

constexpr int refill_end(int s) { return s <= 0 ? 0 : (12 * s + 4) / 5 > 36 ? 36 : (12 * s + 4) / 5; }
constexpr bool refill_ok() { for (int s = 1; s <= 17; ++s) for (int p = refill_end(s - 1); p < refill_end(s); ++p) if (s < 1 + p / 4 || s > 7 + p / 4) return false; return refill_end(15) == 36; }
static_assert(refill_ok(), "refill schedule");
}

__global__ void __launch_bounds__(512, 2) fused_hedgehog_ffn(const char *__restrict__ gx, const char *__restrict__ gwqkv, const char *__restrict__ gwphi, const char *__restrict__ gwo,
                                                              const char *__restrict__ gw1, const char *__restrict__ gb1, const char *__restrict__ gw2, const char *__restrict__ gb2,
                                                              char *__restrict__ gout, char *__restrict__ gws) {
  __shared__ __attribute__((aligned(1024))) char smem[LDS_BYTES];
  lchar *L = (lchar *)smem;
  asm volatile("" ::"s"(gx), "s"(gwqkv), "s"(gwphi), "s"(gwo), "s"(gw1), "s"(gb1), "s"(gw2), "s"(gb2), "s"(gout), "s"(gws));
  const int tid = threadIdx.x, lane = tid & 63, w = rfl(tid >> 6);
  const int b = blockIdx.x, xcd = b & 7, slot = b >> 3;
  const int h = lane >> 5;
  STAMP_DECL; STAMP(0);
  const rsrc_t rx = __builtin_amdgcn_make_buffer_rsrc((void *)gx, 0, 2048 * XROW, 0x00020000);
  const rsrc_t rws = __builtin_amdgcn_make_buffer_rsrc((void *)gws, 0, WS_END, 0x00020000);
  const int tc = slot >= 16 ? slot - 16 : slot + 16;
  const bool has_tile = tc < 28;
  const u32 rt = tc / 7, cc = tc % 7;
  const bool att = slot < 16, ffn = !att && slot < 30;
  const int head = 8 * (xcd & 1) + (slot & 7);
  const int jf = slot - 16, ct = 7 * (xcd & 1) + (jf >= 7 ? jf - 7 : jf);
  const int rga = 2 * (xcd >> 1) + (att ? (slot >> 3) : (jf >= 7));

  if (w >= 4) {
    const int lw = w - 4;
    StageCtx sc;
    sc.rw2 = __builtin_amdgcn_make_buffer_rsrc((void *)gw2, 0, DF * XROW, 0x00020000);
    sc.rwo = __builtin_amdgcn_make_buffer_rsrc((void *)gwo, 0, DM * XROW, 0x00020000);
    sc.vob = (lane >> 3) * XROW + 16 * ((lane & 7) ^ (((lane >> 4) & 1) << 2));
    sc.so2 = rfl((896 * (lw >> 1) + 32 * (lw & 1)) * XROW + 128 * cc);
    sc.soo = rfl(28 * (8 * (lw >> 1) + (lw & 1)) * XROW + 128 * cc);
    sc.ld = rfl((u32)(size_t)L + (lw >> 1) * 8192 + (lw & 1) * 4096);
    u32 E;
    u32x4 br[36];
    const u32 hfoff = WS_HF + ((8 * xcd + 2 * rt) * 14 + (lane < 28 ? lane : 0)) * 64;
    if (att) {
      const rsrc_t rw = __builtin_amdgcn_make_buffer_rsrc((void *)gwqkv, 0, DM * QKVROW, 0x00020000);
      if (lw < 2) {
        const rsrc_t rp = __builtin_amdgcn_make_buffer_rsrc((void *)gwphi, 0, 16 * 28 * 28 * 2, 0x00020000);
        dma16(rp, lane * 16 + lw * 1024, head * 1568, (u32)(size_t)L + L_WPHI + lw * 1024);
      }
      gemmA_loader<true>(L, rx, rw, rfl((256 * rga + 64 * lw) * XROW), rfl(56 * head), lw, lane, sc, br STAMP_PASS);
      BAR();
      E = rfl(*(LAS u32 *)(L + L_E));
      if (has_tile) sfor<0, 5>([&](auto S) { stage_dma<S>(sc); });
      sfor<15, 18>([&](auto S) { stage_load<S>(sc, br); });
      BAR();
      __builtin_amdgcn_s_setprio(3);
      STAMP(4);
      for (int t = 0; t < 2; ++t) {
        f32x4 s = *(LAS f32x4 *)(L + (t * 4 + lw) * 1024 + lane * 16);
        for (int ww = 1; ww < 4; ++ww) s += *(LAS f32x4 *)(L + ww * 8192 + (t * 4 + lw) * 1024 + lane * 16);
        st128_sc1(__builtin_bit_cast(u32x4, s), rws, lane * 16, WS_KVP + (head * 8 + rga) * 8192 + (t * 4 + lw) * 1024);
      }
      vmwait<0>();
      if (lane == 0) st_flag(gws, WS_KF + ((head * 8 + rga) * 4 + lw) * 64, E);
      STAMP(5);
      poll_all(gws, WS_KF + ((head * 8 + (lane & 7)) * 4 + lw) * 64, E);
      STAMP(7);
      u32 hfl;
      asm volatile("global_load_dword %0, %1, off sc1" : "=v"(hfl) : "v"(gws + hfoff) : "memory");
      u32x4 part[2][8];
      for (int t = 0; t < 2; ++t) for (int r = 0; r < 8; ++r) part[t][r] = ld128_sc1(rws, lane * 16, WS_KVP + (head * 8 + r) * 8192 + (t * 4 + lw) * 1024);
      for (int t = 0; t < 2; ++t) {
        f32x4 s = __builtin_bit_cast(f32x4, part[t][0]);
        for (int r = 1; r < 8; ++r) s += __builtin_bit_cast(f32x4, part[t][r]);
        u32x2 pk = {pk2(s.x, s.y), pk2(s.z, s.w)};
        if (lw == 3 && h) pk = u32x2{0, 0};
        *(LAS u32x2 *)(L + L_KVF + ((t * 2 + (lw >> 1)) * 64 + lane) * 16 + (lw & 1) * 8) = pk;
      }
      asm volatile("" : "+v"(hfl));
      BAR();
      __builtin_amdgcn_s_setprio(0);
      STAMP(8);
      if (!has_tile) {
        if (b == 8 * 12 && lw == 0) {
#pragma unroll 1
          for (int spin = 0; spin < SPIN_CAP; ++spin) {
            bool ok = true;
            for (int i = 0; i < 32; ++i) ok &= ld_flag(gws, WS_STAT + (64 * i + lane) * 16 + 4) == E;
            if (__all(ok)) break;
            __builtin_amdgcn_s_sleep(8);
          }
          if (lane == 0) st_flag(gws, WS_EPOCH, E);
        }
        STAMP_OUT();
        return;
      }
      sfor<5, 9>([&](auto S) { stage_dma<S>(sc); });
      if (!__all(hfl == E)) poll_all(gws, hfoff, E);
    } else if (ffn) {
      const rsrc_t rw = __builtin_amdgcn_make_buffer_rsrc((void *)gw1, 0, DM * W1ROW, 0x00020000);
      gemmA_loader<false>(L, rx, rw, rfl((256 * rga + 64 * lw) * XROW), rfl(256 * ct), lw, lane, sc, br STAMP_PASS);
      BAR();
      E = rfl(*(LAS u32 *)(L + L_E));
      sfor<0, 9>([&](auto S) { stage_dma<S>(sc); });
      sfor<15, 18>([&](auto S) { stage_load<S>(sc, br); });
      poll_all(gws, hfoff, E);
    } else {
      sfor<0, 9>([&](auto S) { stage_dma<S>(sc); });
      sfor<9, 18>([&](auto S) { stage_load<S>(sc, br); });
      BAR();
      E = rfl(*(LAS u32 *)(L + L_E));
      poll_all(gws, hfoff, E);
    }
    STAMP(10);
    constexpr int SO = (56 - RING) / 4;
    u32 ofl = 0;
    const u32 ofoff = WS_OF + ((8 * xcd + 2 * rt + ((lane >> 4) & 1)) * 16 + (lane & 15)) * 64;
    const u32 ldv = sc.ld + lane * 16;
    sfor<0, 18>([&](auto S) {
      constexpr int s = S;
      if constexpr (s == SO) {
        vmwait<0>();
        asm volatile("" : "+v"(ofl));
        if (!__all(ofl == E)) poll_all(gws, ofoff, E);
      }
      if constexpr (s == 4) vmwait<0>();
      BAR();
      sfor<refill_end(s - 1), refill_end(s)>([&](auto Pc) {
        constexpr int pc = Pc;
        *(LAS u32x4 *)((lchar *)(size_t)ldv + ((9 + pc / 4 + 4) % NSLOT) * L_SLOT + (pc & 3) * 1024) = br[pc];
      });
      if constexpr (s == SO - 4) asm volatile("global_load_dword %0, %1, off sc1" : "=v"(ofl) : "v"(gws + ofoff) : "memory");
    });
    STAMP(12);
    asm volatile("s_barrier" ::: "memory");
    STAMP_OUT();
    return;
  }

  const Early early = early_issue(gws, rx, 8 * b + 2 * w, lane);
  u32 E;
  auto first = [&]() {
    E = early_finish(early, rws, 8 * b + 2 * w, lane); if (tid == 0) *(LAS u32 *)(L + L_E) = E;
    if (TOUCH_A && (att || ffn)) {
      constexpr u32 OOB = 0x7ffffff0u;
      const u32 lb = (u32)(size_t)L;
      { const u32 g15 = att ? (slot & 7) : 8 + (jf >= 7 ? jf - 7 : jf), i = (g15 * 4 + w) * 22 + lane;
        touch(rx, (lane < 22 && i < 1280) ? (256 * rga + (i & 255)) * XROW + 128 * (2 + (i >> 8)) : OOB, lb); }
      if (att) {
        const rsrc_t rq = __builtin_amdgcn_make_buffer_rsrc((void *)gwqkv, 0, DM * QKVROW, 0x00020000);
        const u32 i = slot * 240 + w * 60 + lane, row = (i * 43691u) >> 19, rem = i - 12 * row;
        touch(rq, lane < 60 ? (128 + row) * QKVROW + 896 * (rem >> 2) + ((448 * (xcd & 1)) & ~127u) + 128 * (rem & 3) : OOB, lb);
      } else {
        const rsrc_t r1 = __builtin_amdgcn_make_buffer_rsrc((void *)gw1, 0, DM * W1ROW, 0x00020000);
        const u32 l8 = 4 * (jf >= 7) + w, i = 64 * l8 + lane, j = 512 + 16 * l8 + lane;
        touch(r1, (128 + (i >> 1)) * W1ROW + 256 * ct + 128 * (i & 1), lb);
        touch(r1, lane < 16 ? (128 + (j >> 1)) * W1ROW + 256 * ct + 128 * (j & 1) : OOB, lb);
      }
    }
  };
  const int rh = w & 1, kg = w >> 1;
  const u32 rb = 8 * xcd + 2 * rt + rh;
  const u32 row = 32 * rb + (lane & 31), col0 = 64 * cc + 32 * kg;
  u32x2 xres[4], b2v[4]; u32x4 sg0;
  auto epi_loads = [&]() {
    for (int g = 0; g < 4; ++g) {
      xres[g] = *(const GAS u32x2 *)(gx + row * XROW + (col0 + 8 * g + 4 * h) * 2);
      b2v[g] = *(const GAS u32x2 *)(gb2 + (col0 + 8 * g + 4 * h) * 2);
    }
    sg0 = ld128_sc1(rws, WS_STAT + row * 16, 0);
  };
  if (att) {
    if (tid < 16) *(LAS u32 *)(L + L_ZERO + 4 * tid) = 0;
    u32x4 idf[2];
    for (int s = 0; s < 2; ++s)
      for (int d = 0; d < 4; ++d) {
        const int k0 = 16 * s + 8 * (d >> 1) + 4 * h + 2 * (d & 1), n = lane & 31;
        idf[s][d] = (n == k0 ? 0x3F80u : 0u) | (n == k0 + 1 ? 0x3F800000u : 0u);
      }
    for (int s = 0; s < 2; ++s) asm volatile("" : "+v"(idf[s]));
    f32x16 acc[2][3]; u32x4 stat[2];
    const u32 vostat = WS_STAT + (256 * rga + 64 * w + (lane & 31)) * 16;
    gemmA_compute<true>(L, w, lane, acc, first, [&]() { for (int r = 0; r < 2; ++r) stat[r] = ld128_sc1(rws, vostat + 512 * r, 0); } STAMP_PASS);
    RowStat st[2];
    for (int r = 0; r < 2; ++r) {
      st[r] = row_stats(stat_settle(stat[r], rws, vostat + 512 * r, E), h);
      *(LAS float *)(L + L_TOK + w * 256 + r * 128 + 4 * (lane & 31)) = st[r].rstd;
    }
    BAR();
    STAMP(3);
    {
      const u32x4 wk = waug(cs_read(L, 0, lane), 0), wv = waug(cs_read(L, 1, lane), 0), wq = waug(cs_read(L, 2, lane), 0);
      for (int r = 0; r < 2; ++r) { acc[r][0] = mfma(wk, st[r].xaug, acc[r][0]); acc[r][1] = mfma(st[r].xaug, wv, acc[r][1]); acc[r][2] = mfma(wq, st[r].xaug, acc[r][2]); }
    }
    u32x4 wp[2];
    {
      const int blk = (lane >> 4) & 1, q = (lane & 15) >> 2, p = lane & 3;
      const u32 col = (blk && p == 3) ? 24 : 16 * blk + 4 * p;
      for (int s = 0; s < 2; ++s) {
        u32x2 r[2];
        for (int t = 0; t < 2; ++t) {
          const int dp = 16 * s + 8 * t + 4 * h + q;
          const u32 a = (dp >= 24 && dp < 28) ? L_ZERO + 8 * p : L_WPHI + (dp >= 28 ? dp - 4 : dp) * 56 + col * 2;
          r[t] = trd(L + a);
        }
        wp[s] = u32x4{r[0].x, r[0].y, r[1].x, r[1].y};
      }
    }
    f32x16 kvp[2] = {zero16(), zero16()};
    {
      f32x16 P[2];
      for (int r = 0; r < 2; ++r) P[r] = mfma(wp[0], pack8(acc[r][0], 0), zero16());
      for (int r = 0; r < 2; ++r) P[r] = mfma(wp[1], pack8(acc[r][0], 1), P[r]);
      u32x4 vf[2][2];
      for (int r = 0; r < 2; ++r) {
        f32x16 v = acc[r][1];
        for (int g = 0; g < 4; ++g) {
          const f32x4 rs = *(LAS f32x4 *)(L + L_TOK + w * 256 + r * 128 + (8 * g + 4 * h) * 4);
          for (int e = 0; e < 4; e += 2) { const f32x2 t2 = f32x2{v[4 * g + e], v[4 * g + e + 1]} * f32x2{rs[e], rs[e + 1]}; v[4 * g + e] = t2.x; v[4 * g + e + 1] = t2.y; }
        }
        for (int s = 0; s < 2; ++s) { vf[r][s] = pack8(v, s); if ((lane & 31) == 24) vf[r][s] = u32x4{0x3F803F80u, 0x3F803F80u, 0x3F803F80u, 0x3F803F80u}; }
      }
      u32x4 pf[2][2][2];
      for (int r = 0; r < 2; ++r) feature_map(P[r], st[r].rstd, h, pf[r]);
      f32x16 Z[2][2];
      for (int r = 0; r < 2; ++r) for (int t = 0; t < 2; ++t) Z[r][t] = mfma(pf[r][t][0], idf[0], zero16());
      for (int r = 0; r < 2; ++r) for (int t = 0; t < 2; ++t) Z[r][t] = mfma(pf[r][t][1], idf[1], Z[r][t]);
      for (int r = 0; r < 2; ++r) for (int s = 0; s < 2; ++s) for (int t = 0; t < 2; ++t) kvp[t] = mfma(pack8(Z[r][t], s), vf[r][s], kvp[t]);
    }
    for (int t = 0; t < 2; ++t)
      for (int g = 0; g < 4; ++g)
        *(LAS f32x4 *)(L + w * 8192 + (t * 4 + g) * 1024 + lane * 16) = f32x4{kvp[t][4 * g], kvp[t][4 * g + 1], kvp[t][4 * g + 2], kvp[t][4 * g + 3]};
    STAMP(4);
    BAR();
    u32x4 pq[2][2][2];
    {
      f32x16 P[2];
      for (int r = 0; r < 2; ++r) P[r] = mfma(wp[0], pack8(acc[r][2], 0), zero16());
      for (int r = 0; r < 2; ++r) P[r] = mfma(wp[1], pack8(acc[r][2], 1), P[r]);
      for (int r = 0; r < 2; ++r) {
        feature_map(P[r], st[r].rstd, h, pq[r]);
        for (int t = 0; t < 2; ++t) for (int s = 0; s < 2; ++s) asm volatile("" : "+v"(pq[r][t][s]));
      }
    }
    if (has_tile) epi_loads();
    else { for (int g = 0; g < 4; ++g) { xres[g] = u32x2{0, 0}; b2v[g] = u32x2{0, 0}; } sg0 = u32x4{0, 0, 0, 0}; }
    STAMP(6);
    BAR();
    STAMP(8);
    {
      u32x4 kf[2][2];
      for (int t = 0; t < 2; ++t) for (int s = 0; s < 2; ++s) kf[t][s] = *(LAS u32x4 *)(L + L_KVF + ((t * 2 + s) * 64 + lane) * 16);
      for (int r = 0; r < 2; ++r) {
        f32x16 num = zero16();
        for (int t = 0; t < 2; ++t) for (int s = 0; s < 2; ++s) num = mfma(kf[t][s], pq[r][t][s], num);
        const float rc = __builtin_amdgcn_rcpf(both_halves(num[12]).x + 1e-6f);
        for (int i = 0; i < 16; ++i) num[i] *= rc;
        if (!h) { num[12] = 0.f; num[13] = 0.f; num[14] = 0.f; num[15] = 0.f; }
        const u32 so = WS_O + ((8 * rga + 2 * w + r) * 32 + 2 * head) * 1024;
        st128_sc1(pack8(num, 0), rws, lane * 16, so);
        st128_sc1(pack8(num, 1), rws, lane * 16, so + 1024);
      }
    }
    vmwait<0>();
    if (lane < 2) st_flag(gws, WS_OF + ((8 * rga + 2 * w + lane) * 16 + head) * 64, E);
    STAMP(9);
    if (!has_tile) { STAMP_OUT(); return; }
  } else if (ffn) {
    u32 bias[4];
    for (int t = 0; t < 4; ++t) bias[t] = *(const GAS unsigned short *)(gb1 + (128 * ct + 32 * t + (lane & 31)) * 2);
    f32x16 acc[2][4]; u32x4 stat[2];
    const u32 vostat = WS_STAT + (256 * rga + 64 * w + (lane & 31)) * 16;
    gemmA_compute<false>(L, w, lane, acc, first, [&]() { for (int r = 0; r < 2; ++r) stat[r] = ld128_sc1(rws, vostat + 512 * r, 0); } STAMP_PASS);
    RowStat st[2];
    for (int r = 0; r < 2; ++r) st[r] = row_stats(stat_settle(stat[r], rws, vostat + 512 * r, E), h);
    BAR();
    STAMP(3);
    epi_loads();
    for (int t = 0; t < 4; ++t) {
      const u32x4 wa = waug(cs_read(L, t, lane), bias[t] | (bias[t] << 16));
      for (int r = 0; r < 2; ++r) {
        f32x16 a = mfma(wa, st[r].xaug, acc[r][t]);
        for (int i = 0; i < 16; i += 2) { f32x2 v = {relu(a[i]), relu(a[i + 1])}; v *= v; a[i] = v.x; a[i + 1] = v.y; }
        const u32 so = WS_H + ((8 * rga + 2 * w + r) * 112 + 8 * ct + 2 * t) * 1024;
        st128_sc1(pack8(a, 0), rws, lane * 16, so);
        st128_sc1(pack8(a, 1), rws, lane * 16, so + 1024);
      }
    }
    STAMP(4);
    vmwait<0>();
    if (lane < 2) st_flag(gws, WS_HF + ((8 * rga + 2 * w + lane) * 14 + ct) * 64, E);
    STAMP(5);
  } else {
    first();
    BAR();
    epi_loads();
  }

  {
    constexpr int RD = RING;
    const u32 soh = WS_H + (rb * 112 + 56 * kg) * 1024, soo = WS_O + (rb * 32 + 16 * kg) * 1024;
    u32x4 ring[RD];
    const float sig = asf(stat_settle(sg0, rws, WS_STAT + row * 16, E)[2]);
    const float rs1 = __builtin_amdgcn_rcpf(sig), rs2 = rs1 * rs1;
    f32x2 bx[8], xx[8];
    for (int g = 0; g < 4; ++g) for (int e = 0; e < 2; ++e) {
      bx[2 * g + e] = f32x2{bf2f(b2v[g][e]), bfhi(b2v[g][e])}; xx[2 * g + e] = f32x2{bf2f(xres[g][e]), bfhi(xres[g][e])};
      asm volatile("" : "+v"(bx[2 * g + e]), "+v"(xx[2 * g + e]));
    }
    const int blk = (lane >> 4) & 1, q = (lane & 15) >> 2, p = lane & 3;
    u32 bb0[2], bb[2];
    for (int c2 = 0; c2 < 2; ++c2) bb0[c2] = (u32)(size_t)L + kg * 8192 + (4 * h + q) * 128 + ((c2 ^ kg ^ (q >> 1)) * 64) + 32 * blk + 8 * p;
    f32x16 af[2] = {zero16(), zero16()}, aa[2] = {zero16(), zero16()};
    u32x4 bf[2][2];
    sfor<0, 72>([&](auto I) {
      constexpr int i = I;
      if constexpr ((i & 3) == 0) asm volatile("s_barrier" ::: "memory");
      if constexpr (i == 0) STAMP(11);
      if constexpr (i == 40) STAMP(14);
      if constexpr (i == 0) {
        for (int j = 0; j < RD; ++j) ring[j] = ld128_sc1(rws, lane * 16, soh + j * 1024);
        __builtin_amdgcn_sched_barrier(0);
        for (int c2 = 0; c2 < 2; ++c2) { bb[c2] = bb0[c2] + 4 * L_SLOT; asm volatile("" : "+v"(bb[c2])); bf[0][c2] = trfrag((lchar *)(size_t)bb[c2], 1024); }
      }
      u32x4 a = ring[i % RD];
      if constexpr (i + 1 < 72) {
        constexpr int n = i + 1;
        if constexpr ((n & 3) == 0) for (int c2 = 0; c2 < 2; ++c2) { bb[c2] = bb0[c2] + (((n >> 2) + 4) % NSLOT) * L_SLOT; asm volatile("" : "+v"(bb[c2])); }
        for (int c2 = 0; c2 < 2; ++c2) bf[n & 1][c2] = trfrag((lchar *)(size_t)bb[c2] + (n & 3) * 2048, 1024);
      }
      if constexpr (i < 56) { af[0] = mfma(bf[i & 1][0], a, af[0]); af[1] = mfma(bf[i & 1][1], a, af[1]); asm volatile("" : "+v"(af[0]), "+v"(af[1])); }
      else { aa[0] = mfma(bf[i & 1][0], a, aa[0]); aa[1] = mfma(bf[i & 1][1], a, aa[1]); asm volatile("" : "+v"(aa[0]), "+v"(aa[1])); }
      if constexpr (i + RD < 56) ring[i % RD] = ld128_sc1(rws, lane * 16, soh + (i + RD) * 1024);
      else if constexpr (i + RD < 72) ring[i % RD] = ld128_sc1(rws, lane * 16, soo + (i + RD - 56) * 1024);
      __builtin_amdgcn_sched_barrier(0);
    });
    STAMP(12);
    lchar *X = L + 4 * L_SLOT;
    {
      const f32x16 &sf = af[1], &sa = aa[1];
      for (int g = 0; g < 4; ++g) {
        *(LAS f32x4 *)(X + w * 8192 + g * 1024 + lane * 16) = f32x4{sf[4 * g], sf[4 * g + 1], sf[4 * g + 2], sf[4 * g + 3]};
        *(LAS f32x4 *)(X + w * 8192 + 4096 + g * 1024 + lane * 16) = f32x4{sa[4 * g], sa[4 * g + 1], sa[4 * g + 2], sa[4 * g + 3]};
      }
    }
    BAR();
    const f32x16 &yf = af[0], &ya = aa[0];
    u32x2 o2[4];
    for (int g = 0; g < 4; ++g) {
      const f32x4 pf = *(LAS f32x4 *)(X + (w ^ 2) * 8192 + g * 1024 + lane * 16), pa = *(LAS f32x4 *)(X + (w ^ 2) * 8192 + 4096 + g * 1024 + lane * 16);
      for (int e = 0; e < 2; ++e) {
        const int i = 4 * g + 2 * e;
        const f32x2 y = (f32x2{yf[i], yf[i + 1]} + f32x2{pf[2 * e], pf[2 * e + 1]}) * rs2 + bx[2 * g + e];
        const f32x2 t = y * -LOG2E;
        const f32x2 d = f32x2{__builtin_amdgcn_exp2f(t.x), __builtin_amdgcn_exp2f(t.y)} + 1.0f;
        const f32x2 sg = y * f32x2{__builtin_amdgcn_rcpf(d.x), __builtin_amdgcn_rcpf(d.y)};
        const f32x2 r = (f32x2{ya[i], ya[i + 1]} + f32x2{pa[2 * e], pa[2 * e + 1]}) * sg + xx[2 * g + e];
        o2[g][e] = pk2(r.x, r.y);
      }
    }
    const rsrc_t rout = __builtin_amdgcn_make_buffer_rsrc((void *)gout, 0, 2048 * XROW, 0x00020000);
    for (int g = 0; g < 4; g += 2) {
      auto rx0 = __builtin_amdgcn_permlane32_swap(o2[g].x, o2[g + 1].x, false, false);
      auto ry0 = __builtin_amdgcn_permlane32_swap(o2[g].y, o2[g + 1].y, false, false);
      __builtin_amdgcn_raw_buffer_store_b128(u32x4{rx0[0], ry0[0], rx0[1], ry0[1]}, rout, row * XROW + (col0 + 8 * (g + h)) * 2, 0, OUT_AUX);
    }
    STAMP(13);
    STAMP(15);
    STAMP_OUT();
  }
}

extern "C" void kernel_launch(void *const *d_in, const int *in_sizes, int n_in, void *d_out, int out_size, void *d_ws, size_t ws_size, hipStream_t stream) {
  fused_hedgehog_ffn<<<256, 512, 0, stream>>>((const char *)d_in[0], (const char *)d_in[1], (const char *)d_in[2], (const char *)d_in[3], (const char *)d_in[5],
                                              (const char *)d_in[6], (const char *)d_in[7], (const char *)d_in[8], (char *)d_out, (char *)d_ws);
}
```

```cpp
#include <hip/hip_runtime.h>
#include <hip/hip_bf16.h>
#include <stdint.h>
#include <type_traits>

typedef unsigned u32;
typedef u32 u32x4 __attribute__((ext_vector_type(4)));
typedef u32 u32x2 __attribute__((ext_vector_type(2)));
typedef float f32x16 __attribute__((ext_vector_type(16)));
typedef float f32x4 __attribute__((ext_vector_type(4)));
typedef float f32x2 __attribute__((ext_vector_type(2)));
typedef short s16x8 __attribute__((ext_vector_type(8)));
typedef short s16x4 __attribute__((ext_vector_type(4)));
typedef __bf16 bf16x2 __attribute__((ext_vector_type(2)));
typedef __bf16 bf16x8 __attribute__((ext_vector_type(8)));
#define LAS __attribute__((address_space(3)))
#define GAS __attribute__((address_space(1)))
#define DI __device__ __forceinline__
typedef __amdgpu_buffer_rsrc_t rsrc_t;
typedef LAS char lchar;

namespace {
constexpr int DM = 448, DF = 1792;
constexpr u32 XROW = DM * 2;
constexpr u32 QKVROW = 3 * DM * 2;
constexpr u32 W1ROW = DF * 2;
constexpr u32 WS_EPOCH = 0;
constexpr u32 WS_STAT = 4096;
constexpr u32 WS_KF = 65536;
constexpr u32 WS_HF = 131072;
constexpr u32 WS_OF = 196608;
constexpr u32 WS_KVP = 262144;
constexpr u32 WS_H = WS_KVP + (1u << 20);
constexpr u32 WS_O = WS_H + 64 * 112 * 1024;
constexpr u32 WS_END = WS_O + 64 * 32 * 1024;
constexpr u32 L_SLOT = 16384, NSLOT = 9;
constexpr u32 A_SLOT = 49152, A_WOFF = 32768;
constexpr u32 L_MISC = NSLOT * L_SLOT;
constexpr u32 L_WPHI = L_MISC;
constexpr u32 L_ZERO = L_MISC + 2048;
constexpr u32 L_E = L_MISC + 2112;
constexpr u32 L_DUMMY = L_MISC + 2304;
constexpr u32 L_CSP = L_MISC + 2560;
constexpr u32 L_TOK = L_CSP + 512;
constexpr u32 L_KVF = L_TOK + 1024;
constexpr u32 L_END = L_KVF + 4096;
constexpr u32 LDS_BYTES = 163840;
static_assert(L_END <= LDS_BYTES, "lds");
constexpr int SPIN_CAP = 1 << 16;
constexpr float LOG2E = 1.4426950408889634f;
#define CS_DOT2 0
#define SGB 1
#define LPRIO 0
#define FIRST_AT -1
#define TOUCH_A 0
#define OUT_AUX 0
#define SPREAD 0
#define PIN_C 0
#define RING 12

#define STAMP(i) do {} while (0)
#define STAMP_DECL do {} while (0)
#define STAMP_OUT() do {} while (0)
#define STAMP_ARG
#define STAMP_PASS
template <int B, int E, class F> DI void sfor(F &&f) {
  if constexpr (B < E) { f(std::integral_constant<int, B>{}); sfor<B + 1, E>(f); }
}
DI u32 rfl(u32 v) { return __builtin_amdgcn_readfirstlane(v); }
DI f32x16 mfma(u32x4 a, u32x4 b, f32x16 c) {
  return __builtin_amdgcn_mfma_f32_32x32x16_bf16(__builtin_bit_cast(bf16x8, a), __builtin_bit_cast(bf16x8, b), c, 0, 0, 0);
}
DI u32 pk2(float lo, float hi) { f32x2 p = {lo, hi}; return __builtin_bit_cast(u32, __builtin_convertvector(p, bf16x2)); }
DI float dot2(u32 a, u32 b, float c) { return __builtin_amdgcn_fdot2_f32_bf16(__builtin_bit_cast(bf16x2, a), __builtin_bit_cast(bf16x2, b), c, false); }
DI float bf2f(u32 lo16) { return __builtin_bit_cast(float, lo16 << 16); }
DI float bfhi(u32 v) { return __builtin_bit_cast(float, v & 0xffff0000u); }
DI float asf(u32 v) { return __builtin_bit_cast(float, v); }
DI f32x2 both_halves(float v) {
  const u32 a = __builtin_bit_cast(u32, v);
  auto r = __builtin_amdgcn_permlane32_swap(a, a, false, false);
  return f32x2{asf(r[0]), asf(r[1])};
}
DI float half_lo(float v) { return both_halves(v).x; }
DI u32x4 pack8(const f32x16 &x, int s) { return u32x4{pk2(x[8 * s], x[8 * s + 1]), pk2(x[8 * s + 2], x[8 * s + 3]), pk2(x[8 * s + 4], x[8 * s + 5]), pk2(x[8 * s + 6], x[8 * s + 7])}; }
DI u32x2 trd(lchar *p) {
  typedef short v4i16 __attribute__((ext_vector_type(4)));
  return __builtin_bit_cast(u32x2, __builtin_amdgcn_ds_read_tr16_b64_v4i16((LAS v4i16 *)p));
}
DI u32x4 trfrag(lchar *p, int tstride) { u32x2 a = trd(p), b = trd(p + tstride); return u32x4{a.x, a.y, b.x, b.y}; }
DI float relu(float v) { return __builtin_amdgcn_fmed3f(v, 0.f, __builtin_inff()); }
DI f32x16 zero16() { f32x16 z; for (int i = 0; i < 16; ++i) z[i] = 0.f; return z; }
#define BAR() asm volatile("s_waitcnt lgkmcnt(0)\n\ts_barrier" ::: "memory")
template <int N> DI void vmwait() { asm volatile("s_waitcnt vmcnt(%0)" ::"i"(N) : "memory"); }
DI void dma16(rsrc_t r, u32 voff, u32 soff, u32 ldsaddr) {
  asm volatile("s_mov_b32 m0, %0\n\ts_nop 0\n\tbuffer_load_dwordx4 %1, %2, %3 offen lds" ::"s"(ldsaddr), "v"(voff), "s"(r), "s"(soff) : "memory", "m0");
}
template <int OFF> DI void ald128(u32x4 &d, rsrc_t r, u32 voff, u32 soff) {
  asm volatile("buffer_load_dwordx4 %0, %1, %2, %3 offen offset:%4" : "=v"(d) : "v"(voff), "s"(r), "s"(soff), "i"(OFF) : "memory");
}
DI u32x4 ld128_sc1(rsrc_t r, u32 voff, u32 soff) { return __builtin_amdgcn_raw_buffer_load_b128(r, voff, soff, 16); }
DI void st128_sc1(u32x4 v, rsrc_t r, u32 voff, u32 soff) {
  __builtin_amdgcn_raw_buffer_store_b128(v, r, voff, soff, 16);
  asm volatile("s_nop 1" ::"v"(v) : "memory");
}
typedef GAS u32 gu32;
DI u32 ld_flag(const char *ws, u32 off) { return __hip_atomic_load((gu32 *)(ws + off), __ATOMIC_RELAXED, __HIP_MEMORY_SCOPE_AGENT); }
DI void st_flag(char *ws, u32 off, u32 v) { __hip_atomic_store((gu32 *)(ws + off), v, __ATOMIC_RELAXED, __HIP_MEMORY_SCOPE_AGENT); }
DI void poll_all(const char *ws, u32 off, u32 E) {
#pragma unroll 1
  for (int spin = 0; spin < SPIN_CAP; ++spin) {
    if (__all(ld_flag(ws, off) == E)) break;
    __builtin_amdgcn_s_sleep(1);
  }
  __builtin_amdgcn_fence(__ATOMIC_ACQUIRE, "wavefront");
}

template <u32 LI, u32 SI> DI void dmai(rsrc_t r, u32 voff, u32 sob, u32 ldsb) {
  u32 t;
  asm volatile("s_add_u32 m0, %2, %5\n\ts_add_u32 %0, %1, %6\n\tbuffer_load_dwordx4 %3, %4, %0 offen lds"
               : "=&s"(t) : "s"(sob), "s"(ldsb), "v"(voff), "s"(r), "i"(LI), "i"(SI) : "memory", "m0", "scc");
}
template <int CTRL, int RMASK> DI float dpp_add(float v) {
  return v + asf(__builtin_amdgcn_update_dpp(0u, __builtin_bit_cast(u32, v), CTRL, RMASK, 0xf, true));
}
DI float wave_sum63(float v) {
  v = dpp_add<0x111, 0xf>(v); v = dpp_add<0x112, 0xf>(v); v = dpp_add<0x114, 0xf>(v); v = dpp_add<0x118, 0xf>(v);
  v = dpp_add<0x142, 0xa>(v); v = dpp_add<0x143, 0xc>(v);
  return v;
}
DI void st64_sc1(char *ws, u32 off, u32 lo, u32 hi) {
  __hip_atomic_store((GAS unsigned long long *)(ws + off), (unsigned long long)lo | ((unsigned long long)hi << 32), __ATOMIC_RELAXED, __HIP_MEMORY_SCOPE_AGENT);
}
struct Early { u32 ev; u32x4 xa, xb; };
DI Early early_issue(char *ws, rsrc_t rx, int row0, int lane) {
  Early e;
  e.ev = ld_flag(ws, WS_EPOCH);
  const u32 vo = lane < 56 ? (u32)lane * 16 : 0x7ffffff0u;
  e.xa = __builtin_amdgcn_raw_buffer_load_b128(rx, vo, row0 * XROW, 0); e.xb = __builtin_amdgcn_raw_buffer_load_b128(rx, vo, (row0 + 1) * XROW, 0);
  return e;
}
DI u32 early_finish(const Early &e, rsrc_t rws, int row0, int lane) {
  const u32 E = rfl(e.ev) + 1, ONES = 0x3F803F80u;
  float s[2], q[2];
  for (int r = 0; r < 2; ++r) {
    const u32x4 &x = r ? e.xb : e.xa;
    s[r] = dot2(x[0], ONES, 0.f); q[r] = dot2(x[0], x[0], 0.f);
    for (int d = 1; d < 4; ++d) { s[r] = dot2(x[d], ONES, s[r]); q[r] = dot2(x[d], x[d], q[r]); }
    s[r] = wave_sum63(s[r]); q[r] = wave_sum63(q[r]);
    const float mean = s[r] * (1.0f / DM);
    const float sig = __builtin_amdgcn_sqrtf(fmaxf(q[r] * (1.0f / DM) - mean * mean, 0.f) + 1e-5f);
    const u32 vo = lane == 63 ? WS_STAT + (row0 + r) * 16 : 0x7ffffff0u;
    __builtin_amdgcn_raw_buffer_store_b64(u32x2{__builtin_bit_cast(u32, mean), E}, rws, vo, 0, 16);
    __builtin_amdgcn_raw_buffer_store_b64(u32x2{__builtin_bit_cast(u32, sig), E}, rws, vo + 8, 0, 16);
  }
  return E;
}
DI u32x4 stat_settle(u32x4 g, rsrc_t rws, u32 vostat, u32 E) {
#pragma unroll 1
  for (int spin = 0; spin < SPIN_CAP && !__all(g[1] == E && g[3] == E); ++spin) { __builtin_amdgcn_s_sleep(1); g = ld128_sc1(rws, vostat, 0); }
  return g;
}
struct RowStat { float rstd; u32x4 xaug; };
DI RowStat row_stats(u32x4 g, int h) {
  const float m = -asf(g[0]), sig = asf(g[2]);
  RowStat r; r.rstd = __builtin_amdgcn_rcpf(sig);
  const float ml = m - bfhi(pk2(m, m)), sl = sig - bfhi(pk2(sig, sig));
  const u32 mm = pk2(m, ml), ss = pk2(sig, sl);
  r.xaug = h ? u32x4{0, 0, 0, 0} : u32x4{mm, mm, ss, 0};
  return r;
}
DI u32x4 waug(float cs, u32 bb) {
  const u32 hh = pk2(cs, cs); const float cl = cs - bfhi(hh);
  return u32x4{hh, pk2(cl, cl), bb, 0};
}
DI float cs_read(lchar *L, int t, int lane) { return *(LAS float *)(L + L_CSP + (t * 32 + (lane & 31)) * 4); }

DI void touch(rsrc_t r, u32 voff, u32 lb) {
  asm volatile("s_mov_b32 m0, %0\n\ts_nop 0\n\tbuffer_load_dword %1, %2, 0 offen lds" ::"s"(lb + L_DUMMY), "v"(voff), "s"(r) : "memory", "m0");
}
template <bool ATT, class First, class After0, class Mid>
DI void gemmA_compute(lchar *L, int w, int lane, f32x16 (&acc)[2][ATT ? 3 : 4], First &&first, After0 &&after0, Mid &&mid STAMP_ARG) {
  constexpr int T = ATT ? 3 : 4, ROWB = ATT ? 64 : 256;
  const int h = lane >> 5, blk = (lane >> 4) & 1, q = (lane & 15) >> 2, p = lane & 3;
  const u32 lb = (u32)(size_t)L;
  u32 wb0[T], xb0[4], wb[T], xb[4], cb0, cb;
  for (int t = 0; t < T; ++t) wb0[t] = lb + A_WOFF + (ATT ? t * 4096 + (8 * h + q) * 64 : (8 * h + q) * 256 + 64 * (t ^ q)) + 32 * blk + 8 * p;
  for (int u = 0; u < 4; ++u) xb0[u] = lb + w * 8192 + (lane & 31) * 128 + 16 * ((2 * u + h) ^ ((lane >> 1) & 7));
  const int tw = w < T ? w : T - 1;
  cb0 = lb + A_WOFF + (ATT ? tw * 4096 + (8 * h + q) * 64 : (8 * h + q) * 256 + 64 * (tw ^ q)) + 32 * blk + 8 * p;
  f32x2 ca = {0.f, 0.f};
  for (int r = 0; r < 2; ++r) for (int t = 0; t < T; ++t) acc[r][t] = zero16();
  u32x4 xf[2][2], wf[2][T], cf[2];
  sfor<0, 29>([&](auto I) {
    constexpr int i = I, c = i >> 2, u = i & 3;
    if constexpr (i == 0 && FIRST_AT < 0) { first(); STAMP(14); }
    if constexpr (u == 0 && i < 28) {
      BAR();
      if constexpr (c == 0) STAMP(1);
      constexpr u32 so = (c % 3) * A_SLOT;
      for (int t = 0; t < T; ++t) { wb[t] = wb0[t] + so; asm volatile("" : "+v"(wb[t])); }
      for (int j = 0; j < 4; ++j) { xb[j] = xb0[j] + so; asm volatile("" : "+v"(xb[j])); }
      cb = cb0 + so; asm volatile("" : "+v"(cb));
    }
    if constexpr (i < 28) {
      cf[i & 1] = trfrag((lchar *)(size_t)cb + 16 * u * ROWB, 4 * ROWB);
      for (int r = 0; r < 2; ++r) xf[i & 1][r] = *(LAS u32x4 *)((lchar *)(size_t)xb[u] + r * 4096);
      for (int t = 0; t < T; ++t) wf[i & 1][t] = trfrag((lchar *)(size_t)wb[t] + 16 * u * ROWB, 4 * ROWB);
    }
    if constexpr (i == FIRST_AT) first();
    if constexpr (i == 1) after0();
    if constexpr (i == 20) mid();
    if constexpr (i > 0) {
      constexpr int j = (i - 1) & 1;
      for (int t = 0; t < T; ++t)
        for (int r = 0; r < 2; ++r) acc[r][t] = (ATT && t == 1) ? mfma(xf[j][r], wf[j][t], acc[r][t]) : mfma(wf[j][t], xf[j][r], acc[r][t]);
      for (int d = 0; d < 4; ++d) ca += f32x2{bf2f(cf[j][d]), bfhi(cf[j][d])};
      asm volatile("" : "+v"(ca));
      for (int m = 0; m < 2 * T; ++m) { __builtin_amdgcn_sched_group_barrier(0x8, 1, 0); __builtin_amdgcn_sched_group_barrier(0x100, 2, 0); __builtin_amdgcn_sched_group_barrier(0x2, 2, 0); }
    }
    __builtin_amdgcn_sched_barrier(0);
  });
  { const f32x2 a = both_halves(ca.x + ca.y); if (lane < 32 && w < T) *(LAS float *)(L + L_CSP + (w * 32 + lane) * 4) = a.x + a.y; }
  STAMP(2);
}
struct StageCtx { rsrc_t rw2, rwo; u32 vob, so2, soo, ld; };
template <int ST> DI void stage_dma(const StageCtx &c) {
  sfor<0, 4>([&](auto I) {
    constexpr u32 i = I, dst = ((ST + 4) % NSLOT) * L_SLOT + i * 1024;
    dmai<dst, (64 * ST + 8 * i) * XROW>(c.rw2, c.vob, c.so2, c.ld);
  });
}
template <int ST> DI void stage_load(const StageCtx &c, u32x4 (&br)[36]) {
  sfor<0, 4>([&](auto I) {
    constexpr u32 i = I;
    if constexpr (ST < 14) br[4 * (ST - 9) + i] = __builtin_amdgcn_raw_buffer_load_b128(c.rw2, c.vob, c.so2 + (64 * ST + 8 * i) * XROW, 0);
    else br[4 * (ST - 9) + i] = __builtin_amdgcn_raw_buffer_load_b128(c.rwo, c.vob, c.soo + (56 * (ST - 14) + (i == 3 ? 20 : 8 * i)) * XROW, 0);
  });
}
template <bool ATT>
DI void gemmA_loader(lchar *L, rsrc_t rx, rsrc_t rw, u32 sox, u32 sow, int lw, int lane, const StageCtx &sc, u32x4 (&br)[36] STAMP_ARG) {
  constexpr int NW = ATT ? 3 : 4, N = 8 + NW;
  const u32 lb = (u32)(size_t)L;
  const u32 vox0 = (lane >> 3) * XROW + 16 * ((lane & 7) ^ ((lane >> 4) & 3)), vox1 = vox0 ^ 64;
  const u32 ldx = rfl(lb + lw * 8192);
  const u32 vow = ATT ? (lane >> 2) * QKVROW + ((lane & 3) == 3 ? 40 : 16 * (lane & 3)) : (lane >> 4) * W1ROW + 16 * ((lane & 15) ^ ((lane >> 4) << 2));
  const u32 swb = rfl(sow + 16 * lw * (ATT ? QKVROW : W1ROW)), ldw = rfl(lb + A_WOFF + lw * (ATT ? 1024 : 4096));
  auto issue = [&](auto C) {
    constexpr u32 c = C, so = (c % 3) * A_SLOT;
    sfor<0, NW>([&](auto I) {
      constexpr u32 i = I;
      if constexpr (ATT) dmai<so + i * 4096, 64 * c * QKVROW + (i == 0 ? 896 : i == 1 ? 1792 : 0)>(rw, vow, swb, ldw);
      else dmai<so + i * 1024, (64 * c + 4 * i) * W1ROW>(rw, vow, swb, ldw);
    });
    sfor<0, 8>([&](auto I) { constexpr u32 i = I; dmai<so + i * 1024, 8 * i * XROW + 128 * c>(rx, (i & 1) ? vox1 : vox0, sox, ldx); });
  };
  issue(std::integral_constant<int, 0>{});
  issue(std::integral_constant<int, 1>{});
  sfor<0, 7>([&](auto C) {
    constexpr int c = C;
    vmwait<(c == 6 ? 12 : N)>();
    asm volatile("s_barrier" ::: "memory");
    if constexpr (c == 0) STAMP(1);
    if constexpr (c + 2 < 7) issue(std::integral_constant<int, c + 2>{});
    else { stage_load<9 + 3 * (c - 5)>(sc, br); stage_load<10 + 3 * (c - 5)>(sc, br); stage_load<11 + 3 * (c - 5)>(sc, br); }
  });
  STAMP(2);
}

DI void feature_map(const f32x16 &P, float rstd, int h, u32x4 (&pf)[2][2]) {
  const float c = rstd * LOG2E;
  float mx = fmaxf(fmaxf(P[0], P[1]), P[2]), mn = fminf(fminf(P[0], P[1]), P[2]);
  for (int i = 3; i < 15; i += 2) { mx = fmaxf(fmaxf(mx, P[i]), P[i + 1]); mn = fminf(fminf(mn, P[i]), P[i + 1]); }
  mx = fmaxf(mx, P[15]); mn = fminf(mn, P[15]);
  { f32x2 a = both_halves(mx), b = both_halves(mn); mx = fmaxf(a.x, a.y); mn = fminf(b.x, b.y); }
  const float mxc = -mx * c, mnc = mn * c;
  f32x16 ep, en;
  for (int i = 0; i < 16; i += 2) {
    const f32x2 p2 = {P[i], P[i + 1]}, ap = p2 * c + mxc, an = mnc - p2 * c;
    ep[i] = __builtin_amdgcn_exp2f(ap.x); ep[i + 1] = __builtin_amdgcn_exp2f(ap.y);
    en[i] = __builtin_amdgcn_exp2f(an.x); en[i + 1] = __builtin_amdgcn_exp2f(an.y);
  }
  f32x2 sp2 = {ep[0], ep[1]}, sn2 = {en[0], en[1]};
  for (int i = 2; i < 12; i += 2) { sp2 += f32x2{ep[i], ep[i + 1]}; sn2 += f32x2{en[i], en[i + 1]}; }
  const float keep = h ? 0.f : 1.f;
  sp2 += (f32x2{ep[12], ep[13]} + f32x2{ep[14], ep[15]}) * keep;
  sn2 += (f32x2{en[12], en[13]} + f32x2{en[14], en[15]}) * keep;
  const f32x2 a = both_halves(sp2.x + sp2.y), b = both_halves(sn2.x + sn2.y);
  const float rp = __builtin_amdgcn_rcpf(a.x + a.y), rn = __builtin_amdgcn_rcpf(b.x + b.y);
  for (int i = 0; i < 16; i += 2) {
    const f32x2 u = f32x2{ep[i], ep[i + 1]} * rp, v = f32x2{en[i], en[i + 1]} * rn;
    ep[i] = u.x; ep[i + 1] = u.y; en[i] = v.x; en[i + 1] = v.y;
  }
  pf[0][0] = pack8(ep, 0); pf[0][1] = pack8(ep, 1); pf[1][0] = pack8(en, 0); pf[1][1] = pack8(en, 1);
}

constexpr int refill_end(int s) { return s <= 0 ? 0 : (12 * s + 4) / 5 > 36 ? 36 : (12 * s + 4) / 5; }
constexpr bool refill_ok() { for (int s = 1; s <= 17; ++s) for (int p = refill_end(s - 1); p < refill_end(s); ++p) if (s < 1 + p / 4 || s > 7 + p / 4) return false; return refill_end(15) == 36; }
static_assert(refill_ok(), "refill schedule");
}

__global__ void __launch_bounds__(512, 2) fused_hedgehog_ffn(const char *__restrict__ gx, const char *__restrict__ gwqkv, const char *__restrict__ gwphi, const char *__restrict__ gwo,
                                                              const char *__restrict__ gw1, const char *__restrict__ gb1, const char *__restrict__ gw2, const char *__restrict__ gb2,
                                                              char *__restrict__ gout, char *__restrict__ gws) {
  __shared__ __attribute__((aligned(1024))) char smem[LDS_BYTES];
  lchar *L = (lchar *)smem;
  asm volatile("" ::"s"(gx), "s"(gwqkv), "s"(gwphi), "s"(gwo), "s"(gw1), "s"(gb1), "s"(gw2), "s"(gb2), "s"(gout), "s"(gws));
  const int tid = threadIdx.x, lane = tid & 63, w = rfl(tid >> 6);
  const int b = blockIdx.x, xcd = b & 7, slot = b >> 3;
  const int h = lane >> 5;
  STAMP_DECL; STAMP(0);
  const rsrc_t rx = __builtin_amdgcn_make_buffer_rsrc((void *)gx, 0, 2048 * XROW, 0x00020000);
  const rsrc_t rws = __builtin_amdgcn_make_buffer_rsrc((void *)gws, 0, WS_END, 0x00020000);
  const int tc = slot >= 16 ? slot - 16 : slot + 16;
  const bool has_tile = tc < 28;
  const u32 rt = tc / 7, cc = tc % 7;
  const bool att = slot < 16, ffn = !att && slot < 30;
  const int head = 8 * (xcd & 1) + (slot & 7);
  const int jf = slot - 16, ct = 7 * (xcd & 1) + (jf >= 7 ? jf - 7 : jf);
  const int rga = 2 * (xcd >> 1) + (att ? (slot >> 3) : (jf >= 7));

  if (w >= 4) {
    const int lw = w - 4;
    StageCtx sc;
    sc.rw2 = __builtin_amdgcn_make_buffer_rsrc((void *)gw2, 0, DF * XROW, 0x00020000);
    sc.rwo = __builtin_amdgcn_make_buffer_rsrc((void *)gwo, 0, DM * XROW, 0x00020000);
    sc.vob = (lane >> 3) * XROW + 16 * ((lane & 7) ^ (((lane >> 4) & 1) << 2));
    sc.so2 = rfl((896 * (lw >> 1) + 32 * (lw & 1)) * XROW + 128 * cc);
    sc.soo = rfl(28 * (8 * (lw >> 1) + (lw & 1)) * XROW + 128 * cc);
    sc.ld = rfl((u32)(size_t)L + (lw >> 1) * 8192 + (lw & 1) * 4096);
    u32 E;
    u32x4 br[36];
    const u32 hfoff = WS_HF + ((8 * xcd + 2 * rt) * 14 + (lane < 28 ? lane : 0)) * 64;
    if (att) {
      const rsrc_t rw = __builtin_amdgcn_make_buffer_rsrc((void *)gwqkv, 0, DM * QKVROW, 0x00020000);
      if (lw < 2) {
        const rsrc_t rp = __builtin_amdgcn_make_buffer_rsrc((void *)gwphi, 0, 16 * 28 * 28 * 2, 0x00020000);
        dma16(rp, lane * 16 + lw * 1024, head * 1568, (u32)(size_t)L + L_WPHI + lw * 1024);
      }
      gemmA_loader<true>(L, rx, rw, rfl((256 * rga + 64 * lw) * XROW), rfl(56 * head), lw, lane, sc, br STAMP_PASS);
      BAR();
      E = rfl(*(LAS u32 *)(L + L_E));
      if (has_tile) sfor<0, 5>([&](auto S) { stage_dma<S>(sc); });
      sfor<15, 18>([&](auto S) { stage_load<S>(sc, br); });
      BAR();
      __builtin_amdgcn_s_setprio(3);
      STAMP(4);
      for (int t = 0; t < 2; ++t) {
        f32x4 s = *(LAS f32x4 *)(L + (t * 4 + lw) * 1024 + lane * 16);
        for (int ww = 1; ww < 4; ++ww) s += *(LAS f32x4 *)(L + ww * 8192 + (t * 4 + lw) * 1024 + lane * 16);
        st128_sc1(__builtin_bit_cast(u32x4, s), rws, lane * 16, WS_KVP + (head * 8 + rga) * 8192 + (t * 4 + lw) * 1024);
      }
      vmwait<0>();
      if (lane == 0) st_flag(gws, WS_KF + ((head * 8 + rga) * 4 + lw) * 64, E);
      STAMP(5);
      poll_all(gws, WS_KF + ((head * 8 + (lane & 7)) * 4 + lw) * 64, E);
      STAMP(7);
      u32 hfl;
      asm volatile("global_load_dword %0, %1, off sc1" : "=v"(hfl) : "v"(gws + hfoff) : "memory");
      u32x4 part[2][8];
      for (int t = 0; t < 2; ++t) for (int r = 0; r < 8; ++r) part[t][r] = ld128_sc1(rws, lane * 16, WS_KVP + (head * 8 + r) * 8192 + (t * 4 + lw) * 1024);
      for (int t = 0; t < 2; ++t) {
        f32x4 s = __builtin_bit_cast(f32x4, part[t][0]);
        for (int r = 1; r < 8; ++r) s += __builtin_bit_cast(f32x4, part[t][r]);
        u32x2 pk = {pk2(s.x, s.y), pk2(s.z, s.w)};
        if (lw == 3 && h) pk = u32x2{0, 0};
        *(LAS u32x2 *)(L + L_KVF + ((t * 2 + (lw >> 1)) * 64 + lane) * 16 + (lw & 1) * 8) = pk;
      }
      asm volatile("" : "+v"(hfl));
      BAR();
      __builtin_amdgcn_s_setprio(0);
      STAMP(8);
      if (!has_tile) {
        if (b == 8 * 12 && lw == 0) {
#pragma unroll 1
          for (int spin = 0; spin < SPIN_CAP; ++spin) {
            bool ok = true;
            for (int i = 0; i < 32; ++i) ok &= ld_flag(gws, WS_STAT + (64 * i + lane) * 16 + 4) == E;
            if (__all(ok)) break;
            __builtin_amdgcn_s_sleep(8);
          }
          if (lane == 0) st_flag(gws, WS_EPOCH, E);
        }
        STAMP_OUT();
        return;
      }
      sfor<5, 9>([&](auto S) { stage_dma<S>(sc); });
      if (!__all(hfl == E)) poll_all(gws, hfoff, E);
    } else if (ffn) {
      const rsrc_t rw = __builtin_amdgcn_make_buffer_rsrc((void *)gw1, 0, DM * W1ROW, 0x00020000);
      gemmA_loader<false>(L, rx, rw, rfl((256 * rga + 64 * lw) * XROW), rfl(256 * ct), lw, lane, sc, br STAMP_PASS);
      BAR();
      E = rfl(*(LAS u32 *)(L + L_E));
      sfor<0, 9>([&](auto S) { stage_dma<S>(sc); });
      sfor<15, 18>([&](auto S) { stage_load<S>(sc, br); });
      poll_all(gws, hfoff, E);
    } else {
      sfor<0, 9>([&](auto S) { stage_dma<S>(sc); });
      sfor<9, 18>([&](auto S) { stage_load<S>(sc, br); });
      BAR();
      E = rfl(*(LAS u32 *)(L + L_E));
      poll_all(gws, hfoff, E);
    }
    STAMP(10);
    constexpr int SO = (56 - RING) / 4;
    u32 ofl = 0;
    const u32 ofoff = WS_OF + ((8 * xcd + 2 * rt + ((lane >> 4) & 1)) * 16 + (lane & 15)) * 64;
    const u32 ldv = sc.ld + lane * 16;
    sfor<0, 18>([&](auto S) {
      constexpr int s = S;
      if constexpr (s == SO) {
        vmwait<0>();
        asm volatile("" : "+v"(ofl));
        if (!__all(ofl == E)) poll_all(gws, ofoff, E);
      }
      if constexpr (s == 4) vmwait<0>();
      BAR();
      sfor<refill_end(s - 1), refill_end(s)>([&](auto Pc) {
        constexpr int pc = Pc;
        *(LAS u32x4 *)((lchar *)(size_t)ldv + ((9 + pc / 4 + 4) % NSLOT) * L_SLOT + (pc & 3) * 1024) = br[pc];
      });
      if constexpr (s == SO - 4) asm volatile("global_load_dword %0, %1, off sc1" : "=v"(ofl) : "v"(gws + ofoff) : "memory");
    });
    STAMP(12);
    asm volatile("s_barrier" ::: "memory");
    STAMP_OUT();
    return;
  }

  const Early early = early_issue(gws, rx, 8 * b + 2 * w, lane);
  u32 E;
  auto touches = [&]() {
    if (att || ffn) {
      constexpr u32 OOB = 0x7ffffff0u;
      const u32 lb = (u32)(size_t)L;
      { const u32 g15 = att ? (slot & 7) : 8 + (jf >= 7 ? jf - 7 : jf), i = (g15 * 4 + w) * 22 + lane;
        touch(rx, (lane < 22 && i < 1280) ? (256 * rga + (i & 255)) * XROW + 128 * (2 + (i >> 8)) : OOB, lb); }
      if (att) {
        const rsrc_t rq = __builtin_amdgcn_make_buffer_rsrc((void *)gwqkv, 0, DM * QKVROW, 0x00020000);
        const u32 i = slot * 240 + w * 60 + lane, row = (i * 43691u) >> 19, rem = i - 12 * row;
        touch(rq, lane < 60 ? (128 + row) * QKVROW + 896 * (rem >> 2) + ((448 * (xcd & 1)) & ~127u) + 128 * (rem & 3) : OOB, lb);
      } else {
        const rsrc_t r1 = __builtin_amdgcn_make_buffer_rsrc((void *)gw1, 0, DM * W1ROW, 0x00020000);
        const u32 l8 = 4 * (jf >= 7) + w, i = 64 * l8 + lane, j = 512 + 16 * l8 + lane;
        touch(r1, (128 + (i >> 1)) * W1ROW + 256 * ct + 128 * (i & 1), lb);
        touch(r1, lane < 16 ? (128 + (j >> 1)) * W1ROW + 256 * ct + 128 * (j & 1) : OOB, lb);
      }
    }
  };
  if (TOUCH_A == 2) touches();
  auto first = [&]() {
    E = early_finish(early, rws, 8 * b + 2 * w, lane); if (tid == 0) *(LAS u32 *)(L + L_E) = E;
    if (TOUCH_A == 1) touches();
  };
  const int rh = w & 1, kg = w >> 1;
  const u32 rb = 8 * xcd + 2 * rt + rh;
  const u32 row = 32 * rb + (lane & 31), col0 = 64 * cc + 32 * kg;
  u32x2 xres[4], b2v[4]; u32x4 sg0;
  auto epi_loads = [&]() {
    for (int g = 0; g < 4; ++g) {
      xres[g] = *(const GAS u32x2 *)(gx + row * XROW + (col0 + 8 * g + 4 * h) * 2);
      b2v[g] = *(const GAS u32x2 *)(gb2 + (col0 + 8 * g + 4 * h) * 2);
    }
    sg0 = ld128_sc1(rws, WS_STAT + row * 16, 0);
  };
  if (att) {
    if (tid < 16) *(LAS u32 *)(L + L_ZERO + 4 * tid) = 0;
    u32x4 idf[2];
    for (int s = 0; s < 2; ++s)
      for (int d = 0; d < 4; ++d) {
        const int k0 = 16 * s + 8 * (d >> 1) + 4 * h + 2 * (d & 1), n = lane & 31;
        idf[s][d] = (n == k0 ? 0x3F80u : 0u) | (n == k0 + 1 ? 0x3F800000u : 0u);
      }
    for (int s = 0; s < 2; ++s) asm volatile("" : "+v"(idf[s]));
    f32x16 acc[2][3]; u32x4 stat[2];
    const u32 vostat = WS_STAT + (256 * rga + 64 * w + (lane & 31)) * 16;
    gemmA_compute<true>(L, w, lane, acc, first, [&]() { if (TOUCH_A == 3) touches(); }, [&]() { for (int r = 0; r < 2; ++r) stat[r] = ld128_sc1(rws, vostat + 512 * r, 0); } STAMP_PASS);
    RowStat st[2];
    for (int r = 0; r < 2; ++r) {
      st[r] = row_stats(stat_settle(stat[r], rws, vostat + 512 * r, E), h);
      *(LAS float *)(L + L_TOK + w * 256 + r * 128 + 4 * (lane & 31)) = st[r].rstd;
    }
    BAR();
    STAMP(3);
    {
      const u32x4 wk = waug(cs_read(L, 0, lane), 0), wv = waug(cs_read(L, 1, lane), 0), wq = waug(cs_read(L, 2, lane), 0);
      for (int r = 0; r < 2; ++r) { acc[r][0] = mfma(wk, st[r].xaug, acc[r][0]); acc[r][1] = mfma(st[r].xaug, wv, acc[r][1]); acc[r][2] = mfma(wq, st[r].xaug, acc[r][2]); }
    }
    u32x4 wp[2];
    {
      const int blk = (lane >> 4) & 1, q = (lane & 15) >> 2, p = lane & 3;
      const u32 col = (blk && p == 3) ? 24 : 16 * blk + 4 * p;
      for (int s = 0; s < 2; ++s) {
        u32x2 r[2];
        for (int t = 0; t < 2; ++t) {
          const int dp = 16 * s + 8 * t + 4 * h + q;
          const u32 a = (dp >= 24 && dp < 28) ? L_ZERO + 8 * p : L_WPHI + (dp >= 28 ? dp - 4 : dp) * 56 + col * 2;
          r[t] = trd(L + a);
        }
        wp[s] = u32x4{r[0].x, r[0].y, r[1].x, r[1].y};
      }
    }
    f32x16 kvp[2] = {zero16(), zero16()};
    {
      f32x16 P[2];
      for (int r = 0; r < 2; ++r) P[r] = mfma(wp[0], pack8(acc[r][0], 0), zero16());
      for (int r = 0; r < 2; ++r) P[r] = mfma(wp[1], pack8(acc[r][0], 1), P[r]);
      u32x4 vf[2][2];
      for (int r = 0; r < 2; ++r) {
        f32x16 v = acc[r][1];
        for (int g = 0; g < 4; ++g) {
          const f32x4 rs = *(LAS f32x4 *)(L + L_TOK + w * 256 + r * 128 + (8 * g + 4 * h) * 4);
          for (int e = 0; e < 4; e += 2) { const f32x2 t2 = f32x2{v[4 * g + e], v[4 * g + e + 1]} * f32x2{rs[e], rs[e + 1]}; v[4 * g + e] = t2.x; v[4 * g + e + 1] = t2.y; }
        }
        for (int s = 0; s < 2; ++s) { vf[r][s] = pack8(v, s); if ((lane & 31) == 24) vf[r][s] = u32x4{0x3F803F80u, 0x3F803F80u, 0x3F803F80u, 0x3F803F80u}; }
      }
      u32x4 pf[2][2][2];
      for (int r = 0; r < 2; ++r) feature_map(P[r], st[r].rstd, h, pf[r]);
      f32x16 Z[2][2];
      for (int r = 0; r < 2; ++r) for (int t = 0; t < 2; ++t) Z[r][t] = mfma(pf[r][t][0], idf[0], zero16());
      for (int r = 0; r < 2; ++r) for (int t = 0; t < 2; ++t) Z[r][t] = mfma(pf[r][t][1], idf[1], Z[r][t]);
      for (int r = 0; r < 2; ++r) for (int s = 0; s < 2; ++s) for (int t = 0; t < 2; ++t) kvp[t] = mfma(pack8(Z[r][t], s), vf[r][s], kvp[t]);
    }
    for (int t = 0; t < 2; ++t)
      for (int g = 0; g < 4; ++g)
        *(LAS f32x4 *)(L + w * 8192 + (t * 4 + g) * 1024 + lane * 16) = f32x4{kvp[t][4 * g], kvp[t][4 * g + 1], kvp[t][4 * g + 2], kvp[t][4 * g + 3]};
    STAMP(4);
    BAR();
    u32x4 pq[2][2][2];
    {
      f32x16 P[2];
      for (int r = 0; r < 2; ++r) P[r] = mfma(wp[0], pack8(acc[r][2], 0), zero16());
      for (int r = 0; r < 2; ++r) P[r] = mfma(wp[1], pack8(acc[r][2], 1), P[r]);
      for (int r = 0; r < 2; ++r) {
        feature_map(P[r], st[r].rstd, h, pq[r]);
        for (int t = 0; t < 2; ++t) for (int s = 0; s < 2; ++s) asm volatile("" : "+v"(pq[r][t][s]));
      }
    }
    if (has_tile) epi_loads();
    else { for (int g = 0; g < 4; ++g) { xres[g] = u32x2{0, 0}; b2v[g] = u32x2{0, 0}; } sg0 = u32x4{0, 0, 0, 0}; }
    STAMP(6);
    BAR();
    STAMP(8);
    {
      u32x4 kf[2][2];
      for (int t = 0; t < 2; ++t) for (int s = 0; s < 2; ++s) kf[t][s] = *(LAS u32x4 *)(L + L_KVF + ((t * 2 + s) * 64 + lane) * 16);
      for (int r = 0; r < 2; ++r) {
        f32x16 num = zero16();
        for (int t = 0; t < 2; ++t) for (int s = 0; s < 2; ++s) num = mfma(kf[t][s], pq[r][t][s], num);
        const float rc = __builtin_amdgcn_rcpf(both_halves(num[12]).x + 1e-6f);
        for (int i = 0; i < 16; ++i) num[i] *= rc;
        if (!h) { num[12] = 0.f; num[13] = 0.f; num[14] = 0.f; num[15] = 0.f; }
        const u32 so = WS_O + ((8 * rga + 2 * w + r) * 32 + 2 * head) * 1024;
        st128_sc1(pack8(num, 0), rws, lane * 16, so);
        st128_sc1(pack8(num, 1), rws, lane * 16, so + 1024);
      }
    }
    vmwait<0>();
    if (lane < 2) st_flag(gws, WS_OF + ((8 * rga + 2 * w + lane) * 16 + head) * 64, E);
    STAMP(9);
    if (!has_tile) { STAMP_OUT(); return; }
  } else if (ffn) {
    u32 bias[4];
    for (int t = 0; t < 4; ++t) bias[t] = *(const GAS unsigned short *)(gb1 + (128 * ct + 32 * t + (lane & 31)) * 2);
    f32x16 acc[2][4]; u32x4 stat[2];
    const u32 vostat = WS_STAT + (256 * rga + 64 * w + (lane & 31)) * 16;
    gemmA_compute<false>(L, w, lane, acc, first, [&]() { if (TOUCH_A == 3) touches(); }, [&]() { for (int r = 0; r < 2; ++r) stat[r] = ld128_sc1(rws, vostat + 512 * r, 0); } STAMP_PASS);
    RowStat st[2];
    for (int r = 0; r < 2; ++r) st[r] = row_stats(stat_settle(stat[r], rws, vostat + 512 * r, E), h);
    BAR();
    STAMP(3);
    epi_loads();
    for (int t = 0; t < 4; ++t) {
      const u32x4 wa = waug(cs_read(L, t, lane), bias[t] | (bias[t] << 16));
      for (int r = 0; r < 2; ++r) {
        f32x16 a = mfma(wa, st[r].xaug, acc[r][t]);
        for (int i = 0; i < 16; i += 2) { f32x2 v = {relu(a[i]), relu(a[i + 1])}; v *= v; a[i] = v.x; a[i + 1] = v.y; }
        const u32 so = WS_H + ((8 * rga + 2 * w + r) * 112 + 8 * ct + 2 * t) * 1024;
        st128_sc1(pack8(a, 0), rws, lane * 16, so);
        st128_sc1(pack8(a, 1), rws, lane * 16, so + 1024);
      }
    }
    STAMP(4);
    vmwait<0>();
    if (lane < 2) st_flag(gws, WS_HF + ((8 * rga + 2 * w + lane) * 14 + ct) * 64, E);
    STAMP(5);
  } else {
    first();
    BAR();
    epi_loads();
  }

  {
    constexpr int RD = RING;
    const u32 soh = WS_H + (rb * 112 + 56 * kg) * 1024, soo = WS_O + (rb * 32 + 16 * kg) * 1024;
    u32x4 ring[RD];
    const float sig = asf(stat_settle(sg0, rws, WS_STAT + row * 16, E)[2]);
    const float rs1 = __builtin_amdgcn_rcpf(sig), rs2 = rs1 * rs1;
    f32x2 bx[8], xx[8];
    for (int g = 0; g < 4; ++g) for (int e = 0; e < 2; ++e) {
      bx[2 * g + e] = f32x2{bf2f(b2v[g][e]), bfhi(b2v[g][e])}; xx[2 * g + e] = f32x2{bf2f(xres[g][e]), bfhi(xres[g][e])};
      asm volatile("" : "+v"(bx[2 * g + e]), "+v"(xx[2 * g + e]));
    }
    const int blk = (lane >> 4) & 1, q = (lane & 15) >> 2, p = lane & 3;
    u32 bb0[2], bb[2];
    for (int c2 = 0; c2 < 2; ++c2) bb0[c2] = (u32)(size_t)L + kg * 8192 + (4 * h + q) * 128 + ((c2 ^ kg ^ (q >> 1)) * 64) + 32 * blk + 8 * p;
    f32x16 af[2] = {zero16(), zero16()}, aa[2] = {zero16(), zero16()};
    u32x4 bf[2][2];
    sfor<0, 72>([&](auto I) {
      constexpr int i = I;
      if constexpr ((i & 3) == 0) asm volatile("s_barrier" ::: "memory");
      if constexpr (i == 0) STAMP(11);
      if constexpr (i == 40) STAMP(14);
      if constexpr (i == 0) {
        for (int j = 0; j < RD; ++j) ring[j] = ld128_sc1(rws, lane * 16, soh + j * 1024);
        __builtin_amdgcn_sched_barrier(0);
        for (int c2 = 0; c2 < 2; ++c2) { bb[c2] = bb0[c2] + 4 * L_SLOT; asm volatile("" : "+v"(bb[c2])); bf[0][c2] = trfrag((lchar *)(size_t)bb[c2], 1024); }
      }
      u32x4 a = ring[i % RD];
      if constexpr (i + 1 < 72) {
        constexpr int n = i + 1;
        if constexpr ((n & 3) == 0) for (int c2 = 0; c2 < 2; ++c2) { bb[c2] = bb0[c2] + (((n >> 2) + 4) % NSLOT) * L_SLOT; asm volatile("" : "+v"(bb[c2])); }
        for (int c2 = 0; c2 < 2; ++c2) bf[n & 1][c2] = trfrag((lchar *)(size_t)bb[c2] + (n & 3) * 2048, 1024);
      }
      if constexpr (i < 56) { af[0] = mfma(bf[i & 1][0], a, af[0]); af[1] = mfma(bf[i & 1][1], a, af[1]); asm volatile("" : "+v"(af[0]), "+v"(af[1])); }
      else { aa[0] = mfma(bf[i & 1][0], a, aa[0]); aa[1] = mfma(bf[i & 1][1], a, aa[1]); asm volatile("" : "+v"(aa[0]), "+v"(aa[1])); }
      if constexpr (i + RD < 56) ring[i % RD] = ld128_sc1(rws, lane * 16, soh + (i + RD) * 1024);
      else if constexpr (i + RD < 72) ring[i % RD] = ld128_sc1(rws, lane * 16, soo + (i + RD - 56) * 1024);
      __builtin_amdgcn_sched_barrier(0);
    });
    STAMP(12);
    lchar *X = L + 4 * L_SLOT;
    {
      const f32x16 &sf = af[1], &sa = aa[1];
      for (int g = 0; g < 4; ++g) {
        *(LAS f32x4 *)(X + w * 8192 + g * 1024 + lane * 16) = f32x4{sf[4 * g], sf[4 * g + 1], sf[4 * g + 2], sf[4 * g + 3]};
        *(LAS f32x4 *)(X + w * 8192 + 4096 + g * 1024 + lane * 16) = f32x4{sa[4 * g], sa[4 * g + 1], sa[4 * g + 2], sa[4 * g + 3]};
      }
    }
    BAR();
    const f32x16 &yf = af[0], &ya = aa[0];
    u32x2 o2[4];
    for (int g = 0; g < 4; ++g) {
      const f32x4 pf = *(LAS f32x4 *)(X + (w ^ 2) * 8192 + g * 1024 + lane * 16), pa = *(LAS f32x4 *)(X + (w ^ 2) * 8192 + 4096 + g * 1024 + lane * 16);
      for (int e = 0; e < 2; ++e) {
        const int i = 4 * g + 2 * e;
        const f32x2 y = (f32x2{yf[i], yf[i + 1]} + f32x2{pf[2 * e], pf[2 * e + 1]}) * rs2 + bx[2 * g + e];
        const f32x2 t = y * -LOG2E;
        const f32x2 d = f32x2{__builtin_amdgcn_exp2f(t.x), __builtin_amdgcn_exp2f(t.y)} + 1.0f;
        const f32x2 sg = y * f32x2{__builtin_amdgcn_rcpf(d.x), __builtin_amdgcn_rcpf(d.y)};
        const f32x2 r = (f32x2{ya[i], ya[i + 1]} + f32x2{pa[2 * e], pa[2 * e + 1]}) * sg + xx[2 * g + e];
        o2[g][e] = pk2(r.x, r.y);
      }
    }
    const rsrc_t rout = __builtin_amdgcn_make_buffer_rsrc((void *)gout, 0, 2048 * XROW, 0x00020000);
    for (int g = 0; g < 4; g += 2) {
      auto rx0 = __builtin_amdgcn_permlane32_swap(o2[g].x, o2[g + 1].x, false, false);
      auto ry0 = __builtin_amdgcn_permlane32_swap(o2[g].y, o2[g + 1].y, false, false);
      __builtin_amdgcn_raw_buffer_store_b128(u32x4{rx0[0], ry0[0], rx0[1], ry0[1]}, rout, row * XROW + (col0 + 8 * (g + h)) * 2, 0, OUT_AUX);
    }
    STAMP(13);
    STAMP(15);
    STAMP_OUT();
  }
}

extern "C" void kernel_launch(void *const *d_in, const int *in_sizes, int n_in, void *d_out, int out_size, void *d_ws, size_t ws_size, hipStream_t stream) {
  fused_hedgehog_ffn<<<256, 512, 0, stream>>>((const char *)d_in[0], (const char *)d_in[1], (const char *)d_in[2], (const char *)d_in[3], (const char *)d_in[5],
                                              (const char *)d_in[6], (const char *)d_in[7], (const char *)d_in[8], (char *)d_out, (char *)d_ws);
}
```

```cpp
#include <hip/hip_runtime.h>
#include <hip/hip_bf16.h>
#include <stdint.h>
#include <type_traits>

typedef unsigned u32;
typedef u32 u32x4 __attribute__((ext_vector_type(4)));
typedef u32 u32x2 __attribute__((ext_vector_type(2)));
typedef float f32x16 __attribute__((ext_vector_type(16)));
typedef float f32x4 __attribute__((ext_vector_type(4)));
typedef float f32x2 __attribute__((ext_vector_type(2)));
typedef short s16x8 __attribute__((ext_vector_type(8)));
typedef short s16x4 __attribute__((ext_vector_type(4)));
typedef __bf16 bf16x2 __attribute__((ext_vector_type(2)));
typedef __bf16 bf16x8 __attribute__((ext_vector_type(8)));
#define LAS __attribute__((address_space(3)))
#define GAS __attribute__((address_space(1)))
#define DI __device__ __forceinline__
typedef __amdgpu_buffer_rsrc_t rsrc_t;
typedef LAS char lchar;

namespace {
constexpr int DM = 448, DF = 1792;
constexpr u32 XROW = DM * 2;
constexpr u32 QKVROW = 3 * DM * 2;
constexpr u32 W1ROW = DF * 2;
constexpr u32 WS_EPOCH = 0;
constexpr u32 WS_STAT = 4096;
constexpr u32 WS_KF = 65536;
constexpr u32 WS_HF = 131072;
constexpr u32 WS_OF = 196608;
constexpr u32 WS_KVP = 262144;
constexpr u32 WS_H = WS_KVP + (1u << 20);
constexpr u32 WS_O = WS_H + 64 * 112 * 1024;
constexpr u32 WS_END = WS_O + 64 * 32 * 1024;
constexpr u32 L_SLOT = 16384, NSLOT = 9;
constexpr u32 A_SLOT = 49152, A_WOFF = 32768;
constexpr u32 L_MISC = NSLOT * L_SLOT;
constexpr u32 L_WPHI = L_MISC;
constexpr u32 L_ZERO = L_MISC + 2048;
constexpr u32 L_E = L_MISC + 2112;
constexpr u32 L_DUMMY = L_MISC + 2304;
constexpr u32 L_CSP = L_MISC + 2560;
constexpr u32 L_TOK = L_CSP + 512;
constexpr u32 L_KVF = L_TOK + 1024;
constexpr u32 L_END = L_KVF + 4096;
constexpr u32 LDS_BYTES = 163840;
static_assert(L_END <= LDS_BYTES, "lds");
constexpr int SPIN_CAP = 1 << 16;
constexpr float LOG2E = 1.4426950408889634f;
#define CS_DOT2 0
#define SGB 1
#define LPRIO 0
#define FIRST_AT -1
#define TOUCH_A 1
#define POLL_SLEEP 1
#define OUT_AUX 0
#define SPREAD 0
#define PIN_C 0
#define RING 12

#define STAMP(i) do {} while (0)
#define STAMP_DECL do {} while (0)
#define STAMP_OUT() do {} while (0)
#define STAMP_ARG
#define STAMP_PASS
template <int B, int E, class F> DI void sfor(F &&f) {
  if constexpr (B < E) { f(std::integral_constant<int, B>{}); sfor<B + 1, E>(f); }
}
DI u32 rfl(u32 v) { return __builtin_amdgcn_readfirstlane(v); }
DI f32x16 mfma(u32x4 a, u32x4 b, f32x16 c) {
  return __builtin_amdgcn_mfma_f32_32x32x16_bf16(__builtin_bit_cast(bf16x8, a), __builtin_bit_cast(bf16x8, b), c, 0, 0, 0);
}
DI u32 pk2(float lo, float hi) { f32x2 p = {lo, hi}; return __builtin_bit_cast(u32, __builtin_convertvector(p, bf16x2)); }
DI float dot2(u32 a, u32 b, float c) { return __builtin_amdgcn_fdot2_f32_bf16(__builtin_bit_cast(bf16x2, a), __builtin_bit_cast(bf16x2, b), c, false); }
DI float bf2f(u32 lo16) { return __builtin_bit_cast(float, lo16 << 16); }
DI float bfhi(u32 v) { return __builtin_bit_cast(float, v & 0xffff0000u); }
DI float asf(u32 v) { return __builtin_bit_cast(float, v); }
DI f32x2 both_halves(float v) {
  const u32 a = __builtin_bit_cast(u32, v);
  auto r = __builtin_amdgcn_permlane32_swap(a, a, false, false);
  return f32x2{asf(r[0]), asf(r[1])};
}
DI float half_lo(float v) { return both_halves(v).x; }
DI u32x4 pack8(const f32x16 &x, int s) { return u32x4{pk2(x[8 * s], x[8 * s + 1]), pk2(x[8 * s + 2], x[8 * s + 3]), pk2(x[8 * s + 4], x[8 * s + 5]), pk2(x[8 * s + 6], x[8 * s + 7])}; }
DI u32x2 trd(lchar *p) {
  typedef short v4i16 __attribute__((ext_vector_type(4)));
  return __builtin_bit_cast(u32x2, __builtin_amdgcn_ds_read_tr16_b64_v4i16((LAS v4i16 *)p));
}
DI u32x4 trfrag(lchar *p, int tstride) { u32x2 a = trd(p), b = trd(p + tstride); return u32x4{a.x, a.y, b.x, b.y}; }
DI float relu(float v) { return __builtin_amdgcn_fmed3f(v, 0.f, __builtin_inff()); }
DI f32x16 zero16() { f32x16 z; for (int i = 0; i < 16; ++i) z[i] = 0.f; return z; }
#define BAR() asm volatile("s_waitcnt lgkmcnt(0)\n\ts_barrier" ::: "memory")
template <int N> DI void vmwait() { asm volatile("s_waitcnt vmcnt(%0)" ::"i"(N) : "memory"); }
DI void dma16(rsrc_t r, u32 voff, u32 soff, u32 ldsaddr) {
  asm volatile("s_mov_b32 m0, %0\n\ts_nop 0\n\tbuffer_load_dwordx4 %1, %2, %3 offen lds" ::"s"(ldsaddr), "v"(voff), "s"(r), "s"(soff) : "memory", "m0");
}
template <int OFF> DI void ald128(u32x4 &d, rsrc_t r, u32 voff, u32 soff) {
  asm volatile("buffer_load_dwordx4 %0, %1, %2, %3 offen offset:%4" : "=v"(d) : "v"(voff), "s"(r), "s"(soff), "i"(OFF) : "memory");
}
DI u32x4 ld128_sc1(rsrc_t r, u32 voff, u32 soff) { return __builtin_amdgcn_raw_buffer_load_b128(r, voff, soff, 16); }
DI void st128_sc1(u32x4 v, rsrc_t r, u32 voff, u32 soff) {
  __builtin_amdgcn_raw_buffer_store_b128(v, r, voff, soff, 16);
  asm volatile("s_nop 1" ::"v"(v) : "memory");
}
typedef GAS u32 gu32;
DI u32 ld_flag(const char *ws, u32 off) { return __hip_atomic_load((gu32 *)(ws + off), __ATOMIC_RELAXED, __HIP_MEMORY_SCOPE_AGENT); }
DI void st_flag(char *ws, u32 off, u32 v) { __hip_atomic_store((gu32 *)(ws + off), v, __ATOMIC_RELAXED, __HIP_MEMORY_SCOPE_AGENT); }
DI void poll_all(const char *ws, u32 off, u32 E) {
#pragma unroll 1
  for (int spin = 0; spin < SPIN_CAP; ++spin) {
    if (__all(ld_flag(ws, off) == E)) break;
    __builtin_amdgcn_s_sleep(POLL_SLEEP);
  }
  __builtin_amdgcn_fence(__ATOMIC_ACQUIRE, "wavefront");
}
template <u32 LI, u32 SI> DI void dmai(rsrc_t r, u32 voff, u32 sob, u32 ldsb) {
  u32 t;
  asm volatile("s_add_u32 m0, %2, %5\n\ts_add_u32 %0, %1, %6\n\tbuffer_load_dwordx4 %3, %4, %0 offen lds"
               : "=&s"(t) : "s"(sob), "s"(ldsb), "v"(voff), "s"(r), "i"(LI), "i"(SI) : "memory", "m0", "scc");
}
template <int CTRL, int RMASK> DI float dpp_add(float v) {
  return v + asf(__builtin_amdgcn_update_dpp(0u, __builtin_bit_cast(u32, v), CTRL, RMASK, 0xf, true));
}
DI float wave_sum63(float v) {
  v = dpp_add<0x111, 0xf>(v); v = dpp_add<0x112, 0xf>(v); v = dpp_add<0x114, 0xf>(v); v = dpp_add<0x118, 0xf>(v);
  v = dpp_add<0x142, 0xa>(v); v = dpp_add<0x143, 0xc>(v);
  return v;
}
DI void st64_sc1(char *ws, u32 off, u32 lo, u32 hi) {
  __hip_atomic_store((GAS unsigned long long *)(ws + off), (unsigned long long)lo | ((unsigned long long)hi << 32), __ATOMIC_RELAXED, __HIP_MEMORY_SCOPE_AGENT);
}
struct Early { u32 ev; u32x4 xa, xb; };
DI Early early_issue(char *ws, rsrc_t rx, int row0, int lane) {
  Early e;
  e.ev = ld_flag(ws, WS_EPOCH);
  const u32 vo = lane < 56 ? (u32)lane * 16 : 0x7ffffff0u;
  e.xa = __builtin_amdgcn_raw_buffer_load_b128(rx, vo, row0 * XROW, 0); e.xb = __builtin_amdgcn_raw_buffer_load_b128(rx, vo, (row0 + 1) * XROW, 0);
  return e;
}
DI u32 early_finish(const Early &e, rsrc_t rws, int row0, int lane) {
  const u32 E = rfl(e.ev) + 1, ONES = 0x3F803F80u;
  float s[2], q[2];
  for (int r = 0; r < 2; ++r) {
    const u32x4 &x = r ? e.xb : e.xa;
    s[r] = dot2(x[0], ONES, 0.f); q[r] = dot2(x[0], x[0], 0.f);
    for (int d = 1; d < 4; ++d) { s[r] = dot2(x[d], ONES, s[r]); q[r] = dot2(x[d], x[d], q[r]); }
    s[r] = wave_sum63(s[r]); q[r] = wave_sum63(q[r]);
    const float mean = s[r] * (1.0f / DM);
    const float sig = __builtin_amdgcn_sqrtf(fmaxf(q[r] * (1.0f / DM) - mean * mean, 0.f) + 1e-5f);
    const u32 vo = lane == 63 ? WS_STAT + (row0 + r) * 16 : 0x7ffffff0u;
    __builtin_amdgcn_raw_buffer_store_b64(u32x2{__builtin_bit_cast(u32, mean), E}, rws, vo, 0, 16);
    __builtin_amdgcn_raw_buffer_store_b64(u32x2{__builtin_bit_cast(u32, sig), E}, rws, vo + 8, 0, 16);
  }
  return E;
}
DI u32x4 stat_settle(u32x4 g, rsrc_t rws, u32 vostat, u32 E) {
#pragma unroll 1
  for (int spin = 0; spin < SPIN_CAP && !__all(g[1] == E && g[3] == E); ++spin) { __builtin_amdgcn_s_sleep(1); g = ld128_sc1(rws, vostat, 0); }
  return g;
}
struct RowStat { float rstd; u32x4 xaug; };
DI RowStat row_stats(u32x4 g, int h) {
  const float m = -asf(g[0]), sig = asf(g[2]);
  RowStat r; r.rstd = __builtin_amdgcn_rcpf(sig);
  const float ml = m - bfhi(pk2(m, m)), sl = sig - bfhi(pk2(sig, sig));
  const u32 mm = pk2(m, ml), ss = pk2(sig, sl);
  r.xaug = h ? u32x4{0, 0, 0, 0} : u32x4{mm, mm, ss, 0};
  return r;
}
DI u32x4 waug(float cs, u32 bb) {
  const u32 hh = pk2(cs, cs); const float cl = cs - bfhi(hh);
  return u32x4{hh, pk2(cl, cl), bb, 0};
}
DI float cs_read(lchar *L, int t, int lane) { return *(LAS float *)(L + L_CSP + (t * 32 + (lane & 31)) * 4); }

DI void touch(rsrc_t r, u32 voff, u32 lb) {
  asm volatile("s_mov_b32 m0, %0\n\ts_nop 0\n\tbuffer_load_dword %1, %2, 0 offen lds" ::"s"(lb + L_DUMMY), "v"(voff), "s"(r) : "memory", "m0");
}
template <bool ATT, class First, class After0, class Mid>
DI void gemmA_compute(lchar *L, int w, int lane, f32x16 (&acc)[2][ATT ? 3 : 4], First &&first, After0 &&after0, Mid &&mid STAMP_ARG) {
  constexpr int T = ATT ? 3 : 4, ROWB = ATT ? 64 : 256;
  const int h = lane >> 5, blk = (lane >> 4) & 1, q = (lane & 15) >> 2, p = lane & 3;
  const u32 lb = (u32)(size_t)L;
  u32 wb0[T], xb0[4], wb[T], xb[4], cb0, cb;
  for (int t = 0; t < T; ++t) wb0[t] = lb + A_WOFF + (ATT ? t * 4096 + (8 * h + q) * 64 : (8 * h + q) * 256 + 64 * (t ^ q)) + 32 * blk + 8 * p;
  for (int u = 0; u < 4; ++u) xb0[u] = lb + w * 8192 + (lane & 31) * 128 + 16 * ((2 * u + h) ^ ((lane >> 1) & 7));
  const int tw = w < T ? w : T - 1;
  cb0 = lb + A_WOFF + (ATT ? tw * 4096 + (8 * h + q) * 64 : (8 * h + q) * 256 + 64 * (tw ^ q)) + 32 * blk + 8 * p;
  f32x2 ca = {0.f, 0.f};
  for (int r = 0; r < 2; ++r) for (int t = 0; t < T; ++t) acc[r][t] = zero16();
  u32x4 xf[2][2], wf[2][T], cf[2];
  sfor<0, 29>([&](auto I) {
    constexpr int i = I, c = i >> 2, u = i & 3;
    if constexpr (i == 0 && FIRST_AT < 0) { first(); STAMP(14); }
    if constexpr (u == 0 && i < 28) {
      BAR();
      if constexpr (c == 0) STAMP(1);
      constexpr u32 so = (c % 3) * A_SLOT;
      for (int t = 0; t < T; ++t) { wb[t] = wb0[t] + so; asm volatile("" : "+v"(wb[t])); }
      for (int j = 0; j < 4; ++j) { xb[j] = xb0[j] + so; asm volatile("" : "+v"(xb[j])); }
      cb = cb0 + so; asm volatile("" : "+v"(cb));
    }
    if constexpr (i < 28) {
      cf[i & 1] = trfrag((lchar *)(size_t)cb + 16 * u * ROWB, 4 * ROWB);
      for (int r = 0; r < 2; ++r) xf[i & 1][r] = *(LAS u32x4 *)((lchar *)(size_t)xb[u] + r * 4096);
      for (int t = 0; t < T; ++t) wf[i & 1][t] = trfrag((lchar *)(size_t)wb[t] + 16 * u * ROWB, 4 * ROWB);
    }
    if constexpr (i == FIRST_AT) first();
    if constexpr (i == 1) after0();
    if constexpr (i == 20) mid();
    if constexpr (i > 0) {
      constexpr int j = (i - 1) & 1;
      for (int t = 0; t < T; ++t)
        for (int r = 0; r < 2; ++r) acc[r][t] = (ATT && t == 1) ? mfma(xf[j][r], wf[j][t], acc[r][t]) : mfma(wf[j][t], xf[j][r], acc[r][t]);
      for (int d = 0; d < 4; ++d) ca += f32x2{bf2f(cf[j][d]), bfhi(cf[j][d])};
      asm volatile("" : "+v"(ca));
      for (int m = 0; m < 2 * T; ++m) { __builtin_amdgcn_sched_group_barrier(0x8, 1, 0); __builtin_amdgcn_sched_group_barrier(0x100, 2, 0); __builtin_amdgcn_sched_group_barrier(0x2, 2, 0); }
    }
    __builtin_amdgcn_sched_barrier(0);
  });
  { const f32x2 a = both_halves(ca.x + ca.y); if (lane < 32 && w < T) *(LAS float *)(L + L_CSP + (w * 32 + lane) * 4) = a.x + a.y; }
  STAMP(2);
}
struct StageCtx { rsrc_t rw2, rwo; u32 vob, so2, soo, ld; };
template <int ST> DI void stage_dma(const StageCtx &c) {
  sfor<0, 4>([&](auto I) {
    constexpr u32 i = I, dst = ((ST + 4) % NSLOT) * L_SLOT + i * 1024;
    dmai<dst, (64 * ST + 8 * i) * XROW>(c.rw2, c.vob, c.so2, c.ld);
  });
}
template <int ST> DI void stage_load(const StageCtx &c, u32x4 (&br)[36]) {
  sfor<0, 4>([&](auto I) {
    constexpr u32 i = I;
    if constexpr (ST < 14) br[4 * (ST - 9) + i] = __builtin_amdgcn_raw_buffer_load_b128(c.rw2, c.vob, c.so2 + (64 * ST + 8 * i) * XROW, 0);
    else br[4 * (ST - 9) + i] = __builtin_amdgcn_raw_buffer_load_b128(c.rwo, c.vob, c.soo + (56 * (ST - 14) + (i == 3 ? 20 : 8 * i)) * XROW, 0);
  });
}
template <bool ATT>
DI void gemmA_loader(lchar *L, rsrc_t rx, rsrc_t rw, u32 sox, u32 sow, int lw, int lane, const StageCtx &sc, u32x4 (&br)[36] STAMP_ARG) {
  constexpr int NW = ATT ? 3 : 4, N = 8 + NW;
  const u32 lb = (u32)(size_t)L;
  const u32 vox0 = (lane >> 3) * XROW + 16 * ((lane & 7) ^ ((lane >> 4) & 3)), vox1 = vox0 ^ 64;
  const u32 ldx = rfl(lb + lw * 8192);
  const u32 vow = ATT ? (lane >> 2) * QKVROW + ((lane & 3) == 3 ? 40 : 16 * (lane & 3)) : (lane >> 4) * W1ROW + 16 * ((lane & 15) ^ ((lane >> 4) << 2));
  const u32 swb = rfl(sow + 16 * lw * (ATT ? QKVROW : W1ROW)), ldw = rfl(lb + A_WOFF + lw * (ATT ? 1024 : 4096));
  auto issue = [&](auto C) {
    constexpr u32 c = C, so = (c % 3) * A_SLOT;
    sfor<0, NW>([&](auto I) {
      constexpr u32 i = I;
      if constexpr (ATT) dmai<so + i * 4096, 64 * c * QKVROW + (i == 0 ? 896 : i == 1 ? 1792 : 0)>(rw, vow, swb, ldw);
      else dmai<so + i * 1024, (64 * c + 4 * i) * W1ROW>(rw, vow, swb, ldw);
    });
    sfor<0, 8>([&](auto I) { constexpr u32 i = I; dmai<so + i * 1024, 8 * i * XROW + 128 * c>(rx, (i & 1) ? vox1 : vox0, sox, ldx); });
  };
  issue(std::integral_constant<int, 0>{});
  issue(std::integral_constant<int, 1>{});
  sfor<0, 7>([&](auto C) {
    constexpr int c = C;
    vmwait<(c == 6 ? 12 : N)>();
    asm volatile("s_barrier" ::: "memory");
    if constexpr (c == 0) STAMP(1);
    if constexpr (c + 2 < 7) issue(std::integral_constant<int, c + 2>{});
    else { stage_load<9 + 3 * (c - 5)>(sc, br); stage_load<10 + 3 * (c - 5)>(sc, br); stage_load<11 + 3 * (c - 5)>(sc, br); }
  });
  STAMP(2);
}

DI void feature_map(const f32x16 &P, float rstd, int h, u32x4 (&pf)[2][2]) {
  const float c = rstd * LOG2E;
  float mx = fmaxf(fmaxf(P[0], P[1]), P[2]), mn = fminf(fminf(P[0], P[1]), P[2]);
  for (int i = 3; i < 15; i += 2) { mx = fmaxf(fmaxf(mx, P[i]), P[i + 1]); mn = fminf(fminf(mn, P[i]), P[i + 1]); }
  mx = fmaxf(mx, P[15]); mn = fminf(mn, P[15]);
  { f32x2 a = both_halves(mx), b = both_halves(mn); mx = fmaxf(a.x, a.y); mn = fminf(b.x, b.y); }
  const float mxc = -mx * c, mnc = mn * c;
  f32x16 ep, en;
  for (int i = 0; i < 16; i += 2) {
    const f32x2 p2 = {P[i], P[i + 1]}, ap = p2 * c + mxc, an = mnc - p2 * c;
    ep[i] = __builtin_amdgcn_exp2f(ap.x); ep[i + 1] = __builtin_amdgcn_exp2f(ap.y);
    en[i] = __builtin_amdgcn_exp2f(an.x); en[i + 1] = __builtin_amdgcn_exp2f(an.y);
  }
  f32x2 sp2 = {ep[0], ep[1]}, sn2 = {en[0], en[1]};
  for (int i = 2; i < 12; i += 2) { sp2 += f32x2{ep[i], ep[i + 1]}; sn2 += f32x2{en[i], en[i + 1]}; }
  const float keep = h ? 0.f : 1.f;
  sp2 += (f32x2{ep[12], ep[13]} + f32x2{ep[14], ep[15]}) * keep;
  sn2 += (f32x2{en[12], en[13]} + f32x2{en[14], en[15]}) * keep;
  const f32x2 a = both_halves(sp2.x + sp2.y), b = both_halves(sn2.x + sn2.y);
  const float rp = __builtin_amdgcn_rcpf(a.x + a.y), rn = __builtin_amdgcn_rcpf(b.x + b.y);
  for (int i = 0; i < 16; i += 2) {
    const f32x2 u = f32x2{ep[i], ep[i + 1]} * rp, v = f32x2{en[i], en[i + 1]} * rn;
    ep[i] = u.x; ep[i + 1] = u.y; en[i] = v.x; en[i + 1] = v.y;
  }
  pf[0][0] = pack8(ep, 0); pf[0][1] = pack8(ep, 1); pf[1][0] = pack8(en, 0); pf[1][1] = pack8(en, 1);
}

constexpr int refill_end(int s) { return s <= 0 ? 0 : (12 * s + 4) / 5 > 36 ? 36 : (12 * s + 4) / 5; }
constexpr bool refill_ok() { for (int s = 1; s <= 17; ++s) for (int p = refill_end(s - 1); p < refill_end(s); ++p) if (s < 1 + p / 4 || s > 7 + p / 4) return false; return refill_end(15) == 36; }
static_assert(refill_ok(), "refill schedule");
}

__global__ void __launch_bounds__(512, 2) fused_hedgehog_ffn(const char *__restrict__ gx, const char *__restrict__ gwqkv, const char *__restrict__ gwphi, const char *__restrict__ gwo,
                                                              const char *__restrict__ gw1, const char *__restrict__ gb1, const char *__restrict__ gw2, const char *__restrict__ gb2,
                                                              char *__restrict__ gout, char *__restrict__ gws) {
  __shared__ __attribute__((aligned(1024))) char smem[LDS_BYTES];
  lchar *L = (lchar *)smem;
  asm volatile("" ::"s"(gx), "s"(gwqkv), "s"(gwphi), "s"(gwo), "s"(gw1), "s"(gb1), "s"(gw2), "s"(gb2), "s"(gout), "s"(gws));
  const int tid = threadIdx.x, lane = tid & 63, w = rfl(tid >> 6);
  const int b = blockIdx.x, xcd = b & 7, slot = b >> 3;
  const int h = lane >> 5;
  STAMP_DECL; STAMP(0);
  const rsrc_t rx = __builtin_amdgcn_make_buffer_rsrc((void *)gx, 0, 2048 * XROW, 0x00020000);
  const rsrc_t rws = __builtin_amdgcn_make_buffer_rsrc((void *)gws, 0, WS_END, 0x00020000);
  const int tc = slot >= 16 ? slot - 16 : slot + 16;
  const bool has_tile = tc < 28;
  const u32 rt = tc / 7, cc = tc % 7;
  const bool att = slot < 16, ffn = !att && slot < 30;
  const int head = 8 * (xcd & 1) + (slot & 7);
  const int jf = slot - 16, ct = 7 * (xcd & 1) + (jf >= 7 ? jf - 7 : jf);
  const int rga = 2 * (xcd >> 1) + (att ? (slot >> 3) : (jf >= 7));

  if (w >= 4) {
    const int lw = w - 4;
    StageCtx sc;
    sc.rw2 = __builtin_amdgcn_make_buffer_rsrc((void *)gw2, 0, DF * XROW, 0x00020000);
    sc.rwo = __builtin_amdgcn_make_buffer_rsrc((void *)gwo, 0, DM * XROW, 0x00020000);
    sc.vob = (lane >> 3) * XROW + 16 * ((lane & 7) ^ (((lane >> 4) & 1) << 2));
    sc.so2 = rfl((896 * (lw >> 1) + 32 * (lw & 1)) * XROW + 128 * cc);
    sc.soo = rfl(28 * (8 * (lw >> 1) + (lw & 1)) * XROW + 128 * cc);
    sc.ld = rfl((u32)(size_t)L + (lw >> 1) * 8192 + (lw & 1) * 4096);
    u32 E;
    u32x4 br[36];
    const u32 hfoff = WS_HF + ((8 * xcd + 2 * rt) * 14 + (lane < 28 ? lane : 0)) * 64;
    if (att) {
      const rsrc_t rw = __builtin_amdgcn_make_buffer_rsrc((void *)gwqkv, 0, DM * QKVROW, 0x00020000);
      if (lw < 2) {
        const rsrc_t rp = __builtin_amdgcn_make_buffer_rsrc((void *)gwphi, 0, 16 * 28 * 28 * 2, 0x00020000);
        dma16(rp, lane * 16 + lw * 1024, head * 1568, (u32)(size_t)L + L_WPHI + lw * 1024);
      }
      gemmA_loader<true>(L, rx, rw, rfl((256 * rga + 64 * lw) * XROW), rfl(56 * head), lw, lane, sc, br STAMP_PASS);
      BAR();
      E = rfl(*(LAS u32 *)(L + L_E));
      if (has_tile) sfor<0, 5>([&](auto S) { stage_dma<S>(sc); });
      sfor<15, 18>([&](auto S) { stage_load<S>(sc, br); });
      BAR();
      __builtin_amdgcn_s_setprio(3);
      STAMP(4);
      for (int t = 0; t < 2; ++t) {
        f32x4 s = *(LAS f32x4 *)(L + (t * 4 + lw) * 1024 + lane * 16);
        for (int ww = 1; ww < 4; ++ww) s += *(LAS f32x4 *)(L + ww * 8192 + (t * 4 + lw) * 1024 + lane * 16);
        st128_sc1(__builtin_bit_cast(u32x4, s), rws, lane * 16, WS_KVP + (head * 8 + rga) * 8192 + (t * 4 + lw) * 1024);
      }
      vmwait<0>();
      if (lane == 0) st_flag(gws, WS_KF + ((head * 8 + rga) * 4 + lw) * 64, E);
      STAMP(5);
      poll_all(gws, WS_KF + ((head * 8 + (lane & 7)) * 4 + lw) * 64, E);
      STAMP(7);
      u32 hfl;
      hfl = 0;
      if (lw == 0) asm volatile("global_load_dword %0, %1, off sc1" : "+v"(hfl) : "v"(gws + hfoff) : "memory");
      u32x4 part[2][8];
      for (int t = 0; t < 2; ++t) for (int r = 0; r < 8; ++r) part[t][r] = ld128_sc1(rws, lane * 16, WS_KVP + (head * 8 + r) * 8192 + (t * 4 + lw) * 1024);
      for (int t = 0; t < 2; ++t) {
        f32x4 s = __builtin_bit_cast(f32x4, part[t][0]);
        for (int r = 1; r < 8; ++r) s += __builtin_bit_cast(f32x4, part[t][r]);
        u32x2 pk = {pk2(s.x, s.y), pk2(s.z, s.w)};
        if (lw == 3 && h) pk = u32x2{0, 0};
        *(LAS u32x2 *)(L + L_KVF + ((t * 2 + (lw >> 1)) * 64 + lane) * 16 + (lw & 1) * 8) = pk;
      }
      asm volatile("" : "+v"(hfl));
      BAR();
      __builtin_amdgcn_s_setprio(0);
      STAMP(8);
      if (!has_tile) {
        if (b == 8 * 12 && lw == 0) {
#pragma unroll 1
          for (int spin = 0; spin < SPIN_CAP; ++spin) {
            bool ok = true;
            for (int i = 0; i < 32; ++i) ok &= ld_flag(gws, WS_STAT + (64 * i + lane) * 16 + 4) == E;
            if (__all(ok)) break;
            __builtin_amdgcn_s_sleep(8);
          }
          if (lane == 0) st_flag(gws, WS_EPOCH, E);
        }
        STAMP_OUT();
        return;
      }
      sfor<5, 9>([&](auto S) { stage_dma<S>(sc); });
      if (lw == 0 && !__all(hfl == E)) poll_all(gws, hfoff, E);
    } else if (ffn) {
      const rsrc_t rw = __builtin_amdgcn_make_buffer_rsrc((void *)gw1, 0, DM * W1ROW, 0x00020000);
      gemmA_loader<false>(L, rx, rw, rfl((256 * rga + 64 * lw) * XROW), rfl(256 * ct), lw, lane, sc, br STAMP_PASS);
      BAR();
      E = rfl(*(LAS u32 *)(L + L_E));
      sfor<0, 9>([&](auto S) { stage_dma<S>(sc); });
      sfor<15, 18>([&](auto S) { stage_load<S>(sc, br); });
      vmwait<0>();
      if (lw == 0) poll_all(gws, hfoff, E);
    } else {
      sfor<0, 9>([&](auto S) { stage_dma<S>(sc); });
      sfor<9, 18>([&](auto S) { stage_load<S>(sc, br); });
      BAR();
      E = rfl(*(LAS u32 *)(L + L_E));
      vmwait<0>();
      if (lw == 0) poll_all(gws, hfoff, E);
    }
    STAMP(10);
    constexpr int SO = (56 - RING) / 4;
    u32 ofl = 0;
    const u32 ofoff = WS_OF + ((8 * xcd + 2 * rt + ((lane >> 4) & 1)) * 16 + (lane & 15)) * 64;
    const u32 ldv = sc.ld + lane * 16;
    sfor<0, 18>([&](auto S) {
      constexpr int s = S;
      if constexpr (s == SO) {
        vmwait<0>();
        asm volatile("" : "+v"(ofl));
        if (lw == 0 && !__all(ofl == E)) poll_all(gws, ofoff, E);
      }
      if constexpr (s == 4) vmwait<0>();
      BAR();
      sfor<refill_end(s - 1), refill_end(s)>([&](auto Pc) {
        constexpr int pc = Pc;
        *(LAS u32x4 *)((lchar *)(size_t)ldv + ((9 + pc / 4 + 4) % NSLOT) * L_SLOT + (pc & 3) * 1024) = br[pc];
      });
      if constexpr (s == SO - 4) { if (lw == 0) asm volatile("global_load_dword %0, %1, off sc1" : "+v"(ofl) : "v"(gws + ofoff) : "memory"); }
    });
    STAMP(12);
    asm volatile("s_barrier" ::: "memory");
    STAMP_OUT();
    return;
  }

  const Early early = early_issue(gws, rx, 8 * b + 2 * w, lane);
  u32 E;
  auto touches = [&]() {
    if (att || ffn) {
      constexpr u32 OOB = 0x7ffffff0u;
      const u32 lb = (u32)(size_t)L;
      { const u32 g15 = att ? (slot & 7) : 8 + (jf >= 7 ? jf - 7 : jf), i = (g15 * 4 + w) * 22 + lane;
        touch(rx, (lane < 22 && i < 1280) ? (256 * rga + (i & 255)) * XROW + 128 * (2 + (i >> 8)) : OOB, lb); }
      if (att) {
        const rsrc_t rq = __builtin_amdgcn_make_buffer_rsrc((void *)gwqkv, 0, DM * QKVROW, 0x00020000);
        const u32 i = slot * 240 + w * 60 + lane, row = (i * 43691u) >> 19, rem = i - 12 * row;
        touch(rq, lane < 60 ? (128 + row) * QKVROW + 896 * (rem >> 2) + ((448 * (xcd & 1)) & ~127u) + 128 * (rem & 3) : OOB, lb);
      } else {
        const rsrc_t r1 = __builtin_amdgcn_make_buffer_rsrc((void *)gw1, 0, DM * W1ROW, 0x00020000);
        const u32 l8 = 4 * (jf >= 7) + w, i = 64 * l8 + lane, j = 512 + 16 * l8 + lane;
        touch(r1, (128 + (i >> 1)) * W1ROW + 256 * ct + 128 * (i & 1), lb);
        touch(r1, lane < 16 ? (128 + (j >> 1)) * W1ROW + 256 * ct + 128 * (j & 1) : OOB, lb);
      }
    }
  };
  if (TOUCH_A == 2) touches();
  auto first = [&]() {
    E = early_finish(early, rws, 8 * b + 2 * w, lane); if (tid == 0) *(LAS u32 *)(L + L_E) = E;
    if (TOUCH_A == 1) touches();
  };
  const int rh = w & 1, kg = w >> 1;
  const u32 rb = 8 * xcd + 2 * rt + rh;
  const u32 row = 32 * rb + (lane & 31), col0 = 64 * cc + 32 * kg;
  u32x2 xres[4], b2v[4]; u32x4 sg0;
  auto epi_loads = [&]() {
    for (int g = 0; g < 4; ++g) {
      xres[g] = *(const GAS u32x2 *)(gx + row * XROW + (col0 + 8 * g + 4 * h) * 2);
      b2v[g] = *(const GAS u32x2 *)(gb2 + (col0 + 8 * g + 4 * h) * 2);
    }
    sg0 = ld128_sc1(rws, WS_STAT + row * 16, 0);
  };
  if (att) {
    if (tid < 16) *(LAS u32 *)(L + L_ZERO + 4 * tid) = 0;
    u32x4 idf[2];
    for (int s = 0; s < 2; ++s)
      for (int d = 0; d < 4; ++d) {
        const int k0 = 16 * s + 8 * (d >> 1) + 4 * h + 2 * (d & 1), n = lane & 31;
        idf[s][d] = (n == k0 ? 0x3F80u : 0u) | (n == k0 + 1 ? 0x3F800000u : 0u);
      }
    for (int s = 0; s < 2; ++s) asm volatile("" : "+v"(idf[s]));
    f32x16 acc[2][3]; u32x4 stat[2];
    const u32 vostat = WS_STAT + (256 * rga + 64 * w + (lane & 31)) * 16;
    gemmA_compute<true>(L, w, lane, acc, first, [&]() { if (TOUCH_A == 3) touches(); }, [&]() { for (int r = 0; r < 2; ++r) stat[r] = ld128_sc1(rws, vostat + 512 * r, 0); } STAMP_PASS);
    RowStat st[2];
    for (int r = 0; r < 2; ++r) {
      st[r] = row_stats(stat_settle(stat[r], rws, vostat + 512 * r, E), h);
      *(LAS float *)(L + L_TOK + w * 256 + r * 128 + 4 * (lane & 31)) = st[r].rstd;
    }
    BAR();
    STAMP(3);
    {
      const u32x4 wk = waug(cs_read(L, 0, lane), 0), wv = waug(cs_read(L, 1, lane), 0), wq = waug(cs_read(L, 2, lane), 0);
      for (int r = 0; r < 2; ++r) { acc[r][0] = mfma(wk, st[r].xaug, acc[r][0]); acc[r][1] = mfma(st[r].xaug, wv, acc[r][1]); acc[r][2] = mfma(wq, st[r].xaug, acc[r][2]); }
    }
    u32x4 wp[2];
    {
      const int blk = (lane >> 4) & 1, q = (lane & 15) >> 2, p = lane & 3;
      const u32 col = (blk && p == 3) ? 24 : 16 * blk + 4 * p;
      for (int s = 0; s < 2; ++s) {
        u32x2 r[2];
        for (int t = 0; t < 2; ++t) {
          const int dp = 16 * s + 8 * t + 4 * h + q;
          const u32 a = (dp >= 24 && dp < 28) ? L_ZERO + 8 * p : L_WPHI + (dp >= 28 ? dp - 4 : dp) * 56 + col * 2;
          r[t] = trd(L + a);
        }
        wp[s] = u32x4{r[0].x, r[0].y, r[1].x, r[1].y};
      }
    }
    f32x16 kvp[2] = {zero16(), zero16()};
    {
      f32x16 P[2];
      for (int r = 0; r < 2; ++r) P[r] = mfma(wp[0], pack8(acc[r][0], 0), zero16());
      for (int r = 0; r < 2; ++r) P[r] = mfma(wp[1], pack8(acc[r][0], 1), P[r]);
      u32x4 vf[2][2];
      for (int r = 0; r < 2; ++r) {
        f32x16 v = acc[r][1];
        for (int g = 0; g < 4; ++g) {
          const f32x4 rs = *(LAS f32x4 *)(L + L_TOK + w * 256 + r * 128 + (8 * g + 4 * h) * 4);
          for (int e = 0; e < 4; e += 2) { const f32x2 t2 = f32x2{v[4 * g + e], v[4 * g + e + 1]} * f32x2{rs[e], rs[e + 1]}; v[4 * g + e] = t2.x; v[4 * g + e + 1] = t2.y; }
        }
        for (int s = 0; s < 2; ++s) { vf[r][s] = pack8(v, s); if ((lane & 31) == 24) vf[r][s] = u32x4{0x3F803F80u, 0x3F803F80u, 0x3F803F80u, 0x3F803F80u}; }
      }
      u32x4 pf[2][2][2];
      for (int r = 0; r < 2; ++r) feature_map(P[r], st[r].rstd, h, pf[r]);
      f32x16 Z[2][2];
      for (int r = 0; r < 2; ++r) for (int t = 0; t < 2; ++t) Z[r][t] = mfma(pf[r][t][0], idf[0], zero16());
      for (int r = 0; r < 2; ++r) for (int t = 0; t < 2; ++t) Z[r][t] = mfma(pf[r][t][1], idf[1], Z[r][t]);
      for (int r = 0; r < 2; ++r) for (int s = 0; s < 2; ++s) for (int t = 0; t < 2; ++t) kvp[t] = mfma(pack8(Z[r][t], s), vf[r][s], kvp[t]);
    }
    for (int t = 0; t < 2; ++t)
      for (int g = 0; g < 4; ++g)
        *(LAS f32x4 *)(L + w * 8192 + (t * 4 + g) * 1024 + lane * 16) = f32x4{kvp[t][4 * g], kvp[t][4 * g + 1], kvp[t][4 * g + 2], kvp[t][4 * g + 3]};
    STAMP(4);
    BAR();
    u32x4 pq[2][2][2];
    {
      f32x16 P[2];
      for (int r = 0; r < 2; ++r) P[r] = mfma(wp[0], pack8(acc[r][2], 0), zero16());
      for (int r = 0; r < 2; ++r) P[r] = mfma(wp[1], pack8(acc[r][2], 1), P[r]);
      for (int r = 0; r < 2; ++r) {
        feature_map(P[r], st[r].rstd, h, pq[r]);
        for (int t = 0; t < 2; ++t) for (int s = 0; s < 2; ++s) asm volatile("" : "+v"(pq[r][t][s]));
      }
    }
    if (has_tile) epi_loads();
    else { for (int g = 0; g < 4; ++g) { xres[g] = u32x2{0, 0}; b2v[g] = u32x2{0, 0}; } sg0 = u32x4{0, 0, 0, 0}; }
    STAMP(6);
    BAR();
    STAMP(8);
    {
      u32x4 kf[2][2];
      for (int t = 0; t < 2; ++t) for (int s = 0; s < 2; ++s) kf[t][s] = *(LAS u32x4 *)(L + L_KVF + ((t * 2 + s) * 64 + lane) * 16);
      for (int r = 0; r < 2; ++r) {
        f32x16 num = zero16();
        for (int t = 0; t < 2; ++t) for (int s = 0; s < 2; ++s) num = mfma(kf[t][s], pq[r][t][s], num);
        const float rc = __builtin_amdgcn_rcpf(both_halves(num[12]).x + 1e-6f);
        for (int i = 0; i < 16; ++i) num[i] *= rc;
        if (!h) { num[12] = 0.f; num[13] = 0.f; num[14] = 0.f; num[15] = 0.f; }
        const u32 so = WS_O + ((8 * rga + 2 * w + r) * 32 + 2 * head) * 1024;
        st128_sc1(pack8(num, 0), rws, lane * 16, so);
        st128_sc1(pack8(num, 1), rws, lane * 16, so + 1024);
      }
    }
    STAMP(9);
    if (!has_tile) {
      vmwait<0>();
      if (lane < 2) st_flag(gws, WS_OF + ((8 * rga + 2 * w + lane) * 16 + head) * 64, E);
      STAMP_OUT();
      return;
    }
  } else if (ffn) {
    u32 bias[4];
    for (int t = 0; t < 4; ++t) bias[t] = *(const GAS unsigned short *)(gb1 + (128 * ct + 32 * t + (lane & 31)) * 2);
    f32x16 acc[2][4]; u32x4 stat[2];
    const u32 vostat = WS_STAT + (256 * rga + 64 * w + (lane & 31)) * 16;
    gemmA_compute<false>(L, w, lane, acc, first, [&]() { if (TOUCH_A == 3) touches(); }, [&]() { for (int r = 0; r < 2; ++r) stat[r] = ld128_sc1(rws, vostat + 512 * r, 0); } STAMP_PASS);
    RowStat st[2];
    for (int r = 0; r < 2; ++r) st[r] = row_stats(stat_settle(stat[r], rws, vostat + 512 * r, E), h);
    BAR();
    STAMP(3);
    epi_loads();
    for (int t = 0; t < 4; ++t) {
      const u32x4 wa = waug(cs_read(L, t, lane), bias[t] | (bias[t] << 16));
      for (int r = 0; r < 2; ++r) {
        f32x16 a = mfma(wa, st[r].xaug, acc[r][t]);
        for (int i = 0; i < 16; i += 2) { f32x2 v = {relu(a[i]), relu(a[i + 1])}; v *= v; a[i] = v.x; a[i + 1] = v.y; }
        const u32 so = WS_H + ((8 * rga + 2 * w + r) * 112 + 8 * ct + 2 * t) * 1024;
        st128_sc1(pack8(a, 0), rws, lane * 16, so);
        st128_sc1(pack8(a, 1), rws, lane * 16, so + 1024);
      }
    }
    STAMP(4);
    vmwait<0>();
    if (lane < 2) st_flag(gws, WS_HF + ((8 * rga + 2 * w + lane) * 14 + ct) * 64, E);
    STAMP(5);
  } else {
    first();
    BAR();
    epi_loads();
  }

  {
    constexpr int RD = RING;
    const u32 soh = WS_H + (rb * 112 + 56 * kg) * 1024, soo = WS_O + (rb * 32 + 16 * kg) * 1024;
    u32x4 ring[RD];
    const float sig = asf(stat_settle(sg0, rws, WS_STAT + row * 16, E)[2]);
    const float rs1 = __builtin_amdgcn_rcpf(sig), rs2 = rs1 * rs1;
    f32x2 bx[8], xx[8];
    for (int g = 0; g < 4; ++g) for (int e = 0; e < 2; ++e) {
      bx[2 * g + e] = f32x2{bf2f(b2v[g][e]), bfhi(b2v[g][e])}; xx[2 * g + e] = f32x2{bf2f(xres[g][e]), bfhi(xres[g][e])};
      asm volatile("" : "+v"(bx[2 * g + e]), "+v"(xx[2 * g + e]));
    }
    const int blk = (lane >> 4) & 1, q = (lane & 15) >> 2, p = lane & 3;
    u32 bb0[2], bb[2];
    for (int c2 = 0; c2 < 2; ++c2) bb0[c2] = (u32)(size_t)L + kg * 8192 + (4 * h + q) * 128 + ((c2 ^ kg ^ (q >> 1)) * 64) + 32 * blk + 8 * p;
    f32x16 af[2] = {zero16(), zero16()}, aa[2] = {zero16(), zero16()};
    u32x4 bf[2][2];
    sfor<0, 72>([&](auto I) {
      constexpr int i = I;
      if constexpr ((i & 3) == 0) asm volatile("s_barrier" ::: "memory");
      if constexpr (i == 0) STAMP(11);
      if constexpr (i == 40) STAMP(14);
      if constexpr (i == 0) {
        for (int j = 0; j < RD; ++j) ring[j] = ld128_sc1(rws, lane * 16, soh + j * 1024);
        if (att) {
          vmwait<RD>();
          if (lane < 2) st_flag(gws, WS_OF + ((8 * rga + 2 * w + lane) * 16 + head) * 64, E);
        }
        __builtin_amdgcn_sched_barrier(0);
        for (int c2 = 0; c2 < 2; ++c2) { bb[c2] = bb0[c2] + 4 * L_SLOT; asm volatile("" : "+v"(bb[c2])); bf[0][c2] = trfrag((lchar *)(size_t)bb[c2], 1024); }
      }
      u32x4 a = ring[i % RD];
      if constexpr (i + 1 < 72) {
        constexpr int n = i + 1;
        if constexpr ((n & 3) == 0) for (int c2 = 0; c2 < 2; ++c2) { bb[c2] = bb0[c2] + (((n >> 2) + 4) % NSLOT) * L_SLOT; asm volatile("" : "+v"(bb[c2])); }
        for (int c2 = 0; c2 < 2; ++c2) bf[n & 1][c2] = trfrag((lchar *)(size_t)bb[c2] + (n & 3) * 2048, 1024);
      }
      if constexpr (i < 56) { af[0] = mfma(bf[i & 1][0], a, af[0]); af[1] = mfma(bf[i & 1][1], a, af[1]); asm volatile("" : "+v"(af[0]), "+v"(af[1])); }
      else { aa[0] = mfma(bf[i & 1][0], a, aa[0]); aa[1] = mfma(bf[i & 1][1], a, aa[1]); asm volatile("" : "+v"(aa[0]), "+v"(aa[1])); }
      if constexpr (i + RD < 56) ring[i % RD] = ld128_sc1(rws, lane * 16, soh + (i + RD) * 1024);
      else if constexpr (i + RD < 72) ring[i % RD] = ld128_sc1(rws, lane * 16, soo + (i + RD - 56) * 1024);
      __builtin_amdgcn_sched_barrier(0);
    });
    STAMP(12);
    lchar *X = L + 4 * L_SLOT;
    {
      const f32x16 &sf = af[1], &sa = aa[1];
      for (int g = 0; g < 4; ++g) {
        *(LAS f32x4 *)(X + w * 8192 + g * 1024 + lane * 16) = f32x4{sf[4 * g], sf[4 * g + 1], sf[4 * g + 2], sf[4 * g + 3]};
        *(LAS f32x4 *)(X + w * 8192 + 4096 + g * 1024 + lane * 16) = f32x4{sa[4 * g], sa[4 * g + 1], sa[4 * g + 2], sa[4 * g + 3]};
      }
    }
    BAR();
    const f32x16 &yf = af[0], &ya = aa[0];
    u32x2 o2[4];
    for (int g = 0; g < 4; ++g) {
      const f32x4 pf = *(LAS f32x4 *)(X + (w ^ 2) * 8192 + g * 1024 + lane * 16), pa = *(LAS f32x4 *)(X + (w ^ 2) * 8192 + 4096 + g * 1024 + lane * 16);
      for (int e = 0; e < 2; ++e) {
        const int i = 4 * g + 2 * e;
        const f32x2 y = (f32x2{yf[i], yf[i + 1]} + f32x2{pf[2 * e], pf[2 * e + 1]}) * rs2 + bx[2 * g + e];
        const f32x2 t = y * -LOG2E;
        const f32x2 d = f32x2{__builtin_amdgcn_exp2f(t.x), __builtin_amdgcn_exp2f(t.y)} + 1.0f;
        const f32x2 sg = y * f32x2{__builtin_amdgcn_rcpf(d.x), __builtin_amdgcn_rcpf(d.y)};
        const f32x2 r = (f32x2{ya[i], ya[i + 1]} + f32x2{pa[2 * e], pa[2 * e + 1]}) * sg + xx[2 * g + e];
        o2[g][e] = pk2(r.x, r.y);
      }
    }
    const rsrc_t rout = __builtin_amdgcn_make_buffer_rsrc((void *)gout, 0, 2048 * XROW, 0x00020000);
    for (int g = 0; g < 4; g += 2) {
      auto rx0 = __builtin_amdgcn_permlane32_swap(o2[g].x, o2[g + 1].x, false, false);
      auto ry0 = __builtin_amdgcn_permlane32_swap(o2[g].y, o2[g + 1].y, false, false);
      __builtin_amdgcn_raw_buffer_store_b128(u32x4{rx0[0], ry0[0], rx0[1], ry0[1]}, rout, row * XROW + (col0 + 8 * (g + h)) * 2, 0, OUT_AUX);
    }
    STAMP(13);
    STAMP(15);
    STAMP_OUT();
  }
}

extern "C" void kernel_launch(void *const *d_in, const int *in_sizes, int n_in, void *d_out, int out_size, void *d_ws, size_t ws_size, hipStream_t stream) {
  fused_hedgehog_ffn<<<256, 512, 0, stream>>>((const char *)d_in[0], (const char *)d_in[1], (const char *)d_in[2], (const char *)d_in[3], (const char *)d_in[5],
                                              (const char *)d_in[6], (const char *)d_in[7], (const char *)d_in[8], (char *)d_out, (char *)d_ws);
}
```

```cpp
#include <hip/hip_runtime.h>
#include <hip/hip_bf16.h>
#include <stdint.h>
#include <type_traits>

typedef unsigned u32;
typedef u32 u32x4 __attribute__((ext_vector_type(4)));
typedef u32 u32x2 __attribute__((ext_vector_type(2)));
typedef float f32x16 __attribute__((ext_vector_type(16)));
typedef float f32x4 __attribute__((ext_vector_type(4)));
typedef float f32x2 __attribute__((ext_vector_type(2)));
typedef short s16x8 __attribute__((ext_vector_type(8)));
typedef short s16x4 __attribute__((ext_vector_type(4)));
typedef __bf16 bf16x2 __attribute__((ext_vector_type(2)));
typedef __bf16 bf16x8 __attribute__((ext_vector_type(8)));
#define LAS __attribute__((address_space(3)))
#define GAS __attribute__((address_space(1)))
#define DI __device__ __forceinline__
typedef __amdgpu_buffer_rsrc_t rsrc_t;
typedef LAS char lchar;

namespace {
constexpr int DM = 448, DF = 1792;
constexpr u32 XROW = DM * 2;
constexpr u32 QKVROW = 3 * DM * 2;
constexpr u32 W1ROW = DF * 2;
constexpr u32 WS_EPOCH = 0;
constexpr u32 WS_STAT = 4096;
constexpr u32 WS_KF = 65536;
constexpr u32 WS_HFA = 98304;
constexpr u32 WS_HFB = WS_HFA + 57344;
constexpr u32 WS_OF = WS_HFB + 57344;
constexpr u32 WS_KVP = 294912;
constexpr u32 WS_H = WS_KVP + (1u << 20);
constexpr u32 WS_O = WS_H + 64 * 112 * 1024;
constexpr u32 WS_END = WS_O + 64 * 32 * 1024;
constexpr u32 L_SLOT = 16384, NSLOT = 9;
constexpr u32 A_SLOT = 49152, A_WOFF = 32768;
constexpr u32 L_MISC = NSLOT * L_SLOT;
constexpr u32 L_WPHI = L_MISC;
constexpr u32 L_ZERO = L_MISC + 2048;
constexpr u32 L_E = L_MISC + 2112;
constexpr u32 L_DUMMY = L_MISC + 2304;
constexpr u32 L_CSP = L_MISC + 2560;
constexpr u32 L_TOK = L_CSP + 512;
constexpr u32 L_KVF = L_TOK + 1024;
constexpr u32 L_XT = L_KVF + 4096;
constexpr u32 L_END = L_XT + 8192;
constexpr u32 LDS_BYTES = 163840;
static_assert(L_END <= LDS_BYTES, "lds");
constexpr int SPIN_CAP = 1 << 16;
constexpr float LOG2E = 1.4426950408889634f;
#define CS_DOT2 0
#define SGB 1
#define LPRIO 0
#define FIRST_AT -1
#define TOUCH_A 0
#define POLL_SLEEP 1
#define HOLD 1
#define XPRIO 3
#define WARM 7
#define OUT_AUX 0
#define SPREAD 0
#define PIN_C 0
#define RING 12

#define STAMP(i) do {} while (0)
#define STAMP_DECL do {} while (0)
#define STAMP_OUT() do {} while (0)
#define STAMP_ARG
#define STAMP_PASS
template <int B, int E, class F> DI void sfor(F &&f) {
  if constexpr (B < E) { f(std::integral_constant<int, B>{}); sfor<B + 1, E>(f); }
}
DI u32 rfl(u32 v) { return __builtin_amdgcn_readfirstlane(v); }
DI f32x16 mfma(u32x4 a, u32x4 b, f32x16 c) {
  return __builtin_amdgcn_mfma_f32_32x32x16_bf16(__builtin_bit_cast(bf16x8, a), __builtin_bit_cast(bf16x8, b), c, 0, 0, 0);
}
DI u32 pk2(float lo, float hi) { f32x2 p = {lo, hi}; return __builtin_bit_cast(u32, __builtin_convertvector(p, bf16x2)); }
DI float dot2(u32 a, u32 b, float c) { return __builtin_amdgcn_fdot2_f32_bf16(__builtin_bit_cast(bf16x2, a), __builtin_bit_cast(bf16x2, b), c, false); }
DI float bf2f(u32 lo16) { return __builtin_bit_cast(float, lo16 << 16); }
DI float bfhi(u32 v) { return __builtin_bit_cast(float, v & 0xffff0000u); }
DI float asf(u32 v) { return __builtin_bit_cast(float, v); }
DI f32x2 both_halves(float v) {
  const u32 a = __builtin_bit_cast(u32, v);
  auto r = __builtin_amdgcn_permlane32_swap(a, a, false, false);
  return f32x2{asf(r[0]), asf(r[1])};
}
DI float half_lo(float v) { return both_halves(v).x; }
DI u32x4 pack8(const f32x16 &x, int s) { return u32x4{pk2(x[8 * s], x[8 * s + 1]), pk2(x[8 * s + 2], x[8 * s + 3]), pk2(x[8 * s + 4], x[8 * s + 5]), pk2(x[8 * s + 6], x[8 * s + 7])}; }
DI u32x2 trd(lchar *p) {
  typedef short v4i16 __attribute__((ext_vector_type(4)));
  return __builtin_bit_cast(u32x2, __builtin_amdgcn_ds_read_tr16_b64_v4i16((LAS v4i16 *)p));
}
DI u32x4 trfrag(lchar *p, int tstride) { u32x2 a = trd(p), b = trd(p + tstride); return u32x4{a.x, a.y, b.x, b.y}; }
DI float relu(float v) { return __builtin_amdgcn_fmed3f(v, 0.f, __builtin_inff()); }
DI f32x16 zero16() { f32x16 z; for (int i = 0; i < 16; ++i) z[i] = 0.f; return z; }
#define BAR() asm volatile("s_waitcnt lgkmcnt(0)\n\ts_barrier" ::: "memory")
template <int N> DI void vmwait() { asm volatile("s_waitcnt vmcnt(%0)" ::"i"(N) : "memory"); }
DI void dma16(rsrc_t r, u32 voff, u32 soff, u32 ldsaddr) {
  asm volatile("s_mov_b32 m0, %0\n\ts_nop 0\n\tbuffer_load_dwordx4 %1, %2, %3 offen lds" ::"s"(ldsaddr), "v"(voff), "s"(r), "s"(soff) : "memory", "m0");
}
template <int OFF> DI void ald128(u32x4 &d, rsrc_t r, u32 voff, u32 soff) {
  asm volatile("buffer_load_dwordx4 %0, %1, %2, %3 offen offset:%4" : "=v"(d) : "v"(voff), "s"(r), "s"(soff), "i"(OFF) : "memory");
}
DI u32x4 ld128_sc1(rsrc_t r, u32 voff, u32 soff) { return __builtin_amdgcn_raw_buffer_load_b128(r, voff, soff, 16); }
DI void st128_sc1(u32x4 v, rsrc_t r, u32 voff, u32 soff) {
  __builtin_amdgcn_raw_buffer_store_b128(v, r, voff, soff, 16);
  asm volatile("s_nop 1" ::"v"(v) : "memory");
}
typedef GAS u32 gu32;
DI u32 ld_flag(const char *ws, u32 off) { return __hip_atomic_load((gu32 *)(ws + off), __ATOMIC_RELAXED, __HIP_MEMORY_SCOPE_AGENT); }
DI void st_flag(char *ws, u32 off, u32 v) { __hip_atomic_store((gu32 *)(ws + off), v, __ATOMIC_RELAXED, __HIP_MEMORY_SCOPE_AGENT); }
DI void poll_all(const char *ws, u32 off, u32 E) {
#pragma unroll 1
  for (int spin = 0; spin < SPIN_CAP; ++spin) {
    if (__all(ld_flag(ws, off) == E)) break;
    __builtin_amdgcn_s_sleep(POLL_SLEEP);
  }
  __builtin_amdgcn_fence(__ATOMIC_ACQUIRE, "wavefront");
}
template <u32 LI, u32 SI> DI void dmai(rsrc_t r, u32 voff, u32 sob, u32 ldsb) {
  u32 t;
  asm volatile("s_add_u32 m0, %2, %5\n\ts_add_u32 %0, %1, %6\n\tbuffer_load_dwordx4 %3, %4, %0 offen lds"
               : "=&s"(t) : "s"(sob), "s"(ldsb), "v"(voff), "s"(r), "i"(LI), "i"(SI) : "memory", "m0", "scc");
}
template <int CTRL, int RMASK> DI float dpp_add(float v) {
  return v + asf(__builtin_amdgcn_update_dpp(0u, __builtin_bit_cast(u32, v), CTRL, RMASK, 0xf, true));
}
DI float wave_sum63(float v) {
  v = dpp_add<0x111, 0xf>(v); v = dpp_add<0x112, 0xf>(v); v = dpp_add<0x114, 0xf>(v); v = dpp_add<0x118, 0xf>(v);
  v = dpp_add<0x142, 0xa>(v); v = dpp_add<0x143, 0xc>(v);
  return v;
}
DI void st64_sc1(char *ws, u32 off, u32 lo, u32 hi) {
  __hip_atomic_store((GAS unsigned long long *)(ws + off), (unsigned long long)lo | ((unsigned long long)hi << 32), __ATOMIC_RELAXED, __HIP_MEMORY_SCOPE_AGENT);
}
struct Early { u32 ev; u32x4 xa, xb; };
DI Early early_issue(char *ws, rsrc_t rx, int row0, int lane) {
  Early e;
  e.ev = ld_flag(ws, WS_EPOCH);
  const u32 vo = lane < 56 ? (u32)lane * 16 : 0x7ffffff0u;
  e.xa = __builtin_amdgcn_raw_buffer_load_b128(rx, vo, row0 * XROW, 0); e.xb = __builtin_amdgcn_raw_buffer_load_b128(rx, vo, (row0 + 1) * XROW, 0);
  return e;
}
DI u32 early_finish(const Early &e, rsrc_t rws, int row0, int lane) {
  const u32 E = rfl(e.ev) + 1, ONES = 0x3F803F80u;
  float s[2], q[2];
  for (int r = 0; r < 2; ++r) {
    const u32x4 &x = r ? e.xb : e.xa;
    s[r] = dot2(x[0], ONES, 0.f); q[r] = dot2(x[0], x[0], 0.f);
    for (int d = 1; d < 4; ++d) { s[r] = dot2(x[d], ONES, s[r]); q[r] = dot2(x[d], x[d], q[r]); }
    s[r] = wave_sum63(s[r]); q[r] = wave_sum63(q[r]);
    const float mean = s[r] * (1.0f / DM);
    const float sig = __builtin_amdgcn_sqrtf(fmaxf(q[r] * (1.0f / DM) - mean * mean, 0.f) + 1e-5f);
    const u32 vo = lane == 63 ? WS_STAT + (row0 + r) * 16 : 0x7ffffff0u;
    __builtin_amdgcn_raw_buffer_store_b64(u32x2{__builtin_bit_cast(u32, mean), E}, rws, vo, 0, 16);
    __builtin_amdgcn_raw_buffer_store_b64(u32x2{__builtin_bit_cast(u32, sig), E}, rws, vo + 8, 0, 16);
  }
  return E;
}
DI u32x4 stat_settle(u32x4 g, rsrc_t rws, u32 vostat, u32 E) {
#pragma unroll 1
  for (int spin = 0; spin < SPIN_CAP && !__all(g[1] == E && g[3] == E); ++spin) { __builtin_amdgcn_s_sleep(1); g = ld128_sc1(rws, vostat, 0); }
  return g;
}
struct RowStat { float rstd; u32x4 xaug; };
DI RowStat row_stats(u32x4 g, int h) {
  const float m = -asf(g[0]), sig = asf(g[2]);
  RowStat r; r.rstd = __builtin_amdgcn_rcpf(sig);
  const float ml = m - bfhi(pk2(m, m)), sl = sig - bfhi(pk2(sig, sig));
  const u32 mm = pk2(m, ml), ss = pk2(sig, sl);
  r.xaug = h ? u32x4{0, 0, 0, 0} : u32x4{mm, mm, ss, 0};
  return r;
}
DI u32x4 waug(float cs, u32 bb) {
  const u32 hh = pk2(cs, cs); const float cl = cs - bfhi(hh);
  return u32x4{hh, pk2(cl, cl), bb, 0};
}
DI float cs_read(lchar *L, int t, int lane) { return *(LAS float *)(L + L_CSP + (t * 32 + (lane & 31)) * 4); }

DI void touch(rsrc_t r, u32 voff, u32 lb) {
  asm volatile("s_mov_b32 m0, %0\n\ts_nop 0\n\tbuffer_load_dword %1, %2, 0 offen lds" ::"s"(lb + L_DUMMY), "v"(voff), "s"(r) : "memory", "m0");
}
constexpr u32 CODE_BYTES = 50 * 1024, CODE_PIECES = (CODE_BYTES + 8191) / 8192;
DI void warm_up(unsigned long long pc, rsrc_t rx, rsrc_t rws, const char *gwqkv, const char *gw1, const char *gw2, const char *gwo, int xcd, int wv, int lane, u32 lb) {
  if (WARM & 8) {
    const rsrc_t rq = __builtin_amdgcn_make_buffer_rsrc((void *)gwqkv, 0, DM * QKVROW, 0x00020000), r1 = __builtin_amdgcn_make_buffer_rsrc((void *)gw1, 0, DM * W1ROW, 0x00020000);
#pragma unroll 1
    for (int q = xcd * 16 + wv; q < 405; q += 128) {
      const int c = 2 + q / 81, r = q % 81;
      if (r < 32) touch(rx, (64 * r + lane) * XROW + 128 * c, lb);
      else if (r < 53) { const u32 j = (r - 32) * 64 + lane, row = (j * 3121u) >> 16; touch(rq, (64 * c + row) * QKVROW + 128 * (j - 21 * row), lb); }
      else { const u32 j = (r - 53) * 64 + lane, row = (j * 2341u) >> 16; touch(r1, (64 * c + row) * W1ROW + 128 * (j - 28 * row), lb); }
    }
  }
  if (WARM & 1) {
    const rsrc_t rc = __builtin_amdgcn_make_buffer_rsrc((void *)(pc & ~127ull), 0, CODE_BYTES, 0x00020000);
    if (wv < (int)CODE_PIECES) touch(rc, wv * 8192 + lane * 128, lb);
  }
  const int g = xcd * 16 + wv;
  if (WARM & 2) { if (g < 27) touch(rws, WS_KF + g * 8192 + lane * 128, lb); }
  if (WARM & 4) {
    const rsrc_t r2 = __builtin_amdgcn_make_buffer_rsrc((void *)gw2, 0, DF * XROW, 0x00020000), ro = __builtin_amdgcn_make_buffer_rsrc((void *)gwo, 0, DM * XROW, 0x00020000);
    touch(r2, g * 8192 + lane * 128, lb);
    if (g < 68) touch(r2, (128 + g) * 8192 + lane * 128, lb);
    else if (g < 117) touch(ro, (g - 68) * 8192 + lane * 128, lb);
  }
}
template <bool ATT, class First, class After0, class Mid>
DI void gemmA_compute(lchar *L, int w, int lane, f32x16 (&acc)[2][ATT ? 3 : 4], First &&first, After0 &&after0, Mid &&mid STAMP_ARG) {
  constexpr int T = ATT ? 3 : 4, ROWB = ATT ? 64 : 256;
  const int h = lane >> 5, blk = (lane >> 4) & 1, q = (lane & 15) >> 2, p = lane & 3;
  const u32 lb = (u32)(size_t)L;
  u32 wb0[T], xb0[4], wb[T], xb[4], cb0, cb;
  for (int t = 0; t < T; ++t) wb0[t] = lb + A_WOFF + (ATT ? t * 4096 + (8 * h + q) * 64 : (8 * h + q) * 256 + 64 * (t ^ q)) + 32 * blk + 8 * p;
  for (int u = 0; u < 4; ++u) xb0[u] = lb + w * 8192 + (lane & 31) * 128 + 16 * ((2 * u + h) ^ ((lane >> 1) & 7));
  const int tw = w < T ? w : T - 1;
  cb0 = lb + A_WOFF + (ATT ? tw * 4096 + (8 * h + q) * 64 : (8 * h + q) * 256 + 64 * (tw ^ q)) + 32 * blk + 8 * p;
  f32x2 ca = {0.f, 0.f};
  for (int r = 0; r < 2; ++r) for (int t = 0; t < T; ++t) acc[r][t] = zero16();
  u32x4 xf[2][2], wf[2][T], cf[2];
  sfor<0, 29>([&](auto I) {
    constexpr int i = I, c = i >> 2, u = i & 3;
    if constexpr (i == 0 && FIRST_AT < 0) { first(); STAMP(14); }
    if constexpr (u == 0 && i < 28) {
      BAR();
      if constexpr (c == 0) STAMP(1);
      constexpr u32 so = (c % 3) * A_SLOT;
      for (int t = 0; t < T; ++t) { wb[t] = wb0[t] + so; asm volatile("" : "+v"(wb[t])); }
      for (int j = 0; j < 4; ++j) { xb[j] = xb0[j] + so; asm volatile("" : "+v"(xb[j])); }
      cb = cb0 + so; asm volatile("" : "+v"(cb));
    }
    if constexpr (i < 28) {
      cf[i & 1] = trfrag((lchar *)(size_t)cb + 16 * u * ROWB, 4 * ROWB);
      for (int r = 0; r < 2; ++r) xf[i & 1][r] = *(LAS u32x4 *)((lchar *)(size_t)xb[u] + r * 4096);
      for (int t = 0; t < T; ++t) wf[i & 1][t] = trfrag((lchar *)(size_t)wb[t] + 16 * u * ROWB, 4 * ROWB);
    }
    if constexpr (i == FIRST_AT) first();
    if constexpr (i == 1) after0();
    if constexpr (i == 20) mid();
    if constexpr (i > 0) {
      constexpr int j = (i - 1) & 1;
      for (int t = 0; t < T; ++t)
        for (int r = 0; r < 2; ++r) acc[r][t] = (ATT && t == 1) ? mfma(xf[j][r], wf[j][t], acc[r][t]) : mfma(wf[j][t], xf[j][r], acc[r][t]);
      for (int d = 0; d < 4; ++d) ca += f32x2{bf2f(cf[j][d]), bfhi(cf[j][d])};
      asm volatile("" : "+v"(ca));
      for (int m = 0; m < 2 * T; ++m) { __builtin_amdgcn_sched_group_barrier(0x8, 1, 0); __builtin_amdgcn_sched_group_barrier(0x100, 2, 0); __builtin_amdgcn_sched_group_barrier(0x2, 2, 0); }
    }
    __builtin_amdgcn_sched_barrier(0);
  });
  { const f32x2 a = both_halves(ca.x + ca.y); if (lane < 32 && w < T) *(LAS float *)(L + L_CSP + (w * 32 + lane) * 4) = a.x + a.y; }
  STAMP(2);
}
constexpr int w2_row(int st) { return st < 7 ? 128 * st : 128 * (st - 7) + 64; }
constexpr int h_step(int i) { return i < 28 ? 8 * (i >> 2) + (i & 3) : 8 * ((i - 28) >> 2) + 4 + (i & 3); }
struct StageCtx { rsrc_t rw2, rwo; u32 vob, so2, soo, ld; };
template <int ST> DI void stage_dma(const StageCtx &c) {
  sfor<0, 4>([&](auto I) {
    constexpr u32 i = I, dst = ((ST + 4) % NSLOT) * L_SLOT + i * 1024;
    dmai<dst, (w2_row(ST) + 8 * i) * XROW>(c.rw2, c.vob, c.so2, c.ld);
  });
}
template <int ST> DI void stage_load(const StageCtx &c, u32x4 (&br)[36]) {
  sfor<0, 4>([&](auto I) {
    constexpr u32 i = I;
    if constexpr (ST < 14) br[4 * (ST - 9) + i] = __builtin_amdgcn_raw_buffer_load_b128(c.rw2, c.vob, c.so2 + (w2_row(ST) + 8 * i) * XROW, 0);
    else br[4 * (ST - 9) + i] = __builtin_amdgcn_raw_buffer_load_b128(c.rwo, c.vob, c.soo + (56 * (ST - 14) + (i == 3 ? 20 : 8 * i)) * XROW, 0);
  });
}
template <bool ATT>
DI void gemmA_loader(lchar *L, rsrc_t rx, rsrc_t rw, u32 sox, u32 sow, int lw, int lane, const StageCtx &sc, u32x4 (&br)[36] STAMP_ARG) {
  constexpr int NW = ATT ? 3 : 4, N = 8 + NW;
  const u32 lb = (u32)(size_t)L;
  const u32 vox0 = (lane >> 3) * XROW + 16 * ((lane & 7) ^ ((lane >> 4) & 3)), vox1 = vox0 ^ 64;
  const u32 ldx = rfl(lb + lw * 8192);
  const u32 vow = ATT ? (lane >> 2) * QKVROW + ((lane & 3) == 3 ? 40 : 16 * (lane & 3)) : (lane >> 4) * W1ROW + 16 * ((lane & 15) ^ ((lane >> 4) << 2));
  const u32 swb = rfl(sow + 16 * lw * (ATT ? QKVROW : W1ROW)), ldw = rfl(lb + A_WOFF + lw * (ATT ? 1024 : 4096));
  auto issue = [&](auto C) {
    constexpr u32 c = C, so = (c % 3) * A_SLOT;
    sfor<0, NW>([&](auto I) {
      constexpr u32 i = I;
      if constexpr (ATT) dmai<so + i * 4096, 64 * c * QKVROW + (i == 0 ? 896 : i == 1 ? 1792 : 0)>(rw, vow, swb, ldw);
      else dmai<so + i * 1024, (64 * c + 4 * i) * W1ROW>(rw, vow, swb, ldw);
    });
    sfor<0, 8>([&](auto I) { constexpr u32 i = I; dmai<so + i * 1024, 8 * i * XROW + 128 * c>(rx, (i & 1) ? vox1 : vox0, sox, ldx); });
  };
  issue(std::integral_constant<int, 0>{});
  issue(std::integral_constant<int, 1>{});
  sfor<0, 7>([&](auto C) {
    constexpr int c = C;
    vmwait<(c == 6 ? 12 : N)>();
    asm volatile("s_barrier" ::: "memory");
    if constexpr (c == 0) STAMP(1);
    if constexpr (c + 2 < 7) issue(std::integral_constant<int, c + 2>{});
    else { stage_load<9 + 3 * (c - 5)>(sc, br); stage_load<10 + 3 * (c - 5)>(sc, br); stage_load<11 + 3 * (c - 5)>(sc, br); }
  });
  STAMP(2);
}

DI void feature_map(const f32x16 &P, float rstd, int h, u32x4 (&pf)[2][2]) {
  const float c = rstd * LOG2E;
  float mx = fmaxf(fmaxf(P[0], P[1]), P[2]), mn = fminf(fminf(P[0], P[1]), P[2]);
  for (int i = 3; i < 15; i += 2) { mx = fmaxf(fmaxf(mx, P[i]), P[i + 1]); mn = fminf(fminf(mn, P[i]), P[i + 1]); }
  mx = fmaxf(mx, P[15]); mn = fminf(mn, P[15]);
  { f32x2 a = both_halves(mx), b = both_halves(mn); mx = fmaxf(a.x, a.y); mn = fminf(b.x, b.y); }
  const float mxc = -mx * c, mnc = mn * c;
  f32x16 ep, en;
  for (int i = 0; i < 16; i += 2) {
    const f32x2 p2 = {P[i], P[i + 1]}, ap = p2 * c + mxc, an = mnc - p2 * c;
    ep[i] = __builtin_amdgcn_exp2f(ap.x); ep[i + 1] = __builtin_amdgcn_exp2f(ap.y);
    en[i] = __builtin_amdgcn_exp2f(an.x); en[i + 1] = __builtin_amdgcn_exp2f(an.y);
  }
  f32x2 sp2 = {ep[0], ep[1]}, sn2 = {en[0], en[1]};
  for (int i = 2; i < 12; i += 2) { sp2 += f32x2{ep[i], ep[i + 1]}; sn2 += f32x2{en[i], en[i + 1]}; }
  const float keep = h ? 0.f : 1.f;
  sp2 += (f32x2{ep[12], ep[13]} + f32x2{ep[14], ep[15]}) * keep;
  sn2 += (f32x2{en[12], en[13]} + f32x2{en[14], en[15]}) * keep;
  const f32x2 a = both_halves(sp2.x + sp2.y), b = both_halves(sn2.x + sn2.y);
  const float rp = __builtin_amdgcn_rcpf(a.x + a.y), rn = __builtin_amdgcn_rcpf(b.x + b.y);
  for (int i = 0; i < 16; i += 2) {
    const f32x2 u = f32x2{ep[i], ep[i + 1]} * rp, v = f32x2{en[i], en[i + 1]} * rn;
    ep[i] = u.x; ep[i + 1] = u.y; en[i] = v.x; en[i + 1] = v.y;
  }
  pf[0][0] = pack8(ep, 0); pf[0][1] = pack8(ep, 1); pf[1][0] = pack8(en, 0); pf[1][1] = pack8(en, 1);
}

constexpr int refill_end(int s) { return s < 6 ? 0 : s <= 11 ? 4 * (s - 5) : s <= 15 ? 24 + 3 * (s - 11) : 36; }
constexpr bool refill_ok() { for (int s = 6; s <= 17; ++s) for (int p = refill_end(s - 1); p < refill_end(s); ++p) { const int g = 9 + p / 4; if (s < g - 8 || s > g - 2) return false; } return refill_end(15) == 36; }
static_assert(refill_ok(), "refill schedule");
}

__global__ void __launch_bounds__(512, 2) fused_hedgehog_ffn(const char *__restrict__ gx, const char *__restrict__ gwqkv, const char *__restrict__ gwphi, const char *__restrict__ gwo,
                                                              const char *__restrict__ gw1, const char *__restrict__ gb1, const char *__restrict__ gw2, const char *__restrict__ gb2,
                                                              char *__restrict__ gout, char *__restrict__ gws) {
  __shared__ __attribute__((aligned(1024))) char smem[LDS_BYTES];
  lchar *L = (lchar *)smem;
  unsigned long long pc0;
  asm volatile("s_getpc_b64 %0" : "=s"(pc0));
  asm volatile("" ::"s"(gx), "s"(gwqkv), "s"(gwphi), "s"(gwo), "s"(gw1), "s"(gb1), "s"(gw2), "s"(gb2), "s"(gout), "s"(gws));
  const int tid = threadIdx.x, lane = tid & 63, w = rfl(tid >> 6);
  const int b = blockIdx.x, xcd = b & 7, slot = b >> 3;
  const int h = lane >> 5;
  STAMP_DECL; STAMP(0);
  const rsrc_t rx = __builtin_amdgcn_make_buffer_rsrc((void *)gx, 0, 2048 * XROW, 0x00020000);
  const rsrc_t rws = __builtin_amdgcn_make_buffer_rsrc((void *)gws, 0, WS_END, 0x00020000);
  const int tc = slot >= 16 ? slot - 16 : slot + 16;
  const bool has_tile = tc < 28;
  const u32 rt = tc / 7, cc = tc % 7;
  const bool att = slot < 16, ffn = !att && slot < 30;
  const int head = 8 * (xcd & 1) + (slot & 7);
  const int jf = slot - 16, ct = 7 * (xcd & 1) + (jf >= 7 ? jf - 7 : jf);
  const int rga = 2 * (xcd >> 1) + (att ? (slot >> 3) : (jf >= 7));

  if (w >= 4) {
    const int lw = w - 4;
    StageCtx sc;
    sc.rw2 = __builtin_amdgcn_make_buffer_rsrc((void *)gw2, 0, DF * XROW, 0x00020000);
    sc.rwo = __builtin_amdgcn_make_buffer_rsrc((void *)gwo, 0, DM * XROW, 0x00020000);
    sc.vob = (lane >> 3) * XROW + 16 * ((lane & 7) ^ (((lane >> 4) & 1) << 2));
    sc.so2 = rfl((896 * (lw >> 1) + 32 * (lw & 1)) * XROW + 128 * cc);
    sc.soo = rfl(28 * (8 * (lw >> 1) + (lw & 1)) * XROW + 128 * cc);
    sc.ld = rfl((u32)(size_t)L + (lw >> 1) * 8192 + (lw & 1) * 4096);
    u32x4 br[36];
    auto resid_dma = [&]() {
      const u32 vo = (lane >> 3) * XROW + 16 * ((lane & 7) ^ ((lane >> 4) & 3)), so = rfl((256 * xcd + 64 * rt + 16 * lw) * XROW + 128 * cc), ld = rfl((u32)(size_t)L + L_XT + lw * 2048);
      dmai<0, 0>(rx, vo, so, ld);
      dmai<1024, 8 * XROW>(rx, vo ^ 64, so, ld);
    };
    if (att) {
      const rsrc_t rw = __builtin_amdgcn_make_buffer_rsrc((void *)gwqkv, 0, DM * QKVROW, 0x00020000);
      if (lw < 2) {
        const rsrc_t rp = __builtin_amdgcn_make_buffer_rsrc((void *)gwphi, 0, 16 * 28 * 28 * 2, 0x00020000);
        dma16(rp, lane * 16 + lw * 1024, head * 1568, (u32)(size_t)L + L_WPHI + lw * 1024);
      }
      gemmA_loader<true>(L, rx, rw, rfl((256 * rga + 64 * lw) * XROW), rfl(56 * head), lw, lane, sc, br STAMP_PASS);
      BAR();
      const u32 E = rfl(*(LAS u32 *)(L + L_E));
      if (has_tile) { sfor<0, 7>([&](auto S) { stage_dma<S>(sc); }); resid_dma(); }
      sfor<15, 18>([&](auto S) { stage_load<S>(sc, br); });
      vmwait<12>();
      BAR();
      __builtin_amdgcn_s_setprio(XPRIO);
      STAMP(4);
      for (int t = 0; t < 2; ++t) {
        lchar *sp = (lchar *)(size_t)(sc.ld + (2 + t) * L_SLOT + lane * 16);
        f32x4 s = *(LAS f32x4 *)sp;
        for (int ww = 1; ww < 4; ++ww) s += *(LAS f32x4 *)(sp + ww * 1024);
        st128_sc1(__builtin_bit_cast(u32x4, s), rws, lane * 16, WS_KVP + (head * 8 + rga) * 8192 + (t * 4 + lw) * 1024);
      }
      if (has_tile) { sfor<7, 9>([&](auto S) { stage_dma<S>(sc); }); vmwait<8>(); }
      else vmwait<0>();
      if (lane == 0) st_flag(gws, WS_KF + ((head * 8 + rga) * 4 + lw) * 64, E);
      STAMP(5);
      poll_all(gws, WS_KF + ((head * 8 + (lane & 7)) * 4 + lw) * 64, E);
      STAMP(7);
      u32x4 part[2][8];
      for (int t = 0; t < 2; ++t) for (int r = 0; r < 8; ++r) part[t][r] = ld128_sc1(rws, lane * 16, WS_KVP + (head * 8 + r) * 8192 + (t * 4 + lw) * 1024);
      for (int t = 0; t < 2; ++t) {
        f32x4 s = __builtin_bit_cast(f32x4, part[t][0]);
        for (int r = 1; r < 8; ++r) s += __builtin_bit_cast(f32x4, part[t][r]);
        u32x2 pk = {pk2(s.x, s.y), pk2(s.z, s.w)};
        if (lw == 3 && h) pk = u32x2{0, 0};
        *(LAS u32x2 *)(L + L_KVF + ((t * 2 + (lw >> 1)) * 64 + lane) * 16 + (lw & 1) * 8) = pk;
      }
      __builtin_amdgcn_s_setprio(0);
      STAMP(8);
      if (!has_tile) {
        BAR();
        if (xcd == 0 && slot == 12 && lw == 0) {
#pragma unroll 1
          for (int spin = 0; spin < SPIN_CAP; ++spin) {
            bool ok = true;
            for (int i = 0; i < 32; ++i) ok &= ld_flag(gws, WS_STAT + (64 * i + lane) * 16 + 4) == E;
            if (__all(ok)) break;
            __builtin_amdgcn_s_sleep(8);
          }
          if (lane == 0) st_flag(gws, WS_EPOCH, E);
        }
        STAMP_OUT();
        return;
      }
      vmwait<0>();
    } else if (ffn) {
      const rsrc_t rw = __builtin_amdgcn_make_buffer_rsrc((void *)gw1, 0, DM * W1ROW, 0x00020000);
      gemmA_loader<false>(L, rx, rw, rfl((256 * rga + 64 * lw) * XROW), rfl(256 * ct), lw, lane, sc, br STAMP_PASS);
      BAR();
      if (HOLD == 1) asm volatile("s_barrier" ::: "memory");
      sfor<0, 9>([&](auto S) { stage_dma<S>(sc); });
      resid_dma();
      sfor<15, 18>([&](auto S) { stage_load<S>(sc, br); });
      vmwait<22>();
      BAR();
    } else {
      warm_up(pc0, rx, rws, gwqkv, gw1, gw2, gwo, xcd, (slot - 30) * 8 + w, lane, (u32)(size_t)L);
      sfor<0, 9>([&](auto S) { stage_dma<S>(sc); });
      resid_dma();
      sfor<9, 18>([&](auto S) { stage_load<S>(sc, br); });
      vmwait<36>();
      BAR();
    }
    STAMP(10);
    const u32 ldv = sc.ld + lane * 16;
    sfor<6, 18>([&](auto S) {
      constexpr int s = S;
      if constexpr (s == 6) vmwait<0>();
      BAR();
      sfor<refill_end(s - 1), refill_end(s)>([&](auto Pc) {
        constexpr int pc = Pc;
        *(LAS u32x4 *)((lchar *)(size_t)ldv + ((9 + pc / 4 + 4) % NSLOT) * L_SLOT + (pc & 3) * 1024) = br[pc];
      });
    });
    STAMP(12);
    asm volatile("s_barrier" ::: "memory");
    STAMP_OUT();
    return;
  }

  const Early early = early_issue(gws, rx, 8 * b + 2 * w, lane);
  u32 E;
  auto touches = [&]() {
    if (att || ffn) {
      constexpr u32 OOB = 0x7ffffff0u;
      const u32 lb = (u32)(size_t)L;
      { const u32 g15 = att ? (slot & 7) : 8 + (jf >= 7 ? jf - 7 : jf), i = (g15 * 4 + w) * 22 + lane;
        touch(rx, (lane < 22 && i < 1280) ? (256 * rga + (i & 255)) * XROW + 128 * (2 + (i >> 8)) : OOB, lb); }
      if (att) {
        const rsrc_t rq = __builtin_amdgcn_make_buffer_rsrc((void *)gwqkv, 0, DM * QKVROW, 0x00020000);
        const u32 i = slot * 240 + w * 60 + lane, row = (i * 43691u) >> 19, rem = i - 12 * row;
        touch(rq, lane < 60 ? (128 + row) * QKVROW + 896 * (rem >> 2) + ((448 * (xcd & 1)) & ~127u) + 128 * (rem & 3) : OOB, lb);
      } else {
        const rsrc_t r1 = __builtin_amdgcn_make_buffer_rsrc((void *)gw1, 0, DM * W1ROW, 0x00020000);
        const u32 l8 = 4 * (jf >= 7) + w, i = 64 * l8 + lane, j = 512 + 16 * l8 + lane;
        touch(r1, (128 + (i >> 1)) * W1ROW + 256 * ct + 128 * (i & 1), lb);
        touch(r1, lane < 16 ? (128 + (j >> 1)) * W1ROW + 256 * ct + 128 * (j & 1) : OOB, lb);
      }
    }
  };
  if (TOUCH_A == 2) touches();
  auto first = [&]() {
    E = early_finish(early, rws, 8 * b + 2 * w, lane); if (tid == 0) *(LAS u32 *)(L + L_E) = E;
    if (TOUCH_A == 1) touches();
  };
  const int rh = w & 1, kg = w >> 1;
  const u32 rb = 8 * xcd + 2 * rt + rh;
  const u32 row = 32 * rb + (lane & 31), col0 = 64 * cc + 32 * kg;
  u32x4 pq[2][2][2];
  auto o_stage = [&]() {
    u32x4 kf[2][2];
    for (int t = 0; t < 2; ++t) for (int s = 0; s < 2; ++s) kf[t][s] = *(LAS u32x4 *)(L + L_KVF + ((t * 2 + s) * 64 + lane) * 16);
    f32x16 num[2] = {zero16(), zero16()};
    for (int t = 0; t < 2; ++t) for (int s = 0; s < 2; ++s) for (int r = 0; r < 2; ++r) num[r] = mfma(kf[t][s], pq[r][t][s], num[r]);
    for (int r = 0; r < 2; ++r) {
      const float rc = __builtin_amdgcn_rcpf(both_halves(num[r][12]).x + 1e-6f);
      for (int i = 0; i < 16; i += 2) { const f32x2 v = f32x2{num[r][i], num[r][i + 1]} * rc; num[r][i] = v.x; num[r][i + 1] = v.y; }
      if (!h) { num[r][12] = 0.f; num[r][13] = 0.f; num[r][14] = 0.f; num[r][15] = 0.f; }
      const u32 so = WS_O + ((8 * rga + 2 * w + r) * 32 + 2 * head) * 1024;
      st128_sc1(pack8(num[r], 0), rws, lane * 16, so);
      st128_sc1(pack8(num[r], 1), rws, lane * 16, so + 1024);
    }
  };
  if (att) {
    if (tid < 16) *(LAS u32 *)(L + L_ZERO + 4 * tid) = 0;
    u32x4 idf[2];
    for (int s = 0; s < 2; ++s)
      for (int d = 0; d < 4; ++d) {
        const int k0 = 16 * s + 8 * (d >> 1) + 4 * h + 2 * (d & 1), n = lane & 31;
        idf[s][d] = (n == k0 ? 0x3F80u : 0u) | (n == k0 + 1 ? 0x3F800000u : 0u);
      }
    for (int s = 0; s < 2; ++s) asm volatile("" : "+v"(idf[s]));
    f32x16 acc[2][3]; u32x4 stat[2];
    const u32 vostat = WS_STAT + (256 * rga + 64 * w + (lane & 31)) * 16;
    gemmA_compute<true>(L, w, lane, acc, first, [&]() { if (TOUCH_A == 3) touches(); }, [&]() { for (int r = 0; r < 2; ++r) stat[r] = ld128_sc1(rws, vostat + 512 * r, 0); } STAMP_PASS);
    RowStat st[2];
    for (int r = 0; r < 2; ++r) {
      st[r] = row_stats(stat_settle(stat[r], rws, vostat + 512 * r, E), h);
      *(LAS float *)(L + L_TOK + w * 256 + r * 128 + 4 * (lane & 31)) = st[r].rstd;
    }
    BAR();
    STAMP(3);
    {
      const u32x4 wk = waug(cs_read(L, 0, lane), 0), wv = waug(cs_read(L, 1, lane), 0), wq = waug(cs_read(L, 2, lane), 0);
      for (int r = 0; r < 2; ++r) { acc[r][0] = mfma(wk, st[r].xaug, acc[r][0]); acc[r][1] = mfma(st[r].xaug, wv, acc[r][1]); acc[r][2] = mfma(wq, st[r].xaug, acc[r][2]); }
    }
    u32x4 wp[2];
    {
      const int blk = (lane >> 4) & 1, q = (lane & 15) >> 2, p = lane & 3;
      const u32 col = (blk && p == 3) ? 24 : 16 * blk + 4 * p;
      for (int s = 0; s < 2; ++s) {
        u32x2 r[2];
        for (int t = 0; t < 2; ++t) {
          const int dp = 16 * s + 8 * t + 4 * h + q;
          const u32 a = (dp >= 24 && dp < 28) ? L_ZERO + 8 * p : L_WPHI + (dp >= 28 ? dp - 4 : dp) * 56 + col * 2;
          r[t] = trd(L + a);
        }
        wp[s] = u32x4{r[0].x, r[0].y, r[1].x, r[1].y};
      }
    }
    f32x16 kvp[2] = {zero16(), zero16()};
    {
      f32x16 P[2];
      for (int r = 0; r < 2; ++r) P[r] = mfma(wp[0], pack8(acc[r][0], 0), zero16());
      for (int r = 0; r < 2; ++r) P[r] = mfma(wp[1], pack8(acc[r][0], 1), P[r]);
      u32x4 vf[2][2];
      for (int r = 0; r < 2; ++r) {
        f32x16 v = acc[r][1];
        for (int g = 0; g < 4; ++g) {
          const f32x4 rs = *(LAS f32x4 *)(L + L_TOK + w * 256 + r * 128 + (8 * g + 4 * h) * 4);
          for (int e = 0; e < 4; e += 2) { const f32x2 t2 = f32x2{v[4 * g + e], v[4 * g + e + 1]} * f32x2{rs[e], rs[e + 1]}; v[4 * g + e] = t2.x; v[4 * g + e + 1] = t2.y; }
        }
        for (int s = 0; s < 2; ++s) { vf[r][s] = pack8(v, s); if ((lane & 31) == 24) vf[r][s] = u32x4{0x3F803F80u, 0x3F803F80u, 0x3F803F80u, 0x3F803F80u}; }
      }
      u32x4 pf[2][2][2];
      for (int r = 0; r < 2; ++r) feature_map(P[r], st[r].rstd, h, pf[r]);
      f32x16 Z[2][2];
      for (int r = 0; r < 2; ++r) for (int t = 0; t < 2; ++t) Z[r][t] = mfma(pf[r][t][0], idf[0], zero16());
      for (int r = 0; r < 2; ++r) for (int t = 0; t < 2; ++t) Z[r][t] = mfma(pf[r][t][1], idf[1], Z[r][t]);
      for (int r = 0; r < 2; ++r) for (int s = 0; s < 2; ++s) for (int t = 0; t < 2; ++t) kvp[t] = mfma(pack8(Z[r][t], s), vf[r][s], kvp[t]);
    }
    for (int t = 0; t < 2; ++t)
      for (int g = 0; g < 4; ++g)
        *(LAS f32x4 *)(L + (2 + t) * L_SLOT + (g >> 1) * 8192 + (g & 1) * 4096 + w * 1024 + lane * 16) = f32x4{kvp[t][4 * g], kvp[t][4 * g + 1], kvp[t][4 * g + 2], kvp[t][4 * g + 3]};
    STAMP(4);
    BAR();
    {
      f32x16 P[2];
      for (int r = 0; r < 2; ++r) P[r] = mfma(wp[0], pack8(acc[r][2], 0), zero16());
      for (int r = 0; r < 2; ++r) P[r] = mfma(wp[1], pack8(acc[r][2], 1), P[r]);
      for (int r = 0; r < 2; ++r) {
        feature_map(P[r], st[r].rstd, h, pq[r]);
        for (int t = 0; t < 2; ++t) for (int s = 0; s < 2; ++s) asm volatile("" : "+v"(pq[r][t][s]));
      }
    }
    STAMP(6);
    if (!has_tile) {
      BAR();
      o_stage();
      vmwait<0>();
      if (lane < 2) st_flag(gws, WS_OF + ((8 * rga + 2 * w + lane) * 16 + head) * 64, E);
      STAMP_OUT();
      return;
    }
  } else if (ffn) {
    u32 bias[4];
    for (int t = 0; t < 4; ++t) bias[t] = *(const GAS unsigned short *)(gb1 + (128 * ct + 32 * t + (lane & 31)) * 2);
    f32x16 acc[2][4]; u32x4 stat[2];
    const u32 vostat = WS_STAT + (256 * rga + 64 * w + (lane & 31)) * 16;
    gemmA_compute<false>(L, w, lane, acc, first, [&]() { if (TOUCH_A == 3) touches(); }, [&]() { for (int r = 0; r < 2; ++r) stat[r] = ld128_sc1(rws, vostat + 512 * r, 0); } STAMP_PASS);
    RowStat st[2];
    for (int r = 0; r < 2; ++r) st[r] = row_stats(stat_settle(stat[r], rws, vostat + 512 * r, E), h);
    BAR();
    STAMP(3);
    if (HOLD == 2) __builtin_amdgcn_s_setprio(3);
    for (int t = 0; t < 4; ++t) {
      const u32x4 wa = waug(cs_read(L, t, lane), bias[t] | (bias[t] << 16));
      for (int r = 0; r < 2; ++r) {
        f32x16 a = mfma(wa, st[r].xaug, acc[r][t]);
        for (int i = 0; i < 16; i += 2) { f32x2 v = {relu(a[i]), relu(a[i + 1])}; v *= v; a[i] = v.x; a[i + 1] = v.y; }
        const u32 so = WS_H + ((8 * rga + 2 * w + r) * 112 + 8 * ct + 2 * t) * 1024;
        st128_sc1(pack8(a, 0), rws, lane * 16, so);
        st128_sc1(pack8(a, 1), rws, lane * 16, so + 1024);
      }
      if (t == 2) {
        vmwait<4>();
        if (lane < 2) st_flag(gws, WS_HFA + ((8 * rga + 2 * w + lane) * 14 + ct) * 64, E);
      }
    }
    if (HOLD == 1) asm volatile("s_barrier" ::: "memory");
    if (HOLD == 2) __builtin_amdgcn_s_setprio(0);
    STAMP(4);
    vmwait<0>();
    if (lane < 2) st_flag(gws, WS_HFB + ((8 * rga + 2 * w + lane) * 14 + ct) * 64, E);
    STAMP(5);
    for (int t = 0; t < 2; ++t) for (int r = 0; r < 2; ++r) for (int s = 0; s < 2; ++s) pq[t][r][s] = u32x4{0, 0, 0, 0};
    BAR();
  } else {
    warm_up(pc0, rx, rws, gwqkv, gw1, gw2, gwo, xcd, (slot - 30) * 8 + w, lane, (u32)(size_t)L);
    first();
    for (int t = 0; t < 2; ++t) for (int r = 0; r < 2; ++r) for (int s = 0; s < 2; ++s) pq[t][r][s] = u32x4{0, 0, 0, 0};
    BAR();
  }

  {
    constexpr int RD = RING;
    const u32 soh = WS_H + (rb * 112 + 56 * kg) * 1024, soo = WS_O + (rb * 32 + 16 * kg) * 1024;
    u32x4 ring[RD];
    u32x2 b2v[4]; u32x4 sg0;
    const int blk = (lane >> 4) & 1, q = (lane & 15) >> 2, p = lane & 3;
    u32 bb0[2], bb[2];
    for (int c2 = 0; c2 < 2; ++c2) bb0[c2] = (u32)(size_t)L + kg * 8192 + (4 * h + q) * 128 + ((c2 ^ kg ^ (q >> 1)) * 64) + 32 * blk + 8 * p;
    f32x16 af[2] = {zero16(), zero16()}, aa[2] = {zero16(), zero16()};
    u32x4 bf[2][2];
    const u32 hfb = WS_HFB + (rb * 14 + 7 * kg + ((lane & 7) < 7 ? (lane & 7) : 0)) * 64, ofo = WS_OF + (rb * 16 + 8 * kg + (lane & 7)) * 64;
    u32 look = 0;
    f32x2 sg[8], xx[8];
    u32x4 oa[16];
    poll_all(gws, WS_HFA + (rb * 14 + 7 * kg + ((lane & 7) < 7 ? (lane & 7) : 0)) * 64, E);
    STAMP(10);
    sfor<0, 72>([&](auto I) {
      constexpr int i = I;
      if constexpr (i == 56) {
        for (int g = 0; g < 4; ++g)
          *(LAS f32x4 *)(L + 7 * L_SLOT + w * 4096 + g * 1024 + lane * 16) = f32x4{af[1][4 * g], af[1][4 * g + 1], af[1][4 * g + 2], af[1][4 * g + 3]};
        BAR();
        u32x4 g4 = sg0;
        if (!__all(g4[1] == E && g4[3] == E)) g4 = stat_settle(g4, rws, WS_STAT + row * 16, E);
        const float rs1 = __builtin_amdgcn_rcpf(asf(g4[2])), rs2 = rs1 * rs1;
        for (int g = 0; g < 4; ++g) {
          const f32x4 pf = *(LAS f32x4 *)(L + 7 * L_SLOT + (w ^ 2) * 4096 + g * 1024 + lane * 16);
          for (int e = 0; e < 2; ++e) {
            const int j = 4 * g + 2 * e;
            const f32x2 y = (f32x2{af[0][j], af[0][j + 1]} + f32x2{pf[2 * e], pf[2 * e + 1]}) * rs2 + f32x2{bf2f(b2v[g][e]), bfhi(b2v[g][e])};
            const f32x2 t = y * -LOG2E;
            const f32x2 d = f32x2{__builtin_amdgcn_exp2f(t.x), __builtin_amdgcn_exp2f(t.y)} + 1.0f;
            sg[2 * g + e] = y * f32x2{__builtin_amdgcn_rcpf(d.x), __builtin_amdgcn_rcpf(d.y)};
            asm volatile("" : "+v"(sg[2 * g + e]));
          }
        }
        STAMP(14);
        if (!__all(look == E)) poll_all(gws, ofo, E);
        for (int j = 0; j < 16; ++j) oa[j] = ld128_sc1(rws, lane * 16, soo + j * 1024);
        __builtin_amdgcn_sched_barrier(0);
        for (int g = 0; g < 4; ++g) {
          const u32 r64 = 32 * rh + (lane & 31);
          const u32x2 xr = *(LAS u32x2 *)(L + L_XT + r64 * 128 + 16 * ((4 * kg + g) ^ ((r64 >> 1) & 7)) + 8 * h);
          for (int e = 0; e < 2; ++e) { xx[2 * g + e] = f32x2{bf2f(xr[e]), bfhi(xr[e])}; asm volatile("" : "+v"(xx[2 * g + e])); }
        }
      }
      if constexpr (i >= 24 && (i & 3) == 0 && i != 56) asm volatile("s_barrier" ::: "memory");
      if constexpr (i == 0) STAMP(11);
      if constexpr (i == 0) {
        for (int j = 0; j < RD; ++j) ring[j] = ld128_sc1(rws, lane * 16, soh + h_step(j) * 1024);
        for (int g = 0; g < 4; ++g) b2v[g] = *(const GAS u32x2 *)(gb2 + (col0 + 8 * g + 4 * h) * 2);
        sg0 = ld128_sc1(rws, WS_STAT + row * 16, 0);
        __builtin_amdgcn_sched_barrier(0);
        for (int c2 = 0; c2 < 2; ++c2) { bb[c2] = bb0[c2] + 4 * L_SLOT; asm volatile("" : "+v"(bb[c2])); bf[0][c2] = trfrag((lchar *)(size_t)bb[c2], 1024); }
      }
      if constexpr (i == 28 - RD - 6) look = ld_flag(gws, hfb);
      if constexpr (i == 28 - RD) { if (!__all(look == E)) poll_all(gws, hfb, E); }
      if constexpr (i == 50) look = ld_flag(gws, ofo);
      if constexpr (i == 24) { if (att) o_stage(); }
      if constexpr (i == 28) {
        if (att) { vmwait<4>(); if (lane < 2) st_flag(gws, WS_OF + ((8 * rga + 2 * w + lane) * 16 + head) * 64, E); }
      }
      u32x4 a;
      if constexpr (i < 56) a = ring[i % RD]; else a = oa[i - 56];
      if constexpr (i + 1 < 72) {
        constexpr int n = i + 1;
        if constexpr ((n & 3) == 0) for (int c2 = 0; c2 < 2; ++c2) { bb[c2] = bb0[c2] + (((n >> 2) + 4) % NSLOT) * L_SLOT; asm volatile("" : "+v"(bb[c2])); }
        for (int c2 = 0; c2 < 2; ++c2) bf[n & 1][c2] = trfrag((lchar *)(size_t)bb[c2] + (n & 3) * 2048, 1024);
      }
      if constexpr (i < 56) { af[0] = mfma(bf[i & 1][0], a, af[0]); af[1] = mfma(bf[i & 1][1], a, af[1]); asm volatile("" : "+v"(af[0]), "+v"(af[1])); }
      else { aa[0] = mfma(bf[i & 1][0], a, aa[0]); aa[1] = mfma(bf[i & 1][1], a, aa[1]); asm volatile("" : "+v"(aa[0]), "+v"(aa[1])); }
      if constexpr (i + RD < 56) ring[i % RD] = ld128_sc1(rws, lane * 16, soh + h_step(i + RD) * 1024);
      __builtin_amdgcn_sched_barrier(0);
    });
    STAMP(12);
    for (int g = 0; g < 4; ++g)
      *(LAS f32x4 *)(L + 8 * L_SLOT + w * 4096 + g * 1024 + lane * 16) = f32x4{aa[1][4 * g], aa[1][4 * g + 1], aa[1][4 * g + 2], aa[1][4 * g + 3]};
    BAR();
    u32x2 o2[4];
    for (int g = 0; g < 4; ++g) {
      const f32x4 pa = *(LAS f32x4 *)(L + 8 * L_SLOT + (w ^ 2) * 4096 + g * 1024 + lane * 16);
      for (int e = 0; e < 2; ++e) {
        const int i = 4 * g + 2 * e;
        const f32x2 r = (f32x2{aa[0][i], aa[0][i + 1]} + f32x2{pa[2 * e], pa[2 * e + 1]}) * sg[2 * g + e] + xx[2 * g + e];
        o2[g][e] = pk2(r.x, r.y);
      }
    }
    const rsrc_t rout = __builtin_amdgcn_make_buffer_rsrc((void *)gout, 0, 2048 * XROW, 0x00020000);
    for (int g = 0; g < 4; g += 2) {
      auto rx0 = __builtin_amdgcn_permlane32_swap(o2[g].x, o2[g + 1].x, false, false);
      auto ry0 = __builtin_amdgcn_permlane32_swap(o2[g].y, o2[g + 1].y, false, false);
      __builtin_amdgcn_raw_buffer_store_b128(u32x4{rx0[0], ry0[0], rx0[1], ry0[1]}, rout, row * XROW + (col0 + 8 * (g + h)) * 2, 0, OUT_AUX);
    }
    STAMP(13);
    STAMP(15);
    STAMP_OUT();
  }
}

extern "C" void kernel_launch(void *const *d_in, const int *in_sizes, int n_in, void *d_out, int out_size, void *d_ws, size_t ws_size, hipStream_t stream) {
  fused_hedgehog_ffn<<<256, 512, 0, stream>>>((const char *)d_in[0], (const char *)d_in[1], (const char *)d_in[2], (const char *)d_in[3], (const char *)d_in[5],
                                              (const char *)d_in[6], (const char *)d_in[7], (const char *)d_in[8], (char *)d_out, (char *)d_ws);
}
```

```cpp
#include <hip/hip_runtime.h>
#include <hip/hip_bf16.h>
#include <stdint.h>
#include <type_traits>

typedef unsigned u32;
typedef u32 u32x4 __attribute__((ext_vector_type(4)));
typedef u32 u32x2 __attribute__((ext_vector_type(2)));
typedef float f32x16 __attribute__((ext_vector_type(16)));
typedef float f32x4 __attribute__((ext_vector_type(4)));
typedef float f32x2 __attribute__((ext_vector_type(2)));
typedef short s16x8 __attribute__((ext_vector_type(8)));
typedef short s16x4 __attribute__((ext_vector_type(4)));
typedef __bf16 bf16x2 __attribute__((ext_vector_type(2)));
typedef __bf16 bf16x8 __attribute__((ext_vector_type(8)));
#define LAS __attribute__((address_space(3)))
#define GAS __attribute__((address_space(1)))
#define DI __device__ __forceinline__
typedef __amdgpu_buffer_rsrc_t rsrc_t;
typedef LAS char lchar;

namespace {
constexpr int DM = 448, DF = 1792;
constexpr u32 XROW = DM * 2;
constexpr u32 QKVROW = 3 * DM * 2;
constexpr u32 W1ROW = DF * 2;
constexpr u32 WS_EPOCH = 0;
constexpr u32 WS_STAT = 4096;
constexpr u32 WS_KF = 65536;
constexpr u32 WS_HFA = 98304;
constexpr u32 WS_HFB = WS_HFA + 57344;
constexpr u32 WS_OF = WS_HFB + 57344;
constexpr u32 WS_KVP = 294912;
constexpr u32 WS_H = WS_KVP + (1u << 20);
constexpr u32 WS_O = WS_H + 64 * 112 * 1024;
constexpr u32 WS_END = WS_O + 64 * 32 * 1024;
constexpr u32 L_SLOT = 16384, NSLOT = 9;
constexpr u32 A_SLOT = 49152, A_WOFF = 32768;
constexpr u32 L_MISC = NSLOT * L_SLOT;
constexpr u32 L_WPHI = L_MISC;
constexpr u32 L_ZERO = L_MISC + 2048;
constexpr u32 L_E = L_MISC + 2112;
constexpr u32 L_DUMMY = L_MISC + 2304;
constexpr u32 L_CSP = L_MISC + 2560;
constexpr u32 L_TOK = L_CSP + 512;
constexpr u32 L_KVF = L_TOK + 1024;
constexpr u32 L_XT = L_KVF + 4096;
constexpr u32 L_END = L_XT + 8192;
constexpr u32 LDS_BYTES = 163840;
static_assert(L_END <= LDS_BYTES, "lds");
constexpr int SPIN_CAP = 1 << 16;
constexpr float LOG2E = 1.4426950408889634f;
#define CS_DOT2 0
#define SGB 1
#define LPRIO 0
#define FIRST_AT -1
#define TOUCH_A 0
#define POLL_SLEEP 3
#define HOLD 1
#define XPRIO 3
#define WARM 15
#define BPD 3
#define OB 8
#define FFN_ALL 0
#define OUT_AUX 0
#define SPREAD 0
#define PIN_C 0
#define RING 10

#define STAMP(i) do {} while (0)
#define STAMP_DECL do {} while (0)
#define STAMP_OUT() do {} while (0)
#define STAMP_ARG
#define STAMP_PASS
template <int B, int E, class F> DI void sfor(F &&f) {
  if constexpr (B < E) { f(std::integral_constant<int, B>{}); sfor<B + 1, E>(f); }
}
DI u32 rfl(u32 v) { return __builtin_amdgcn_readfirstlane(v); }
DI f32x16 mfma(u32x4 a, u32x4 b, f32x16 c) {
  return __builtin_amdgcn_mfma_f32_32x32x16_bf16(__builtin_bit_cast(bf16x8, a), __builtin_bit_cast(bf16x8, b), c, 0, 0, 0);
}
DI u32 pk2(float lo, float hi) { f32x2 p = {lo, hi}; return __builtin_bit_cast(u32, __builtin_convertvector(p, bf16x2)); }
DI float dot2(u32 a, u32 b, float c) { return __builtin_amdgcn_fdot2_f32_bf16(__builtin_bit_cast(bf16x2, a), __builtin_bit_cast(bf16x2, b), c, false); }
DI float bf2f(u32 lo16) { return __builtin_bit_cast(float, lo16 << 16); }
DI float bfhi(u32 v) { return __builtin_bit_cast(float, v & 0xffff0000u); }
DI float asf(u32 v) { return __builtin_bit_cast(float, v); }
DI f32x2 both_halves(float v) {
  const u32 a = __builtin_bit_cast(u32, v);
  auto r = __builtin_amdgcn_permlane32_swap(a, a, false, false);
  return f32x2{asf(r[0]), asf(r[1])};
}
DI float half_lo(float v) { return both_halves(v).x; }
DI u32x4 pack8(const f32x16 &x, int s) { return u32x4{pk2(x[8 * s], x[8 * s + 1]), pk2(x[8 * s + 2], x[8 * s + 3]), pk2(x[8 * s + 4], x[8 * s + 5]), pk2(x[8 * s + 6], x[8 * s + 7])}; }
DI u32x2 trd(lchar *p) {
  typedef short v4i16 __attribute__((ext_vector_type(4)));
  return __builtin_bit_cast(u32x2, __builtin_amdgcn_ds_read_tr16_b64_v4i16((LAS v4i16 *)p));
}
DI u32x4 trfrag(lchar *p, int tstride) { u32x2 a = trd(p), b = trd(p + tstride); return u32x4{a.x, a.y, b.x, b.y}; }
DI float relu(float v) { return __builtin_amdgcn_fmed3f(v, 0.f, __builtin_inff()); }
DI f32x16 zero16() { f32x16 z; for (int i = 0; i < 16; ++i) z[i] = 0.f; return z; }
#define BAR() asm volatile("s_waitcnt lgkmcnt(0)\n\ts_barrier" ::: "memory")
template <int N> DI void vmwait() { asm volatile("s_waitcnt vmcnt(%0)" ::"i"(N) : "memory"); }
DI void dma16(rsrc_t r, u32 voff, u32 soff, u32 ldsaddr) {
  asm volatile("s_mov_b32 m0, %0\n\ts_nop 0\n\tbuffer_load_dwordx4 %1, %2, %3 offen lds" ::"s"(ldsaddr), "v"(voff), "s"(r), "s"(soff) : "memory", "m0");
}
template <int OFF> DI void ald128(u32x4 &d, rsrc_t r, u32 voff, u32 soff) {
  asm volatile("buffer_load_dwordx4 %0, %1, %2, %3 offen offset:%4" : "=v"(d) : "v"(voff), "s"(r), "s"(soff), "i"(OFF) : "memory");
}
DI u32x4 ld128_sc1(rsrc_t r, u32 voff, u32 soff) { return __builtin_amdgcn_raw_buffer_load_b128(r, voff, soff, 16); }
DI void st128_sc1(u32x4 v, rsrc_t r, u32 voff, u32 soff) {
  __builtin_amdgcn_raw_buffer_store_b128(v, r, voff, soff, 16);
  asm volatile("s_nop 1" ::"v"(v) : "memory");
}
typedef GAS u32 gu32;
DI u32 ld_flag(const char *ws, u32 off) { return __hip_atomic_load((gu32 *)(ws + off), __ATOMIC_RELAXED, __HIP_MEMORY_SCOPE_AGENT); }
DI void st_flag(char *ws, u32 off, u32 v) { __hip_atomic_store((gu32 *)(ws + off), v, __ATOMIC_RELAXED, __HIP_MEMORY_SCOPE_AGENT); }
DI void poll_all(const char *ws, u32 off, u32 E) {
#pragma unroll 1
  for (int spin = 0; spin < SPIN_CAP; ++spin) {
    if (__all(ld_flag(ws, off) == E)) break;
    __builtin_amdgcn_s_sleep(POLL_SLEEP);
  }
  __builtin_amdgcn_fence(__ATOMIC_ACQUIRE, "wavefront");
}
template <u32 LI, u32 SI> DI void dmai(rsrc_t r, u32 voff, u32 sob, u32 ldsb) {
  u32 t;
  asm volatile("s_add_u32 m0, %2, %5\n\ts_add_u32 %0, %1, %6\n\tbuffer_load_dwordx4 %3, %4, %0 offen lds"
               : "=&s"(t) : "s"(sob), "s"(ldsb), "v"(voff), "s"(r), "i"(LI), "i"(SI) : "memory", "m0", "scc");
}
template <int CTRL, int RMASK> DI float dpp_add(float v) {
  return v + asf(__builtin_amdgcn_update_dpp(0u, __builtin_bit_cast(u32, v), CTRL, RMASK, 0xf, true));
}
DI float wave_sum63(float v) {
  v = dpp_add<0x111, 0xf>(v); v = dpp_add<0x112, 0xf>(v); v = dpp_add<0x114, 0xf>(v); v = dpp_add<0x118, 0xf>(v);
  v = dpp_add<0x142, 0xa>(v); v = dpp_add<0x143, 0xc>(v);
  return v;
}
DI void st64_sc1(char *ws, u32 off, u32 lo, u32 hi) {
  __hip_atomic_store((GAS unsigned long long *)(ws + off), (unsigned long long)lo | ((unsigned long long)hi << 32), __ATOMIC_RELAXED, __HIP_MEMORY_SCOPE_AGENT);
}
struct Early { u32 ev; u32x4 xa, xb; };
DI Early early_issue(char *ws, rsrc_t rx, int row0, int lane) {
  Early e;
  e.ev = ld_flag(ws, WS_EPOCH);
  const u32 vo = lane < 56 ? (u32)lane * 16 : 0x7ffffff0u;
  e.xa = __builtin_amdgcn_raw_buffer_load_b128(rx, vo, row0 * XROW, 0); e.xb = __builtin_amdgcn_raw_buffer_load_b128(rx, vo, (row0 + 1) * XROW, 0);
  return e;
}
DI u32 early_finish(const Early &e, rsrc_t rws, int row0, int lane) {
  const u32 E = rfl(e.ev) + 1, ONES = 0x3F803F80u;
  float s[2], q[2];
  for (int r = 0; r < 2; ++r) {
    const u32x4 &x = r ? e.xb : e.xa;
    s[r] = dot2(x[0], ONES, 0.f); q[r] = dot2(x[0], x[0], 0.f);
    for (int d = 1; d < 4; ++d) { s[r] = dot2(x[d], ONES, s[r]); q[r] = dot2(x[d], x[d], q[r]); }
    s[r] = wave_sum63(s[r]); q[r] = wave_sum63(q[r]);
    const float mean = s[r] * (1.0f / DM);
    const float sig = __builtin_amdgcn_sqrtf(fmaxf(q[r] * (1.0f / DM) - mean * mean, 0.f) + 1e-5f);
    const u32 vo = lane == 63 ? WS_STAT + (row0 + r) * 16 : 0x7ffffff0u;
    __builtin_amdgcn_raw_buffer_store_b64(u32x2{__builtin_bit_cast(u32, mean), E}, rws, vo, 0, 16);
    __builtin_amdgcn_raw_buffer_store_b64(u32x2{__builtin_bit_cast(u32, sig), E}, rws, vo + 8, 0, 16);
  }
  return E;
}
DI u32x4 stat_settle(u32x4 g, rsrc_t rws, u32 vostat, u32 E) {
#pragma unroll 1
  for (int spin = 0; spin < SPIN_CAP && !__all(g[1] == E && g[3] == E); ++spin) { __builtin_amdgcn_s_sleep(1); g = ld128_sc1(rws, vostat, 0); }
  return g;
}
struct RowStat { float rstd; u32x4 xaug; };
DI RowStat row_stats(u32x4 g, int h) {
  const float m = -asf(g[0]), sig = asf(g[2]);
  RowStat r; r.rstd = __builtin_amdgcn_rcpf(sig);
  const float ml = m - bfhi(pk2(m, m)), sl = sig - bfhi(pk2(sig, sig));
  const u32 mm = pk2(m, ml), ss = pk2(sig, sl);
  r.xaug = h ? u32x4{0, 0, 0, 0} : u32x4{mm, mm, ss, 0};
  return r;
}
DI u32x4 waug(float cs, u32 bb) {
  const u32 hh = pk2(cs, cs); const float cl = cs - bfhi(hh);
  return u32x4{hh, pk2(cl, cl), bb, 0};
}
DI float cs_read(lchar *L, int t, int lane) { return *(LAS float *)(L + L_CSP + (t * 32 + (lane & 31)) * 4); }

DI void touch(rsrc_t r, u32 voff, u32 lb) {
  asm volatile("s_mov_b32 m0, %0\n\ts_nop 0\n\tbuffer_load_dword %1, %2, 0 offen lds" ::"s"(lb + L_DUMMY), "v"(voff), "s"(r) : "memory", "m0");
}
constexpr u32 CODE_BYTES = 50 * 1024, CODE_PIECES = (CODE_BYTES + 8191) / 8192;
DI void warm_up(unsigned long long pc, rsrc_t rx, rsrc_t rws, const char *gwqkv, const char *gw1, const char *gw2, const char *gwo, int xcd, int wv, int lane, u32 lb) {
  if (WARM & 8) {
    const rsrc_t rq = __builtin_amdgcn_make_buffer_rsrc((void *)gwqkv, 0, DM * QKVROW, 0x00020000), r1 = __builtin_amdgcn_make_buffer_rsrc((void *)gw1, 0, DM * W1ROW, 0x00020000);
#pragma unroll 1
    for (int q = xcd * 16 + wv; q < 405; q += 128) {
      const int c = 2 + q / 81, r = q % 81;
      if (r < 32) touch(rx, (64 * r + lane) * XROW + 128 * c, lb);
      else if (r < 53) { const u32 j = (r - 32) * 64 + lane, row = (j * 3121u) >> 16; touch(rq, (64 * c + row) * QKVROW + 128 * (j - 21 * row), lb); }
      else { const u32 j = (r - 53) * 64 + lane, row = (j * 2341u) >> 16; touch(r1, (64 * c + row) * W1ROW + 128 * (j - 28 * row), lb); }
    }
  }
  if (WARM & 1) {
    const rsrc_t rc = __builtin_amdgcn_make_buffer_rsrc((void *)(pc & ~127ull), 0, CODE_BYTES, 0x00020000);
    if (wv < (int)CODE_PIECES) touch(rc, wv * 8192 + lane * 128, lb);
  }
  const int g = xcd * 16 + wv;
  if (WARM & 2) { if (g < 27) touch(rws, WS_KF + g * 8192 + lane * 128, lb); }
  if (WARM & 4) {
    const rsrc_t r2 = __builtin_amdgcn_make_buffer_rsrc((void *)gw2, 0, DF * XROW, 0x00020000), ro = __builtin_amdgcn_make_buffer_rsrc((void *)gwo, 0, DM * XROW, 0x00020000);
    touch(r2, g * 8192 + lane * 128, lb);
    if (g < 68) touch(r2, (128 + g) * 8192 + lane * 128, lb);
    else if (g < 117) touch(ro, (g - 68) * 8192 + lane * 128, lb);
  }
}
template <bool ATT, class First, class After0, class Mid>
DI void gemmA_compute(lchar *L, int w, int lane, f32x16 (&acc)[2][ATT ? 3 : 4], First &&first, After0 &&after0, Mid &&mid STAMP_ARG) {
  constexpr int T = ATT ? 3 : 4, ROWB = ATT ? 64 : 256;
  const int h = lane >> 5, blk = (lane >> 4) & 1, q = (lane & 15) >> 2, p = lane & 3;
  const u32 lb = (u32)(size_t)L;
  u32 wb0[T], xb0[4], wb[T], xb[4], cb0, cb;
  for (int t = 0; t < T; ++t) wb0[t] = lb + A_WOFF + (ATT ? t * 4096 + (8 * h + q) * 64 : (8 * h + q) * 256 + 64 * (t ^ q)) + 32 * blk + 8 * p;
  for (int u = 0; u < 4; ++u) xb0[u] = lb + w * 8192 + (lane & 31) * 128 + 16 * ((2 * u + h) ^ ((lane >> 1) & 7));
  const int tw = w < T ? w : T - 1;
  cb0 = lb + A_WOFF + (ATT ? tw * 4096 + (8 * h + q) * 64 : (8 * h + q) * 256 + 64 * (tw ^ q)) + 32 * blk + 8 * p;
  f32x2 ca = {0.f, 0.f};
  for (int r = 0; r < 2; ++r) for (int t = 0; t < T; ++t) acc[r][t] = zero16();
  u32x4 xf[2][2], wf[2][T], cf[2];
  sfor<0, 29>([&](auto I) {
    constexpr int i = I, c = i >> 2, u = i & 3;
    if constexpr (i == 0 && FIRST_AT < 0) { first(); STAMP(14); }
    if constexpr (u == 0 && i < 28) {
      BAR();
      if constexpr (c == 0) STAMP(1);
      constexpr u32 so = (c % 3) * A_SLOT;
      for (int t = 0; t < T; ++t) { wb[t] = wb0[t] + so; asm volatile("" : "+v"(wb[t])); }
      for (int j = 0; j < 4; ++j) { xb[j] = xb0[j] + so; asm volatile("" : "+v"(xb[j])); }
      cb = cb0 + so; asm volatile("" : "+v"(cb));
    }
    if constexpr (i < 28) {
      cf[i & 1] = trfrag((lchar *)(size_t)cb + 16 * u * ROWB, 4 * ROWB);
      for (int r = 0; r < 2; ++r) xf[i & 1][r] = *(LAS u32x4 *)((lchar *)(size_t)xb[u] + r * 4096);
      for (int t = 0; t < T; ++t) wf[i & 1][t] = trfrag((lchar *)(size_t)wb[t] + 16 * u * ROWB, 4 * ROWB);
    }
    if constexpr (i == FIRST_AT) first();
    if constexpr (i == 1) after0();
    if constexpr (i == 20) mid();
    if constexpr (i > 0) {
      constexpr int j = (i - 1) & 1;
      for (int t = 0; t < T; ++t)
        for (int r = 0; r < 2; ++r) acc[r][t] = (ATT && t == 1) ? mfma(xf[j][r], wf[j][t], acc[r][t]) : mfma(wf[j][t], xf[j][r], acc[r][t]);
      for (int d = 0; d < 4; ++d) ca += f32x2{bf2f(cf[j][d]), bfhi(cf[j][d])};
      asm volatile("" : "+v"(ca));
      for (int m = 0; m < 2 * T; ++m) { __builtin_amdgcn_sched_group_barrier(0x8, 1, 0); __builtin_amdgcn_sched_group_barrier(0x100, 2, 0); __builtin_amdgcn_sched_group_barrier(0x2, 2, 0); }
    }
    __builtin_amdgcn_sched_barrier(0);
  });
  { const f32x2 a = both_halves(ca.x + ca.y); if (lane < 32 && w < T) *(LAS float *)(L + L_CSP + (w * 32 + lane) * 4) = a.x + a.y; }
  STAMP(2);
}
constexpr int w2_row(int st) { return st < 7 ? 128 * st : 128 * (st - 7) + 64; }
constexpr int h_step(int i) { return i < 28 ? 8 * (i >> 2) + (i & 3) : 8 * ((i - 28) >> 2) + 4 + (i & 3); }
struct StageCtx { rsrc_t rw2, rwo; u32 vob, so2, soo, ld; };
template <int ST> DI void stage_dma(const StageCtx &c) {
  sfor<0, 4>([&](auto I) {
    constexpr u32 i = I, dst = ((ST + 4) % NSLOT) * L_SLOT + i * 1024;
    dmai<dst, (w2_row(ST) + 8 * i) * XROW>(c.rw2, c.vob, c.so2, c.ld);
  });
}
template <int ST> DI void stage_load(const StageCtx &c, u32x4 (&br)[36]) {
  sfor<0, 4>([&](auto I) {
    constexpr u32 i = I;
    if constexpr (ST < 14) br[4 * (ST - 9) + i] = __builtin_amdgcn_raw_buffer_load_b128(c.rw2, c.vob, c.so2 + (w2_row(ST) + 8 * i) * XROW, 0);
    else br[4 * (ST - 9) + i] = __builtin_amdgcn_raw_buffer_load_b128(c.rwo, c.vob, c.soo + (56 * (ST - 14) + (i == 3 ? 20 : 8 * i)) * XROW, 0);
  });
}
template <bool ATT>
DI void gemmA_loader(lchar *L, rsrc_t rx, rsrc_t rw, u32 sox, u32 sow, int lw, int lane, const StageCtx &sc, u32x4 (&br)[36] STAMP_ARG) {
  constexpr int NW = ATT ? 3 : 4, N = 8 + NW;
  const u32 lb = (u32)(size_t)L;
  const u32 vox0 = (lane >> 3) * XROW + 16 * ((lane & 7) ^ ((lane >> 4) & 3)), vox1 = vox0 ^ 64;
  const u32 ldx = rfl(lb + lw * 8192);
  const u32 vow = ATT ? (lane >> 2) * QKVROW + ((lane & 3) == 3 ? 40 : 16 * (lane & 3)) : (lane >> 4) * W1ROW + 16 * ((lane & 15) ^ ((lane >> 4) << 2));
  const u32 swb = rfl(sow + 16 * lw * (ATT ? QKVROW : W1ROW)), ldw = rfl(lb + A_WOFF + lw * (ATT ? 1024 : 4096));
  auto issue = [&](auto C) {
    constexpr u32 c = C, so = (c % 3) * A_SLOT;
    sfor<0, NW>([&](auto I) {
      constexpr u32 i = I;
      if constexpr (ATT) dmai<so + i * 4096, 64 * c * QKVROW + (i == 0 ? 896 : i == 1 ? 1792 : 0)>(rw, vow, swb, ldw);
      else dmai<so + i * 1024, (64 * c + 4 * i) * W1ROW>(rw, vow, swb, ldw);
    });
    sfor<0, 8>([&](auto I) { constexpr u32 i = I; dmai<so + i * 1024, 8 * i * XROW + 128 * c>(rx, (i & 1) ? vox1 : vox0, sox, ldx); });
  };
  issue(std::integral_constant<int, 0>{});
  issue(std::integral_constant<int, 1>{});
  sfor<0, 7>([&](auto C) {
    constexpr int c = C;
    vmwait<(c == 6 ? 12 : N)>();
    asm volatile("s_barrier" ::: "memory");
    if constexpr (c == 0) STAMP(1);
    if constexpr (c + 2 < 7) issue(std::integral_constant<int, c + 2>{});
    else { stage_load<9 + 3 * (c - 5)>(sc, br); stage_load<10 + 3 * (c - 5)>(sc, br); stage_load<11 + 3 * (c - 5)>(sc, br); }
  });
  STAMP(2);
}

DI void feature_map(const f32x16 &P, float rstd, int h, u32x4 (&pf)[2][2]) {
  const float c = rstd * LOG2E;
  float mx = fmaxf(fmaxf(P[0], P[1]), P[2]), mn = fminf(fminf(P[0], P[1]), P[2]);
  for (int i = 3; i < 15; i += 2) { mx = fmaxf(fmaxf(mx, P[i]), P[i + 1]); mn = fminf(fminf(mn, P[i]), P[i + 1]); }
  mx = fmaxf(mx, P[15]); mn = fminf(mn, P[15]);
  { f32x2 a = both_halves(mx), b = both_halves(mn); mx = fmaxf(a.x, a.y); mn = fminf(b.x, b.y); }
  const float mxc = -mx * c, mnc = mn * c;
  f32x16 ep, en;
  for (int i = 0; i < 16; i += 2) {
    const f32x2 p2 = {P[i], P[i + 1]}, ap = p2 * c + mxc, an = mnc - p2 * c;
    ep[i] = __builtin_amdgcn_exp2f(ap.x); ep[i + 1] = __builtin_amdgcn_exp2f(ap.y);
    en[i] = __builtin_amdgcn_exp2f(an.x); en[i + 1] = __builtin_amdgcn_exp2f(an.y);
  }
  f32x2 sp2 = {ep[0], ep[1]}, sn2 = {en[0], en[1]};
  for (int i = 2; i < 12; i += 2) { sp2 += f32x2{ep[i], ep[i + 1]}; sn2 += f32x2{en[i], en[i + 1]}; }
  const float keep = h ? 0.f : 1.f;
  sp2 += (f32x2{ep[12], ep[13]} + f32x2{ep[14], ep[15]}) * keep;
  sn2 += (f32x2{en[12], en[13]} + f32x2{en[14], en[15]}) * keep;
  const f32x2 a = both_halves(sp2.x + sp2.y), b = both_halves(sn2.x + sn2.y);
  const float rp = __builtin_amdgcn_rcpf(a.x + a.y), rn = __builtin_amdgcn_rcpf(b.x + b.y);
  for (int i = 0; i < 16; i += 2) {
    const f32x2 u = f32x2{ep[i], ep[i + 1]} * rp, v = f32x2{en[i], en[i + 1]} * rn;
    ep[i] = u.x; ep[i + 1] = u.y; en[i] = v.x; en[i + 1] = v.y;
  }
  pf[0][0] = pack8(ep, 0); pf[0][1] = pack8(ep, 1); pf[1][0] = pack8(en, 0); pf[1][1] = pack8(en, 1);
}

constexpr int refill_end(int k) { return k < 0 ? 0 : k == 0 ? 7 : k == 1 ? 14 : k == 2 ? 20 : k == 3 ? 26 : k == 4 ? 32 : 36; }
constexpr bool refill_ok() { for (int k = 0; k < 6; ++k) for (int p = refill_end(k - 1); p < refill_end(k); ++p) { const int g = 9 + p / 4, s = 4 + 2 * k; if (s < g - 8 || 4 * (s + 2) > 4 * g - 2) return false; } return true; }
static_assert(refill_ok(), "refill schedule");
}

__global__ void __launch_bounds__(512, 2) fused_hedgehog_ffn(const char *__restrict__ gx, const char *__restrict__ gwqkv, const char *__restrict__ gwphi, const char *__restrict__ gwo,
                                                              const char *__restrict__ gw1, const char *__restrict__ gb1, const char *__restrict__ gw2, const char *__restrict__ gb2,
                                                              char *__restrict__ gout, char *__restrict__ gws) {
  __shared__ __attribute__((aligned(1024))) char smem[LDS_BYTES];
  lchar *L = (lchar *)smem;
  unsigned long long pc0;
  asm volatile("s_getpc_b64 %0" : "=s"(pc0));
  asm volatile("" ::"s"(gx), "s"(gwqkv), "s"(gwphi), "s"(gwo), "s"(gw1), "s"(gb1), "s"(gw2), "s"(gb2), "s"(gout), "s"(gws));
  const int tid = threadIdx.x, lane = tid & 63, w = rfl(tid >> 6);
  const int b = blockIdx.x, xcd = b & 7, slot = b >> 3;
  const int h = lane >> 5;
  STAMP_DECL; STAMP(0);
  const rsrc_t rx = __builtin_amdgcn_make_buffer_rsrc((void *)gx, 0, 2048 * XROW, 0x00020000);
  const rsrc_t rws = __builtin_amdgcn_make_buffer_rsrc((void *)gws, 0, WS_END, 0x00020000);
  const int tc = slot >= 16 ? slot - 16 : slot + 16;
  const bool has_tile = tc < 28;
  const u32 rt = tc / 7, cc = tc % 7;
  const bool att = slot < 16, ffn = !att && slot < 30;
  const int head = 8 * (xcd & 1) + (slot & 7);
  const int jf = slot - 16, ct = 7 * (xcd & 1) + (jf >= 7 ? jf - 7 : jf);
  const int rga = 2 * (xcd >> 1) + (att ? (slot >> 3) : (jf >= 7));

  if (w >= 4) {
    const int lw = w - 4;
    StageCtx sc;
    sc.rw2 = __builtin_amdgcn_make_buffer_rsrc((void *)gw2, 0, DF * XROW, 0x00020000);
    sc.rwo = __builtin_amdgcn_make_buffer_rsrc((void *)gwo, 0, DM * XROW, 0x00020000);
    sc.vob = (lane >> 3) * XROW + 16 * ((lane & 7) ^ (((lane >> 4) & 1) << 2));
    sc.so2 = rfl((896 * (lw >> 1) + 32 * (lw & 1)) * XROW + 128 * cc);
    sc.soo = rfl(28 * (8 * (lw >> 1) + (lw & 1)) * XROW + 128 * cc);
    sc.ld = rfl((u32)(size_t)L + (lw >> 1) * 8192 + (lw & 1) * 4096);
    u32x4 br[36];
    auto resid_dma = [&]() {
      const u32 vo = (lane >> 3) * XROW + 16 * ((lane & 7) ^ ((lane >> 4) & 3)), so = rfl((256 * xcd + 64 * rt + 16 * lw) * XROW + 128 * cc), ld = rfl((u32)(size_t)L + L_XT + lw * 2048);
      dmai<0, 0>(rx, vo, so, ld);
      dmai<1024, 8 * XROW>(rx, vo ^ 64, so, ld);
    };
    if (att) {
      const rsrc_t rw = __builtin_amdgcn_make_buffer_rsrc((void *)gwqkv, 0, DM * QKVROW, 0x00020000);
      if (lw < 2) {
        const rsrc_t rp = __builtin_amdgcn_make_buffer_rsrc((void *)gwphi, 0, 16 * 28 * 28 * 2, 0x00020000);
        dma16(rp, lane * 16 + lw * 1024, head * 1568, (u32)(size_t)L + L_WPHI + lw * 1024);
      }
      gemmA_loader<true>(L, rx, rw, rfl((256 * rga + 64 * lw) * XROW), rfl(56 * head), lw, lane, sc, br STAMP_PASS);
      BAR();
      const u32 E = rfl(*(LAS u32 *)(L + L_E));
      if (has_tile) { sfor<0, 5>([&](auto S) { stage_dma<S>(sc); }); sfor<5, 7>([&](auto S) { stage_dma<S>(sc); }); resid_dma(); }
      sfor<15, 18>([&](auto S) { stage_load<S>(sc, br); });
      if (has_tile) vmwait<22>();
      BAR();
      __builtin_amdgcn_s_setprio(XPRIO);
      STAMP(4);
      for (int t = 0; t < 2; ++t) {
        lchar *sp = (lchar *)(size_t)(sc.ld + (2 + t) * L_SLOT + lane * 16);
        f32x4 s = *(LAS f32x4 *)sp;
        for (int ww = 1; ww < 4; ++ww) s += *(LAS f32x4 *)(sp + ww * 1024);
        st128_sc1(__builtin_bit_cast(u32x4, s), rws, lane * 16, WS_KVP + (head * 8 + rga) * 8192 + (t * 4 + lw) * 1024);
      }
      if (has_tile) { sfor<7, 9>([&](auto S) { stage_dma<S>(sc); }); vmwait<8>(); }
      else vmwait<0>();
      if (lane == 0) st_flag(gws, WS_KF + ((head * 8 + rga) * 4 + lw) * 64, E);
      STAMP(5);
      poll_all(gws, WS_KF + ((head * 8 + (lane & 7)) * 4 + lw) * 64, E);
      STAMP(7);
      u32x4 part[2][8];
      for (int t = 0; t < 2; ++t) for (int r = 0; r < 8; ++r) part[t][r] = ld128_sc1(rws, lane * 16, WS_KVP + (head * 8 + r) * 8192 + (t * 4 + lw) * 1024);
      for (int t = 0; t < 2; ++t) {
        f32x4 s = __builtin_bit_cast(f32x4, part[t][0]);
        for (int r = 1; r < 8; ++r) s += __builtin_bit_cast(f32x4, part[t][r]);
        u32x2 pk = {pk2(s.x, s.y), pk2(s.z, s.w)};
        if (lw == 3 && h) pk = u32x2{0, 0};
        *(LAS u32x2 *)(L + L_KVF + ((t * 2 + (lw >> 1)) * 64 + lane) * 16 + (lw & 1) * 8) = pk;
      }
      __builtin_amdgcn_s_setprio(0);
      STAMP(8);
      if (!has_tile) {
        BAR();
        if (xcd == 0 && slot == 12 && lw == 0) {
#pragma unroll 1
          for (int spin = 0; spin < SPIN_CAP; ++spin) {
            bool ok = true;
            for (int i = 0; i < 32; ++i) ok &= ld_flag(gws, WS_STAT + (64 * i + lane) * 16 + 4) == E;
            if (__all(ok)) break;
            __builtin_amdgcn_s_sleep(8);
          }
          if (lane == 0) st_flag(gws, WS_EPOCH, E);
        }
        STAMP_OUT();
        return;
      }
      vmwait<0>();
    } else if (ffn) {
      const rsrc_t rw = __builtin_amdgcn_make_buffer_rsrc((void *)gw1, 0, DM * W1ROW, 0x00020000);
      gemmA_loader<false>(L, rx, rw, rfl((256 * rga + 64 * lw) * XROW), rfl(256 * ct), lw, lane, sc, br STAMP_PASS);
      BAR();
      if (HOLD == 1) asm volatile("s_barrier" ::: "memory");
      sfor<0, 5>([&](auto S) { stage_dma<S>(sc); });
      vmwait<0>();
      BAR();
      sfor<5, 9>([&](auto S) { stage_dma<S>(sc); });
      resid_dma();
      sfor<15, 18>([&](auto S) { stage_load<S>(sc, br); });
    } else {
      warm_up(pc0, rx, rws, gwqkv, gw1, gw2, gwo, xcd, (slot - 30) * 8 + w, lane, (u32)(size_t)L);
      sfor<0, 9>([&](auto S) { stage_dma<S>(sc); });
      resid_dma();
      sfor<9, 18>([&](auto S) { stage_load<S>(sc, br); });
      vmwait<36>();
      BAR();
    }
    STAMP(10);
    const u32 ldv = sc.ld + lane * 16;
    sfor<0, 7>([&](auto K) {
      constexpr int k = K;
      if constexpr (k == 0) vmwait<12>();
      BAR();
      sfor<refill_end(k - 1), refill_end(k)>([&](auto Pc) {
        constexpr int pc = Pc;
        *(LAS u32x4 *)((lchar *)(size_t)ldv + ((9 + pc / 4 + 4) % NSLOT) * L_SLOT + (pc & 3) * 1024) = br[pc];
        if (SPREAD) asm volatile("s_nop %0" ::"i"(SPREAD ? SPREAD - 1 : 0) : "memory");
      });
    });
    STAMP(12);
    asm volatile("s_barrier" ::: "memory");
    STAMP_OUT();
    return;
  }

  const Early early = early_issue(gws, rx, 8 * b + 2 * w, lane);
  u32 E;
  auto touches = [&]() {
    if (att || ffn) {
      constexpr u32 OOB = 0x7ffffff0u;
      const u32 lb = (u32)(size_t)L;
      { const u32 g15 = att ? (slot & 7) : 8 + (jf >= 7 ? jf - 7 : jf), i = (g15 * 4 + w) * 22 + lane;
        touch(rx, (lane < 22 && i < 1280) ? (256 * rga + (i & 255)) * XROW + 128 * (2 + (i >> 8)) : OOB, lb); }
      if (att) {
        const rsrc_t rq = __builtin_amdgcn_make_buffer_rsrc((void *)gwqkv, 0, DM * QKVROW, 0x00020000);
        const u32 i = slot * 240 + w * 60 + lane, row = (i * 43691u) >> 19, rem = i - 12 * row;
        touch(rq, lane < 60 ? (128 + row) * QKVROW + 896 * (rem >> 2) + ((448 * (xcd & 1)) & ~127u) + 128 * (rem & 3) : OOB, lb);
      } else {
        const rsrc_t r1 = __builtin_amdgcn_make_buffer_rsrc((void *)gw1, 0, DM * W1ROW, 0x00020000);
        const u32 l8 = 4 * (jf >= 7) + w, i = 64 * l8 + lane, j = 512 + 16 * l8 + lane;
        touch(r1, (128 + (i >> 1)) * W1ROW + 256 * ct + 128 * (i & 1), lb);
        touch(r1, lane < 16 ? (128 + (j >> 1)) * W1ROW + 256 * ct + 128 * (j & 1) : OOB, lb);
      }
    }
  };
  if (TOUCH_A == 2) touches();
  auto first = [&]() {
    E = early_finish(early, rws, 8 * b + 2 * w, lane); if (tid == 0) *(LAS u32 *)(L + L_E) = E;
    if (TOUCH_A == 1) touches();
  };
  const int rh = w & 1, kg = w >> 1;
  const u32 rb = 8 * xcd + 2 * rt + rh;
  const u32 row = 32 * rb + (lane & 31), col0 = 64 * cc + 32 * kg;
  u32x4 pq[2][2][2];
  auto o_stage = [&]() {
    u32x4 kf[2][2];
    for (int t = 0; t < 2; ++t) for (int s = 0; s < 2; ++s) kf[t][s] = *(LAS u32x4 *)(L + L_KVF + ((t * 2 + s) * 64 + lane) * 16);
    f32x16 num[2] = {zero16(), zero16()};
    for (int t = 0; t < 2; ++t) for (int s = 0; s < 2; ++s) for (int r = 0; r < 2; ++r) num[r] = mfma(kf[t][s], pq[r][t][s], num[r]);
    for (int r = 0; r < 2; ++r) {
      const float rc = __builtin_amdgcn_rcpf(both_halves(num[r][12]).x + 1e-6f);
      for (int i = 0; i < 16; i += 2) { const f32x2 v = f32x2{num[r][i], num[r][i + 1]} * rc; num[r][i] = v.x; num[r][i + 1] = v.y; }
      if (!h) { num[r][12] = 0.f; num[r][13] = 0.f; num[r][14] = 0.f; num[r][15] = 0.f; }
      const u32 so = WS_O + ((8 * rga + 2 * w + r) * 32 + 2 * head) * 1024;
      st128_sc1(pack8(num[r], 0), rws, lane * 16, so);
      st128_sc1(pack8(num[r], 1), rws, lane * 16, so + 1024);
    }
  };
  if (att) {
    if (tid < 16) *(LAS u32 *)(L + L_ZERO + 4 * tid) = 0;
    u32x4 idf[2];
    for (int s = 0; s < 2; ++s)
      for (int d = 0; d < 4; ++d) {
        const int k0 = 16 * s + 8 * (d >> 1) + 4 * h + 2 * (d & 1), n = lane & 31;
        idf[s][d] = (n == k0 ? 0x3F80u : 0u) | (n == k0 + 1 ? 0x3F800000u : 0u);
      }
    for (int s = 0; s < 2; ++s) asm volatile("" : "+v"(idf[s]));
    f32x16 acc[2][3]; u32x4 stat[2];
    const u32 vostat = WS_STAT + (256 * rga + 64 * w + (lane & 31)) * 16;
    gemmA_compute<true>(L, w, lane, acc, first, [&]() { if (TOUCH_A == 3) touches(); }, [&]() { for (int r = 0; r < 2; ++r) stat[r] = ld128_sc1(rws, vostat + 512 * r, 0); } STAMP_PASS);
    RowStat st[2];
    for (int r = 0; r < 2; ++r) {
      st[r] = row_stats(stat_settle(stat[r], rws, vostat + 512 * r, E), h);
      *(LAS float *)(L + L_TOK + w * 256 + r * 128 + 4 * (lane & 31)) = st[r].rstd;
    }
    BAR();
    STAMP(3);
    {
      const u32x4 wk = waug(cs_read(L, 0, lane), 0), wv = waug(cs_read(L, 1, lane), 0), wq = waug(cs_read(L, 2, lane), 0);
      for (int r = 0; r < 2; ++r) { acc[r][0] = mfma(wk, st[r].xaug, acc[r][0]); acc[r][1] = mfma(st[r].xaug, wv, acc[r][1]); acc[r][2] = mfma(wq, st[r].xaug, acc[r][2]); }
    }
    u32x4 wp[2];
    {
      const int blk = (lane >> 4) & 1, q = (lane & 15) >> 2, p = lane & 3;
      const u32 col = (blk && p == 3) ? 24 : 16 * blk + 4 * p;
      for (int s = 0; s < 2; ++s) {
        u32x2 r[2];
        for (int t = 0; t < 2; ++t) {
          const int dp = 16 * s + 8 * t + 4 * h + q;
          const u32 a = (dp >= 24 && dp < 28) ? L_ZERO + 8 * p : L_WPHI + (dp >= 28 ? dp - 4 : dp) * 56 + col * 2;
          r[t] = trd(L + a);
        }
        wp[s] = u32x4{r[0].x, r[0].y, r[1].x, r[1].y};
      }
    }
    f32x16 kvp[2] = {zero16(), zero16()};
    {
      f32x16 P[2];
      for (int r = 0; r < 2; ++r) P[r] = mfma(wp[0], pack8(acc[r][0], 0), zero16());
      for (int r = 0; r < 2; ++r) P[r] = mfma(wp[1], pack8(acc[r][0], 1), P[r]);
      u32x4 vf[2][2];
      for (int r = 0; r < 2; ++r) {
        f32x16 v = acc[r][1];
        for (int g = 0; g < 4; ++g) {
          const f32x4 rs = *(LAS f32x4 *)(L + L_TOK + w * 256 + r * 128 + (8 * g + 4 * h) * 4);
          for (int e = 0; e < 4; e += 2) { const f32x2 t2 = f32x2{v[4 * g + e], v[4 * g + e + 1]} * f32x2{rs[e], rs[e + 1]}; v[4 * g + e] = t2.x; v[4 * g + e + 1] = t2.y; }
        }
        for (int s = 0; s < 2; ++s) { vf[r][s] = pack8(v, s); if ((lane & 31) == 24) vf[r][s] = u32x4{0x3F803F80u, 0x3F803F80u, 0x3F803F80u, 0x3F803F80u}; }
      }
      u32x4 pf[2][2][2];
      for (int r = 0; r < 2; ++r) feature_map(P[r], st[r].rstd, h, pf[r]);
      f32x16 Z[2][2];
      for (int r = 0; r < 2; ++r) for (int t = 0; t < 2; ++t) Z[r][t] = mfma(pf[r][t][0], idf[0], zero16());
      for (int r = 0; r < 2; ++r) for (int t = 0; t < 2; ++t) Z[r][t] = mfma(pf[r][t][1], idf[1], Z[r][t]);
      for (int r = 0; r < 2; ++r) for (int s = 0; s < 2; ++s) for (int t = 0; t < 2; ++t) kvp[t] = mfma(pack8(Z[r][t], s), vf[r][s], kvp[t]);
    }
    for (int t = 0; t < 2; ++t)
      for (int g = 0; g < 4; ++g)
        *(LAS f32x4 *)(L + (2 + t) * L_SLOT + (g >> 1) * 8192 + (g & 1) * 4096 + w * 1024 + lane * 16) = f32x4{kvp[t][4 * g], kvp[t][4 * g + 1], kvp[t][4 * g + 2], kvp[t][4 * g + 3]};
    STAMP(4);
    BAR();
    {
      f32x16 P[2];
      for (int r = 0; r < 2; ++r) P[r] = mfma(wp[0], pack8(acc[r][2], 0), zero16());
      for (int r = 0; r < 2; ++r) P[r] = mfma(wp[1], pack8(acc[r][2], 1), P[r]);
      for (int r = 0; r < 2; ++r) {
        feature_map(P[r], st[r].rstd, h, pq[r]);
        for (int t = 0; t < 2; ++t) for (int s = 0; s < 2; ++s) asm volatile("" : "+v"(pq[r][t][s]));
      }
    }
    STAMP(6);
    if (!has_tile) {
      BAR();
      o_stage();
      vmwait<0>();
      if (lane < 2) st_flag(gws, WS_OF + ((8 * rga + 2 * w + lane) * 16 + head) * 64, E);
      STAMP_OUT();
      return;
    }
  } else if (ffn) {
    u32 bias[4];
    for (int t = 0; t < 4; ++t) bias[t] = *(const GAS unsigned short *)(gb1 + (128 * ct + 32 * t + (lane & 31)) * 2);
    f32x16 acc[2][4]; u32x4 stat[2];
    const u32 vostat = WS_STAT + (256 * rga + 64 * w + (lane & 31)) * 16;
    gemmA_compute<false>(L, w, lane, acc, first, [&]() { if (TOUCH_A == 3) touches(); }, [&]() { for (int r = 0; r < 2; ++r) stat[r] = ld128_sc1(rws, vostat + 512 * r, 0); } STAMP_PASS);
    RowStat st[2];
    for (int r = 0; r < 2; ++r) st[r] = row_stats(stat_settle(stat[r], rws, vostat + 512 * r, E), h);
    BAR();
    STAMP(3);
    if (HOLD == 2) __builtin_amdgcn_s_setprio(3);
    for (int t = 0; t < 4; ++t) {
      const u32x4 wa = waug(cs_read(L, t, lane), bias[t] | (bias[t] << 16));
      for (int r = 0; r < 2; ++r) {
        f32x16 a = mfma(wa, st[r].xaug, acc[r][t]);
        for (int i = 0; i < 16; i += 2) { f32x2 v = {relu(a[i]), relu(a[i + 1])}; v *= v; a[i] = v.x; a[i + 1] = v.y; }
        const u32 so = WS_H + ((8 * rga + 2 * w + r) * 112 + 8 * ct + 2 * t) * 1024;
        st128_sc1(pack8(a, 0), rws, lane * 16, so);
        st128_sc1(pack8(a, 1), rws, lane * 16, so + 1024);
      }
      if (t == 2) {
        vmwait<4>();
        if (lane < 2) st_flag(gws, WS_HFA + ((8 * rga + 2 * w + lane) * 14 + ct) * 64, E);
      }
    }
    if (HOLD == 1) asm volatile("s_barrier" ::: "memory");
    if (HOLD == 2) __builtin_amdgcn_s_setprio(0);
    STAMP(4);
    vmwait<0>();
    if (lane < 2) st_flag(gws, WS_HFB + ((8 * rga + 2 * w + lane) * 14 + ct) * 64, E);
    STAMP(5);
    for (int t = 0; t < 2; ++t) for (int r = 0; r < 2; ++r) for (int s = 0; s < 2; ++s) pq[t][r][s] = u32x4{0, 0, 0, 0};
  } else {
    warm_up(pc0, rx, rws, gwqkv, gw1, gw2, gwo, xcd, (slot - 30) * 8 + w, lane, (u32)(size_t)L);
    first();
    for (int t = 0; t < 2; ++t) for (int r = 0; r < 2; ++r) for (int s = 0; s < 2; ++s) pq[t][r][s] = u32x4{0, 0, 0, 0};
  }

  {
    constexpr int RD = RING;
    const u32 soh = WS_H + (rb * 112 + 56 * kg) * 1024, soo = WS_O + (rb * 32 + 16 * kg) * 1024;
    u32x4 ring[RD];
    u32x2 b2v[4]; u32x4 sg0;
    const int blk = (lane >> 4) & 1, q = (lane & 15) >> 2, p = lane & 3;
    u32 bb0[2], bb[2];
    for (int c2 = 0; c2 < 2; ++c2) bb0[c2] = (u32)(size_t)L + kg * 8192 + (4 * h + q) * 128 + ((c2 ^ kg ^ (q >> 1)) * 64) + 32 * blk + 8 * p;
    f32x16 af[2] = {zero16(), zero16()}, aa[2] = {zero16(), zero16()};
    constexpr int PD = BPD;
    u32x4 bf[PD + 1][2];
    const u32 hfb = WS_HFB + (rb * 14 + 7 * kg + ((lane & 7) < 7 ? (lane & 7) : 0)) * 64, ofo = WS_OF + (rb * 16 + 8 * kg + (lane & 7)) * 64;
    u32 look = 0, soe = 0;
    bool o_early = false;
    float rs2; f32x2 bx[8], sg[8], xx[8];
    u32x4 oa[16];
    poll_all(gws, WS_HFA + (rb * 14 + 7 * kg + ((lane & 7) < 7 ? (lane & 7) : 0)) * 64, E);
    STAMP(10);
    if (!att) BAR();
    sfor<0, 72>([&](auto I) {
      constexpr int i = I;
      if constexpr (i == 56) {
        for (int g = 0; g < 4; ++g)
          *(LAS f32x4 *)(L + 6 * L_SLOT + w * 4096 + g * 1024 + lane * 16) = f32x4{af[1][4 * g], af[1][4 * g + 1], af[1][4 * g + 2], af[1][4 * g + 3]};
        BAR();
        for (int g = 0; g < 4; ++g) {
          const f32x4 pf = *(LAS f32x4 *)(L + 6 * L_SLOT + (w ^ 2) * 4096 + g * 1024 + lane * 16);
          for (int e = 0; e < 2; ++e) {
            const int j = 4 * g + 2 * e;
            const f32x2 y = (f32x2{af[0][j], af[0][j + 1]} + f32x2{pf[2 * e], pf[2 * e + 1]}) * rs2 + bx[2 * g + e];
            const f32x2 t = y * -LOG2E;
            const f32x2 d = f32x2{__builtin_amdgcn_exp2f(t.x), __builtin_amdgcn_exp2f(t.y)} + 1.0f;
            sg[2 * g + e] = y * f32x2{__builtin_amdgcn_rcpf(d.x), __builtin_amdgcn_rcpf(d.y)};
            asm volatile("" : "+v"(sg[2 * g + e]));
          }
        }
        STAMP(14);
        if (!o_early) {
          if (!__all(look == E)) poll_all(gws, ofo, E);
          for (int j = 0; j < OB; ++j) oa[j] = ld128_sc1(rws, lane * 16, soo + j * 1024);
        }
        __builtin_amdgcn_sched_barrier(0);
      }
      if constexpr (i >= 16 && (i & 7) == 0 && i != 56) asm volatile("s_barrier" ::: "memory");
      if constexpr (i == 0) STAMP(11);
      if constexpr (i == 0) {
        for (int j = 0; j < RD; ++j) ring[j] = ld128_sc1(rws, lane * 16, soh + h_step(j) * 1024);
        __builtin_amdgcn_sched_barrier(0);
        for (int c2 = 0; c2 < 2; ++c2) { bb[c2] = bb0[c2] + 4 * L_SLOT; asm volatile("" : "+v"(bb[c2])); for (int j = 0; j < PD; ++j) bf[j][c2] = trfrag((lchar *)(size_t)bb[c2] + j * 2048, 1024); }
      }
      static_assert(28 - RD - 6 < 16, "no look between the o stores and their counted wait");
      if constexpr (i == 28 - RD - 6) look = ld_flag(gws, hfb);
      if constexpr (i == 28 - RD) { if (!__all(look == E)) poll_all(gws, hfb, E); }
      if constexpr (i == 44) {
        for (int g = 0; g < 4; ++g) b2v[g] = *(const GAS u32x2 *)(gb2 + (col0 + 8 * g + 4 * h) * 2);
        sg0 = ld128_sc1(rws, WS_STAT + row * 16, 0);
      }
      if constexpr (i == 40) look = ld_flag(gws, ofo);
      if constexpr (i == 47) { o_early = __all(look == E); soe = o_early ? soo : WS_END; }
      if constexpr (i >= 48 && i < 48 + OB) oa[i - 48] = ld128_sc1(rws, lane * 16, soe + (i - 48) * 1024);
      if constexpr (i == 50) look = ld_flag(gws, ofo);
      if constexpr (i == 52) {
        u32x4 g4 = sg0;
        if (!__all(g4[1] == E && g4[3] == E)) g4 = stat_settle(g4, rws, WS_STAT + row * 16, E);
        const float rs1 = __builtin_amdgcn_rcpf(asf(g4[2]));
        rs2 = rs1 * rs1;
        for (int g = 0; g < 4; ++g) for (int e = 0; e < 2; ++e) { bx[2 * g + e] = f32x2{bf2f(b2v[g][e]), bfhi(b2v[g][e])}; asm volatile("" : "+v"(bx[2 * g + e])); }
      }
      if constexpr (i == 60) {
        for (int g = 0; g < 4; ++g) {
          const u32 r64 = 32 * rh + (lane & 31);
          const u32x2 xr = *(LAS u32x2 *)(L + L_XT + r64 * 128 + 16 * ((4 * kg + g) ^ ((r64 >> 1) & 7)) + 8 * h);
          for (int e = 0; e < 2; ++e) { xx[2 * g + e] = f32x2{bf2f(xr[e]), bfhi(xr[e])}; asm volatile("" : "+v"(xx[2 * g + e])); }
        }
      }
      if constexpr (i == 16) { if (att) o_stage(); }
      if constexpr (i == 20) {
        if (att) { vmwait<4>(); if (lane < 2) st_flag(gws, WS_OF + ((8 * rga + 2 * w + lane) * 16 + head) * 64, E); }
      }
      u32x4 a;
      if constexpr (i < 56) a = ring[i % RD]; else a = oa[i - 56];
      if constexpr (i + PD < 72) {
        constexpr int n = i + PD;
        if constexpr ((n & 3) == 0) for (int c2 = 0; c2 < 2; ++c2) { bb[c2] = bb0[c2] + (((n >> 2) + 4) % NSLOT) * L_SLOT; asm volatile("" : "+v"(bb[c2])); }
        for (int c2 = 0; c2 < 2; ++c2) bf[n % (PD + 1)][c2] = trfrag((lchar *)(size_t)bb[c2] + (n & 3) * 2048, 1024);
      }
      if constexpr (i < 56) { af[0] = mfma(bf[i % (PD + 1)][0], a, af[0]); af[1] = mfma(bf[i % (PD + 1)][1], a, af[1]); asm volatile("" : "+v"(af[0]), "+v"(af[1])); }
      else { aa[0] = mfma(bf[i % (PD + 1)][0], a, aa[0]); aa[1] = mfma(bf[i % (PD + 1)][1], a, aa[1]); asm volatile("" : "+v"(aa[0]), "+v"(aa[1])); }
      if constexpr (i + RD < 56) ring[i % RD] = ld128_sc1(rws, lane * 16, soh + h_step(i + RD) * 1024);
      if constexpr (i >= 56 && i - 56 + OB < 16) oa[i - 56 + OB] = ld128_sc1(rws, lane * 16, soo + (i - 56 + OB) * 1024);
      __builtin_amdgcn_sched_barrier(0);
    });
    STAMP(12);
    for (int g = 0; g < 4; ++g)
      *(LAS f32x4 *)(L + 8 * L_SLOT + w * 4096 + g * 1024 + lane * 16) = f32x4{aa[1][4 * g], aa[1][4 * g + 1], aa[1][4 * g + 2], aa[1][4 * g + 3]};
    BAR();
    u32x2 o2[4];
    for (int g = 0; g < 4; ++g) {
      const f32x4 pa = *(LAS f32x4 *)(L + 8 * L_SLOT + (w ^ 2) * 4096 + g * 1024 + lane * 16);
      for (int e = 0; e < 2; ++e) {
        const int i = 4 * g + 2 * e;
        const f32x2 r = (f32x2{aa[0][i], aa[0][i + 1]} + f32x2{pa[2 * e], pa[2 * e + 1]}) * sg[2 * g + e] + xx[2 * g + e];
        o2[g][e] = pk2(r.x, r.y);
      }
    }
    const rsrc_t rout = __builtin_amdgcn_make_buffer_rsrc((void *)gout, 0, 2048 * XROW, 0x00020000);
    for (int g = 0; g < 4; g += 2) {
      auto rx0 = __builtin_amdgcn_permlane32_swap(o2[g].x, o2[g + 1].x, false, false);
      auto ry0 = __builtin_amdgcn_permlane32_swap(o2[g].y, o2[g + 1].y, false, false);
      __builtin_amdgcn_raw_buffer_store_b128(u32x4{rx0[0], ry0[0], rx0[1], ry0[1]}, rout, row * XROW + (col0 + 8 * (g + h)) * 2, 0, OUT_AUX);
    }
    STAMP(13);
    STAMP(15);
    STAMP_OUT();
  }
}

extern "C" void kernel_launch(void *const *d_in, const int *in_sizes, int n_in, void *d_out, int out_size, void *d_ws, size_t ws_size, hipStream_t stream) {
  fused_hedgehog_ffn<<<256, 512, 0, stream>>>((const char *)d_in[0], (const char *)d_in[1], (const char *)d_in[2], (const char *)d_in[3], (const char *)d_in[5],
                                              (const char *)d_in[6], (const char *)d_in[7], (const char *)d_in[8], (char *)d_out, (char *)d_ws);
}
```

```cpp
#include <hip/hip_runtime.h>
#include <hip/hip_bf16.h>
#include <stdint.h>
#include <type_traits>

typedef unsigned u32;
typedef u32 u32x4 __attribute__((ext_vector_type(4)));
typedef u32 u32x2 __attribute__((ext_vector_type(2)));
typedef float f32x16 __attribute__((ext_vector_type(16)));
typedef float f32x4 __attribute__((ext_vector_type(4)));
typedef float f32x2 __attribute__((ext_vector_type(2)));
typedef short s16x8 __attribute__((ext_vector_type(8)));
typedef short s16x4 __attribute__((ext_vector_type(4)));
typedef __bf16 bf16x2 __attribute__((ext_vector_type(2)));
typedef __bf16 bf16x8 __attribute__((ext_vector_type(8)));
#define LAS __attribute__((address_space(3)))
#define GAS __attribute__((address_space(1)))
#define DI __device__ __forceinline__
typedef __amdgpu_buffer_rsrc_t rsrc_t;
typedef LAS char lchar;

namespace {
constexpr int DM = 448, DF = 1792;
constexpr u32 XROW = DM * 2;
constexpr u32 QKVROW = 3 * DM * 2;
constexpr u32 W1ROW = DF * 2;
constexpr u32 WS_EPOCH = 0;
constexpr u32 WS_STAT = 4096;
constexpr u32 WS_KF = 65536;
constexpr u32 WS_HFA = 98304;
constexpr u32 WS_HFB = WS_HFA + 57344;
constexpr u32 WS_OF = WS_HFB + 57344;
constexpr u32 WS_KVP = 294912;
constexpr u32 WS_H = WS_KVP + (1u << 20);
constexpr u32 WS_O = WS_H + 64 * 112 * 1024;
constexpr u32 WS_END = WS_O + 64 * 32 * 1024;
constexpr u32 L_SLOT = 16384, NSLOT = 9;
constexpr u32 A_SLOT = 49152, A_WOFF = 32768;
constexpr u32 L_MISC = NSLOT * L_SLOT;
constexpr u32 L_WPHI = L_MISC;
constexpr u32 L_ZERO = L_MISC + 2048;
constexpr u32 L_E = L_MISC + 2112;
constexpr u32 L_DUMMY = L_MISC + 2304;
constexpr u32 L_CSP = L_MISC + 2560;
constexpr u32 L_TOK = L_CSP + 512;
constexpr u32 L_KVF = L_TOK + 1024;
constexpr u32 L_XT = L_KVF + 4096;
constexpr u32 L_END = L_XT + 8192;
constexpr u32 LDS_BYTES = 163840;
static_assert(L_END <= LDS_BYTES, "lds");
constexpr int SPIN_CAP = 1 << 16;
constexpr float LOG2E = 1.4426950408889634f;
#define CS_DOT2 0
#define SGB 1
#define LPRIO 0
#define FIRST_AT -1
#define TOUCH_A 0
#define POLL_SLEEP 3
#define HOLD 1
#define XPRIO 3
#define WARM 15
#define BPD 3
#define OB 8
#define FFN_ALL 0
#define OFD 10
#define OUT_AUX 0
#define SPREAD 0
#define PIN_C 0
#define RING 10

#define STAMP(i) do {} while (0)
#define STAMP_DECL do {} while (0)
#define STAMP_OUT() do {} while (0)
#define STAMP_ARG
#define STAMP_PASS
template <int B, int E, class F> DI void sfor(F &&f) {
  if constexpr (B < E) { f(std::integral_constant<int, B>{}); sfor<B + 1, E>(f); }
}
DI u32 rfl(u32 v) { return __builtin_amdgcn_readfirstlane(v); }
DI f32x16 mfma(u32x4 a, u32x4 b, f32x16 c) {
  return __builtin_amdgcn_mfma_f32_32x32x16_bf16(__builtin_bit_cast(bf16x8, a), __builtin_bit_cast(bf16x8, b), c, 0, 0, 0);
}
DI u32 pk2(float lo, float hi) { f32x2 p = {lo, hi}; return __builtin_bit_cast(u32, __builtin_convertvector(p, bf16x2)); }
DI float dot2(u32 a, u32 b, float c) { return __builtin_amdgcn_fdot2_f32_bf16(__builtin_bit_cast(bf16x2, a), __builtin_bit_cast(bf16x2, b), c, false); }
DI float bf2f(u32 lo16) { return __builtin_bit_cast(float, lo16 << 16); }
DI float bfhi(u32 v) { return __builtin_bit_cast(float, v & 0xffff0000u); }
DI float asf(u32 v) { return __builtin_bit_cast(float, v); }
DI f32x2 both_halves(float v) {
  const u32 a = __builtin_bit_cast(u32, v);
  auto r = __builtin_amdgcn_permlane32_swap(a, a, false, false);
  return f32x2{asf(r[0]), asf(r[1])};
}
DI float half_lo(float v) { return both_halves(v).x; }
DI u32x4 pack8(const f32x16 &x, int s) { return u32x4{pk2(x[8 * s], x[8 * s + 1]), pk2(x[8 * s + 2], x[8 * s + 3]), pk2(x[8 * s + 4], x[8 * s + 5]), pk2(x[8 * s + 6], x[8 * s + 7])}; }
DI u32x2 trd(lchar *p) {
  typedef short v4i16 __attribute__((ext_vector_type(4)));
  return __builtin_bit_cast(u32x2, __builtin_amdgcn_ds_read_tr16_b64_v4i16((LAS v4i16 *)p));
}
DI u32x4 trfrag(lchar *p, int tstride) { u32x2 a = trd(p), b = trd(p + tstride); return u32x4{a.x, a.y, b.x, b.y}; }
DI float relu(float v) { return __builtin_amdgcn_fmed3f(v, 0.f, __builtin_inff()); }
DI f32x16 zero16() { f32x16 z; for (int i = 0; i < 16; ++i) z[i] = 0.f; return z; }
#define BAR() asm volatile("s_waitcnt lgkmcnt(0)\n\ts_barrier" ::: "memory")
template <int N> DI void vmwait() { asm volatile("s_waitcnt vmcnt(%0)" ::"i"(N) : "memory"); }
DI void dma16(rsrc_t r, u32 voff, u32 soff, u32 ldsaddr) {
  asm volatile("s_mov_b32 m0, %0\n\ts_nop 0\n\tbuffer_load_dwordx4 %1, %2, %3 offen lds" ::"s"(ldsaddr), "v"(voff), "s"(r), "s"(soff) : "memory", "m0");
}
template <int OFF> DI void ald128(u32x4 &d, rsrc_t r, u32 voff, u32 soff) {
  asm volatile("buffer_load_dwordx4 %0, %1, %2, %3 offen offset:%4" : "=v"(d) : "v"(voff), "s"(r), "s"(soff), "i"(OFF) : "memory");
}
DI u32x4 ld128_sc1(rsrc_t r, u32 voff, u32 soff) { return __builtin_amdgcn_raw_buffer_load_b128(r, voff, soff, 16); }
DI void st128_sc1(u32x4 v, rsrc_t r, u32 voff, u32 soff) {
  __builtin_amdgcn_raw_buffer_store_b128(v, r, voff, soff, 16);
  asm volatile("s_nop 1" ::"v"(v) : "memory");
}
typedef GAS u32 gu32;
DI u32 ld_flag(const char *ws, u32 off) { return __hip_atomic_load((gu32 *)(ws + off), __ATOMIC_RELAXED, __HIP_MEMORY_SCOPE_AGENT); }
DI void st_flag(char *ws, u32 off, u32 v) { __hip_atomic_store((gu32 *)(ws + off), v, __ATOMIC_RELAXED, __HIP_MEMORY_SCOPE_AGENT); }
DI void poll_all(const char *ws, u32 off, u32 E) {
#pragma unroll 1
  for (int spin = 0; spin < SPIN_CAP; ++spin) {
    if (__all(ld_flag(ws, off) == E)) break;
    __builtin_amdgcn_s_sleep(POLL_SLEEP);
  }
  __builtin_amdgcn_fence(__ATOMIC_ACQUIRE, "wavefront");
}
template <u32 LI, u32 SI> DI void dmai(rsrc_t r, u32 voff, u32 sob, u32 ldsb) {
  u32 t;
  asm volatile("s_add_u32 m0, %2, %5\n\ts_add_u32 %0, %1, %6\n\tbuffer_load_dwordx4 %3, %4, %0 offen lds"
               : "=&s"(t) : "s"(sob), "s"(ldsb), "v"(voff), "s"(r), "i"(LI), "i"(SI) : "memory", "m0", "scc");
}
template <int CTRL, int RMASK> DI float dpp_add(float v) {
  return v + asf(__builtin_amdgcn_update_dpp(0u, __builtin_bit_cast(u32, v), CTRL, RMASK, 0xf, true));
}
DI float wave_sum63(float v) {
  v = dpp_add<0x111, 0xf>(v); v = dpp_add<0x112, 0xf>(v); v = dpp_add<0x114, 0xf>(v); v = dpp_add<0x118, 0xf>(v);
  v = dpp_add<0x142, 0xa>(v); v = dpp_add<0x143, 0xc>(v);
  return v;
}
DI void st64_sc1(char *ws, u32 off, u32 lo, u32 hi) {
  __hip_atomic_store((GAS unsigned long long *)(ws + off), (unsigned long long)lo | ((unsigned long long)hi << 32), __ATOMIC_RELAXED, __HIP_MEMORY_SCOPE_AGENT);
}
struct Early { u32 ev; u32x4 xa, xb; };
DI Early early_issue(char *ws, rsrc_t rx, int row0, int lane) {
  Early e;
  e.ev = ld_flag(ws, WS_EPOCH);
  const u32 vo = lane < 56 ? (u32)lane * 16 : 0x7ffffff0u;
  e.xa = __builtin_amdgcn_raw_buffer_load_b128(rx, vo, row0 * XROW, 0); e.xb = __builtin_amdgcn_raw_buffer_load_b128(rx, vo, (row0 + 1) * XROW, 0);
  return e;
}
DI u32 early_finish(const Early &e, rsrc_t rws, int row0, int lane) {
  const u32 E = rfl(e.ev) + 1, ONES = 0x3F803F80u;
  float s[2], q[2];
  for (int r = 0; r < 2; ++r) {
    const u32x4 &x = r ? e.xb : e.xa;
    s[r] = dot2(x[0], ONES, 0.f); q[r] = dot2(x[0], x[0], 0.f);
    for (int d = 1; d < 4; ++d) { s[r] = dot2(x[d], ONES, s[r]); q[r] = dot2(x[d], x[d], q[r]); }
    s[r] = wave_sum63(s[r]); q[r] = wave_sum63(q[r]);
    const float mean = s[r] * (1.0f / DM);
    const float sig = __builtin_amdgcn_sqrtf(fmaxf(q[r] * (1.0f / DM) - mean * mean, 0.f) + 1e-5f);
    const u32 vo = lane == 63 ? WS_STAT + (row0 + r) * 16 : 0x7ffffff0u;
    __builtin_amdgcn_raw_buffer_store_b64(u32x2{__builtin_bit_cast(u32, mean), E}, rws, vo, 0, 16);
    __builtin_amdgcn_raw_buffer_store_b64(u32x2{__builtin_bit_cast(u32, sig), E}, rws, vo + 8, 0, 16);
  }
  return E;
}
DI u32x4 stat_settle(u32x4 g, rsrc_t rws, u32 vostat, u32 E) {
#pragma unroll 1
  for (int spin = 0; spin < SPIN_CAP && !__all(g[1] == E && g[3] == E); ++spin) { __builtin_amdgcn_s_sleep(1); g = ld128_sc1(rws, vostat, 0); }
  return g;
}
struct RowStat { float rstd; u32x4 xaug; };
DI RowStat row_stats(u32x4 g, int h) {
  const float m = -asf(g[0]), sig = asf(g[2]);
  RowStat r; r.rstd = __builtin_amdgcn_rcpf(sig);
  const float ml = m - bfhi(pk2(m, m)), sl = sig - bfhi(pk2(sig, sig));
  const u32 mm = pk2(m, ml), ss = pk2(sig, sl);
  r.xaug = h ? u32x4{0, 0, 0, 0} : u32x4{mm, mm, ss, 0};
  return r;
}
DI u32x4 waug(float cs, u32 bb) {
  const u32 hh = pk2(cs, cs); const float cl = cs - bfhi(hh);
  return u32x4{hh, pk2(cl, cl), bb, 0};
}
DI float cs_read(lchar *L, int t, int lane) { return *(LAS float *)(L + L_CSP + (t * 32 + (lane & 31)) * 4); }

DI void touch(rsrc_t r, u32 voff, u32 lb) {
  asm volatile("s_mov_b32 m0, %0\n\ts_nop 0\n\tbuffer_load_dword %1, %2, 0 offen lds" ::"s"(lb + L_DUMMY), "v"(voff), "s"(r) : "memory", "m0");
}
constexpr u32 CODE_BYTES = 50 * 1024, CODE_PIECES = (CODE_BYTES + 8191) / 8192;
DI void warm_up(unsigned long long pc, rsrc_t rx, rsrc_t rws, const char *gwqkv, const char *gw1, const char *gw2, const char *gwo, int xcd, int wv, int lane, u32 lb) {
  if (WARM & 8) {
    const rsrc_t rq = __builtin_amdgcn_make_buffer_rsrc((void *)gwqkv, 0, DM * QKVROW, 0x00020000), r1 = __builtin_amdgcn_make_buffer_rsrc((void *)gw1, 0, DM * W1ROW, 0x00020000);
#pragma unroll 1
    for (int q = xcd * 16 + wv; q < 405; q += 128) {
      const int c = 2 + q / 81, r = q % 81;
      if (r < 32) touch(rx, (64 * r + lane) * XROW + 128 * c, lb);
      else if (r < 53) { const u32 j = (r - 32) * 64 + lane, row = (j * 3121u) >> 16; touch(rq, (64 * c + row) * QKVROW + 128 * (j - 21 * row), lb); }
      else { const u32 j = (r - 53) * 64 + lane, row = (j * 2341u) >> 16; touch(r1, (64 * c + row) * W1ROW + 128 * (j - 28 * row), lb); }
    }
  }
  if (WARM & 1) {
    const rsrc_t rc = __builtin_amdgcn_make_buffer_rsrc((void *)(pc & ~127ull), 0, CODE_BYTES, 0x00020000);
    if (wv < (int)CODE_PIECES) touch(rc, wv * 8192 + lane * 128, lb);
  }
  const int g = xcd * 16 + wv;
  if (WARM & 2) { if (g < 27) touch(rws, WS_KF + g * 8192 + lane * 128, lb); }
  if (WARM & 4) {
    const rsrc_t r2 = __builtin_amdgcn_make_buffer_rsrc((void *)gw2, 0, DF * XROW, 0x00020000), ro = __builtin_amdgcn_make_buffer_rsrc((void *)gwo, 0, DM * XROW, 0x00020000);
    touch(r2, g * 8192 + lane * 128, lb);
    if (g < 68) touch(r2, (128 + g) * 8192 + lane * 128, lb);
    else if (g < 117) touch(ro, (g - 68) * 8192 + lane * 128, lb);
  }
}
template <bool ATT, class First, class After0, class Mid>
DI void gemmA_compute(lchar *L, int w, int lane, f32x16 (&acc)[2][ATT ? 3 : 4], First &&first, After0 &&after0, Mid &&mid STAMP_ARG) {
  constexpr int T = ATT ? 3 : 4, ROWB = ATT ? 64 : 256;
  const int h = lane >> 5, blk = (lane >> 4) & 1, q = (lane & 15) >> 2, p = lane & 3;
  const u32 lb = (u32)(size_t)L;
  u32 wb0[T], xb0[4], wb[T], xb[4], cb0, cb;
  for (int t = 0; t < T; ++t) wb0[t] = lb + A_WOFF + (ATT ? t * 4096 + (8 * h + q) * 64 : (8 * h + q) * 256 + 64 * (t ^ q)) + 32 * blk + 8 * p;
  for (int u = 0; u < 4; ++u) xb0[u] = lb + w * 8192 + (lane & 31) * 128 + 16 * ((2 * u + h) ^ ((lane >> 1) & 7));
  const int tw = w < T ? w : T - 1;
  cb0 = lb + A_WOFF + (ATT ? tw * 4096 + (8 * h + q) * 64 : (8 * h + q) * 256 + 64 * (tw ^ q)) + 32 * blk + 8 * p;
  f32x2 ca = {0.f, 0.f};
  for (int r = 0; r < 2; ++r) for (int t = 0; t < T; ++t) acc[r][t] = zero16();
  u32x4 xf[2][2], wf[2][T], cf[2];
  sfor<0, 29>([&](auto I) {
    constexpr int i = I, c = i >> 2, u = i & 3;
    if constexpr (i == 0 && FIRST_AT < 0) { first(); STAMP(14); }
    if constexpr (u == 0 && i < 28) {
      BAR();
      if constexpr (c == 0) STAMP(1);
      constexpr u32 so = (c % 3) * A_SLOT;
      for (int t = 0; t < T; ++t) { wb[t] = wb0[t] + so; asm volatile("" : "+v"(wb[t])); }
      for (int j = 0; j < 4; ++j) { xb[j] = xb0[j] + so; asm volatile("" : "+v"(xb[j])); }
      cb = cb0 + so; asm volatile("" : "+v"(cb));
    }
    if constexpr (i < 28) {
      cf[i & 1] = trfrag((lchar *)(size_t)cb + 16 * u * ROWB, 4 * ROWB);
      for (int r = 0; r < 2; ++r) xf[i & 1][r] = *(LAS u32x4 *)((lchar *)(size_t)xb[u] + r * 4096);
      for (int t = 0; t < T; ++t) wf[i & 1][t] = trfrag((lchar *)(size_t)wb[t] + 16 * u * ROWB, 4 * ROWB);
    }
    if constexpr (i == FIRST_AT) first();
    if constexpr (i == 1) after0();
    if constexpr (i == 20) mid();
    if constexpr (i > 0) {
      constexpr int j = (i - 1) & 1;
      for (int t = 0; t < T; ++t)
        for (int r = 0; r < 2; ++r) acc[r][t] = (ATT && t == 1) ? mfma(xf[j][r], wf[j][t], acc[r][t]) : mfma(wf[j][t], xf[j][r], acc[r][t]);
      for (int d = 0; d < 4; ++d) ca += f32x2{bf2f(cf[j][d]), bfhi(cf[j][d])};
      asm volatile("" : "+v"(ca));
      for (int m = 0; m < 2 * T; ++m) { __builtin_amdgcn_sched_group_barrier(0x8, 1, 0); __builtin_amdgcn_sched_group_barrier(0x100, 2, 0); __builtin_amdgcn_sched_group_barrier(0x2, 2, 0); }
    }
    __builtin_amdgcn_sched_barrier(0);
  });
  { const f32x2 a = both_halves(ca.x + ca.y); if (lane < 32 && w < T) *(LAS float *)(L + L_CSP + (w * 32 + lane) * 4) = a.x + a.y; }
  STAMP(2);
}
constexpr int w2_row(int st) { return st < 7 ? 128 * st : 128 * (st - 7) + 64; }
constexpr int h_step(int i) { return i < 28 ? 8 * (i >> 2) + (i & 3) : 8 * ((i - 28) >> 2) + 4 + (i & 3); }
struct StageCtx { rsrc_t rw2, rwo; u32 vob, so2, soo, ld; };
template <int ST> DI void stage_dma(const StageCtx &c) {
  sfor<0, 4>([&](auto I) {
    constexpr u32 i = I, dst = ((ST + 4) % NSLOT) * L_SLOT + i * 1024;
    dmai<dst, (w2_row(ST) + 8 * i) * XROW>(c.rw2, c.vob, c.so2, c.ld);
  });
}
template <int ST> DI void stage_load(const StageCtx &c, u32x4 (&br)[36]) {
  sfor<0, 4>([&](auto I) {
    constexpr u32 i = I;
    if constexpr (ST < 14) br[4 * (ST - 9) + i] = __builtin_amdgcn_raw_buffer_load_b128(c.rw2, c.vob, c.so2 + (w2_row(ST) + 8 * i) * XROW, 0);
    else br[4 * (ST - 9) + i] = __builtin_amdgcn_raw_buffer_load_b128(c.rwo, c.vob, c.soo + (56 * (ST - 14) + (i == 3 ? 20 : 8 * i)) * XROW, 0);
  });
}
template <bool ATT>
DI void gemmA_loader(lchar *L, rsrc_t rx, rsrc_t rw, u32 sox, u32 sow, int lw, int lane, const StageCtx &sc, u32x4 (&br)[36] STAMP_ARG) {
  constexpr int NW = ATT ? 3 : 4, N = 8 + NW;
  const u32 lb = (u32)(size_t)L;
  const u32 vox0 = (lane >> 3) * XROW + 16 * ((lane & 7) ^ ((lane >> 4) & 3)), vox1 = vox0 ^ 64;
  const u32 ldx = rfl(lb + lw * 8192);
  const u32 vow = ATT ? (lane >> 2) * QKVROW + ((lane & 3) == 3 ? 40 : 16 * (lane & 3)) : (lane >> 4) * W1ROW + 16 * ((lane & 15) ^ ((lane >> 4) << 2));
  const u32 swb = rfl(sow + 16 * lw * (ATT ? QKVROW : W1ROW)), ldw = rfl(lb + A_WOFF + lw * (ATT ? 1024 : 4096));
  auto issue = [&](auto C) {
    constexpr u32 c = C, so = (c % 3) * A_SLOT;
    sfor<0, NW>([&](auto I) {
      constexpr u32 i = I;
      if constexpr (ATT) dmai<so + i * 4096, 64 * c * QKVROW + (i == 0 ? 896 : i == 1 ? 1792 : 0)>(rw, vow, swb, ldw);
      else dmai<so + i * 1024, (64 * c + 4 * i) * W1ROW>(rw, vow, swb, ldw);
    });
    sfor<0, 8>([&](auto I) { constexpr u32 i = I; dmai<so + i * 1024, 8 * i * XROW + 128 * c>(rx, (i & 1) ? vox1 : vox0, sox, ldx); });
  };
  issue(std::integral_constant<int, 0>{});
  issue(std::integral_constant<int, 1>{});
  sfor<0, 7>([&](auto C) {
    constexpr int c = C;
    vmwait<(c == 6 ? 12 : N)>();
    asm volatile("s_barrier" ::: "memory");
    if constexpr (c == 0) STAMP(1);
    if constexpr (c + 2 < 7) issue(std::integral_constant<int, c + 2>{});
    else { stage_load<9 + 3 * (c - 5)>(sc, br); stage_load<10 + 3 * (c - 5)>(sc, br); stage_load<11 + 3 * (c - 5)>(sc, br); }
  });
  STAMP(2);
}

DI void feature_map(const f32x16 &P, float rstd, int h, u32x4 (&pf)[2][2]) {
  const float c = rstd * LOG2E;
  float mx = fmaxf(fmaxf(P[0], P[1]), P[2]), mn = fminf(fminf(P[0], P[1]), P[2]);
  for (int i = 3; i < 15; i += 2) { mx = fmaxf(fmaxf(mx, P[i]), P[i + 1]); mn = fminf(fminf(mn, P[i]), P[i + 1]); }
  mx = fmaxf(mx, P[15]); mn = fminf(mn, P[15]);
  { f32x2 a = both_halves(mx), b = both_halves(mn); mx = fmaxf(a.x, a.y); mn = fminf(b.x, b.y); }
  const float mxc = -mx * c, mnc = mn * c;
  f32x16 ep, en;
  for (int i = 0; i < 16; i += 2) {
    const f32x2 p2 = {P[i], P[i + 1]}, ap = p2 * c + mxc, an = mnc - p2 * c;
    ep[i] = __builtin_amdgcn_exp2f(ap.x); ep[i + 1] = __builtin_amdgcn_exp2f(ap.y);
    en[i] = __builtin_amdgcn_exp2f(an.x); en[i + 1] = __builtin_amdgcn_exp2f(an.y);
  }
  f32x2 sp2 = {ep[0], ep[1]}, sn2 = {en[0], en[1]};
  for (int i = 2; i < 12; i += 2) { sp2 += f32x2{ep[i], ep[i + 1]}; sn2 += f32x2{en[i], en[i + 1]}; }
  const float keep = h ? 0.f : 1.f;
  sp2 += (f32x2{ep[12], ep[13]} + f32x2{ep[14], ep[15]}) * keep;
  sn2 += (f32x2{en[12], en[13]} + f32x2{en[14], en[15]}) * keep;
  const f32x2 a = both_halves(sp2.x + sp2.y), b = both_halves(sn2.x + sn2.y);
  const float rp = __builtin_amdgcn_rcpf(a.x + a.y), rn = __builtin_amdgcn_rcpf(b.x + b.y);
  for (int i = 0; i < 16; i += 2) {
    const f32x2 u = f32x2{ep[i], ep[i + 1]} * rp, v = f32x2{en[i], en[i + 1]} * rn;
    ep[i] = u.x; ep[i + 1] = u.y; en[i] = v.x; en[i + 1] = v.y;
  }
  pf[0][0] = pack8(ep, 0); pf[0][1] = pack8(ep, 1); pf[1][0] = pack8(en, 0); pf[1][1] = pack8(en, 1);
}

constexpr int refill_end(int k) { return k < 0 ? 0 : k == 0 ? 7 : k == 1 ? 14 : k == 2 ? 20 : k == 3 ? 26 : k == 4 ? 32 : 36; }
constexpr bool refill_ok() { for (int k = 0; k < 6; ++k) for (int p = refill_end(k - 1); p < refill_end(k); ++p) { const int g = 9 + p / 4, s = 4 + 2 * k; if (s < g - 8 || 4 * (s + 2) > 4 * g - 2) return false; } return true; }
static_assert(refill_ok(), "refill schedule");
}

__global__ void __launch_bounds__(512, 2) fused_hedgehog_ffn(const char *__restrict__ gx, const char *__restrict__ gwqkv, const char *__restrict__ gwphi, const char *__restrict__ gwo,
                                                              const char *__restrict__ gw1, const char *__restrict__ gb1, const char *__restrict__ gw2, const char *__restrict__ gb2,
                                                              char *__restrict__ gout, char *__restrict__ gws) {
  __shared__ __attribute__((aligned(1024))) char smem[LDS_BYTES];
  lchar *L = (lchar *)smem;
  unsigned long long pc0;
  asm volatile("s_getpc_b64 %0" : "=s"(pc0));
  asm volatile("" ::"s"(gx), "s"(gwqkv), "s"(gwphi), "s"(gwo), "s"(gw1), "s"(gb1), "s"(gw2), "s"(gb2), "s"(gout), "s"(gws));
  const int tid = threadIdx.x, lane = tid & 63, w = rfl(tid >> 6);
  const int b = blockIdx.x, xcd = b & 7, slot = b >> 3;
  const int h = lane >> 5;
  STAMP_DECL; STAMP(0);
  const rsrc_t rx = __builtin_amdgcn_make_buffer_rsrc((void *)gx, 0, 2048 * XROW, 0x00020000);
  const rsrc_t rws = __builtin_amdgcn_make_buffer_rsrc((void *)gws, 0, WS_END, 0x00020000);
  const int tc = slot >= 16 ? slot - 16 : slot + 16;
  const bool has_tile = tc < 28;
  const u32 rt = tc / 7, cc = tc % 7;
  const bool att = slot < 16, ffn = !att && slot < 30;
  const int head = 8 * (xcd & 1) + (slot & 7);
  const int jf = slot - 16, ct = 7 * (xcd & 1) + (jf >= 7 ? jf - 7 : jf);
  const int rga = 2 * (xcd >> 1) + (att ? (slot >> 3) : (jf >= 7));

  if (w >= 4) {
    const int lw = w - 4;
    StageCtx sc;
    sc.rw2 = __builtin_amdgcn_make_buffer_rsrc((void *)gw2, 0, DF * XROW, 0x00020000);
    sc.rwo = __builtin_amdgcn_make_buffer_rsrc((void *)gwo, 0, DM * XROW, 0x00020000);
    sc.vob = (lane >> 3) * XROW + 16 * ((lane & 7) ^ (((lane >> 4) & 1) << 2));
    sc.so2 = rfl((896 * (lw >> 1) + 32 * (lw & 1)) * XROW + 128 * cc);
    sc.soo = rfl(28 * (8 * (lw >> 1) + (lw & 1)) * XROW + 128 * cc);
    sc.ld = rfl((u32)(size_t)L + (lw >> 1) * 8192 + (lw & 1) * 4096);
    u32x4 br[36];
    auto resid_dma = [&]() {
      const u32 vo = (lane >> 3) * XROW + 16 * ((lane & 7) ^ ((lane >> 4) & 3)), so = rfl((256 * xcd + 64 * rt + 16 * lw) * XROW + 128 * cc), ld = rfl((u32)(size_t)L + L_XT + lw * 2048);
      dmai<0, 0>(rx, vo, so, ld);
      dmai<1024, 8 * XROW>(rx, vo ^ 64, so, ld);
    };
    if (att) {
      const rsrc_t rw = __builtin_amdgcn_make_buffer_rsrc((void *)gwqkv, 0, DM * QKVROW, 0x00020000);
      if (lw < 2) {
        const rsrc_t rp = __builtin_amdgcn_make_buffer_rsrc((void *)gwphi, 0, 16 * 28 * 28 * 2, 0x00020000);
        dma16(rp, lane * 16 + lw * 1024, head * 1568, (u32)(size_t)L + L_WPHI + lw * 1024);
      }
      gemmA_loader<true>(L, rx, rw, rfl((256 * rga + 64 * lw) * XROW), rfl(56 * head), lw, lane, sc, br STAMP_PASS);
      BAR();
      const u32 E = rfl(*(LAS u32 *)(L + L_E));
      if (has_tile) { sfor<0, 5>([&](auto S) { stage_dma<S>(sc); }); sfor<5, 7>([&](auto S) { stage_dma<S>(sc); }); resid_dma(); }
      sfor<15, 18>([&](auto S) { stage_load<S>(sc, br); });
      if (has_tile) vmwait<22>();
      BAR();
      __builtin_amdgcn_s_setprio(XPRIO);
      STAMP(4);
      for (int t = 0; t < 2; ++t) {
        lchar *sp = (lchar *)(size_t)(sc.ld + (2 + t) * L_SLOT + lane * 16);
        f32x4 s = *(LAS f32x4 *)sp;
        for (int ww = 1; ww < 4; ++ww) s += *(LAS f32x4 *)(sp + ww * 1024);
        st128_sc1(u32x4{pk2(s.x, s.y), E, pk2(s.z, s.w), E}, rws, lane * 16, WS_KVP + (head * 8 + rga) * 8192 + (t * 4 + lw) * 1024);
      }
      STAMP(5);
      poll_all(gws, WS_KVP + (head * 8 + (lane & 7)) * 8192 + (4 + lw) * 1024 + 63 * 16 + 12, E);
      STAMP(7);
      f32x2 ks[2][2];
#pragma unroll 1
      for (int spin = 0; spin < SPIN_CAP; ++spin) {
        u32x4 part[2][8];
        for (int t = 0; t < 2; ++t) for (int r = 0; r < 8; ++r) part[t][r] = ld128_sc1(rws, lane * 16, WS_KVP + (head * 8 + r) * 8192 + (t * 4 + lw) * 1024);
        u32 ta = E, to = E;
        for (int t = 0; t < 2; ++t) {
          ks[t][0] = f32x2{0.f, 0.f}; ks[t][1] = f32x2{0.f, 0.f};
          for (int r = 0; r < 8; ++r) {
            ks[t][0] += f32x2{bf2f(part[t][r][0]), bfhi(part[t][r][0])}; ks[t][1] += f32x2{bf2f(part[t][r][2]), bfhi(part[t][r][2])};
            ta &= part[t][r][1] & part[t][r][3]; to |= part[t][r][1] | part[t][r][3];
          }
        }
        if (__all(ta == E && to == E)) break;
        __builtin_amdgcn_s_sleep(POLL_SLEEP);
      }
      for (int t = 0; t < 2; ++t) {
        u32x2 pk = {pk2(ks[t][0].x, ks[t][0].y), pk2(ks[t][1].x, ks[t][1].y)};
        if (lw == 3 && h) pk = u32x2{0, 0};
        *(LAS u32x2 *)(L + L_KVF + ((t * 2 + (lw >> 1)) * 64 + lane) * 16 + (lw & 1) * 8) = pk;
      }
      __builtin_amdgcn_s_setprio(0);
      STAMP(8);
      if (!has_tile) {
        BAR();
        if (xcd == 0 && slot == 12 && lw == 0) {
#pragma unroll 1
          for (int spin = 0; spin < SPIN_CAP; ++spin) {
            bool ok = true;
            for (int i = 0; i < 32; ++i) ok &= ld_flag(gws, WS_STAT + (64 * i + lane) * 16 + 4) == E;
            if (__all(ok)) break;
            __builtin_amdgcn_s_sleep(8);
          }
          if (lane == 0) st_flag(gws, WS_EPOCH, E);
        }
        STAMP_OUT();
        return;
      }
    } else if (ffn) {
      const rsrc_t rw = __builtin_amdgcn_make_buffer_rsrc((void *)gw1, 0, DM * W1ROW, 0x00020000);
      gemmA_loader<false>(L, rx, rw, rfl((256 * rga + 64 * lw) * XROW), rfl(256 * ct), lw, lane, sc, br STAMP_PASS);
      BAR();
      if (HOLD == 1 || HOLD == 3) asm volatile("s_barrier" ::: "memory");
      sfor<0, 5>([&](auto S) { stage_dma<S>(sc); });
      vmwait<0>();
      BAR();
      sfor<5, 9>([&](auto S) { stage_dma<S>(sc); });
      resid_dma();
      sfor<15, 18>([&](auto S) { stage_load<S>(sc, br); });
    } else {
      warm_up(pc0, rx, rws, gwqkv, gw1, gw2, gwo, xcd, (slot - 30) * 8 + w, lane, (u32)(size_t)L);
      sfor<0, 9>([&](auto S) { stage_dma<S>(sc); });
      resid_dma();
      sfor<9, 18>([&](auto S) { stage_load<S>(sc, br); });
      vmwait<36>();
      BAR();
    }
    STAMP(10);
    const u32 ldv = sc.ld + lane * 16;
    sfor<0, 7>([&](auto K) {
      constexpr int k = K;
      if constexpr (k == 0) vmwait<12>();
      if constexpr (k == 1) vmwait<0>();
      BAR();
      if constexpr (k == 0) { if (att) sfor<7, 9>([&](auto S) { stage_dma<S>(sc); }); }
      sfor<refill_end(k - 1), refill_end(k)>([&](auto Pc) {
        constexpr int pc = Pc;
        *(LAS u32x4 *)((lchar *)(size_t)ldv + ((9 + pc / 4 + 4) % NSLOT) * L_SLOT + (pc & 3) * 1024) = br[pc];
        if (SPREAD) asm volatile("s_nop %0" ::"i"(SPREAD ? SPREAD - 1 : 0) : "memory");
      });
    });
    STAMP(12);
    asm volatile("s_barrier" ::: "memory");
    STAMP_OUT();
    return;
  }

  const Early early = early_issue(gws, rx, 8 * b + 2 * w, lane);
  u32 E;
  auto touches = [&]() {
    if (att || ffn) {
      constexpr u32 OOB = 0x7ffffff0u;
      const u32 lb = (u32)(size_t)L;
      { const u32 g15 = att ? (slot & 7) : 8 + (jf >= 7 ? jf - 7 : jf), i = (g15 * 4 + w) * 22 + lane;
        touch(rx, (lane < 22 && i < 1280) ? (256 * rga + (i & 255)) * XROW + 128 * (2 + (i >> 8)) : OOB, lb); }
      if (att) {
        const rsrc_t rq = __builtin_amdgcn_make_buffer_rsrc((void *)gwqkv, 0, DM * QKVROW, 0x00020000);
        const u32 i = slot * 240 + w * 60 + lane, row = (i * 43691u) >> 19, rem = i - 12 * row;
        touch(rq, lane < 60 ? (128 + row) * QKVROW + 896 * (rem >> 2) + ((448 * (xcd & 1)) & ~127u) + 128 * (rem & 3) : OOB, lb);
      } else {
        const rsrc_t r1 = __builtin_amdgcn_make_buffer_rsrc((void *)gw1, 0, DM * W1ROW, 0x00020000);
        const u32 l8 = 4 * (jf >= 7) + w, i = 64 * l8 + lane, j = 512 + 16 * l8 + lane;
        touch(r1, (128 + (i >> 1)) * W1ROW + 256 * ct + 128 * (i & 1), lb);
        touch(r1, lane < 16 ? (128 + (j >> 1)) * W1ROW + 256 * ct + 128 * (j & 1) : OOB, lb);
      }
    }
  };
  if (TOUCH_A == 2) touches();
  auto first = [&]() {
    E = early_finish(early, rws, 8 * b + 2 * w, lane); if (tid == 0) *(LAS u32 *)(L + L_E) = E;
    if (TOUCH_A == 1) touches();
  };
  const int rh = w & 1, kg = w >> 1;
  const u32 rb = 8 * xcd + 2 * rt + rh;
  const u32 row = 32 * rb + (lane & 31), col0 = 64 * cc + 32 * kg;
  u32x4 pq[2][2][2];
  const u32 hfa = WS_HFA + (rb * 14 + 7 * kg + ((lane & 7) < 7 ? (lane & 7) : 0)) * 64;
  u32 hlook = 0;
  auto o_stage = [&]() {
    u32x4 kf[2][2];
    for (int t = 0; t < 2; ++t) for (int s = 0; s < 2; ++s) kf[t][s] = *(LAS u32x4 *)(L + L_KVF + ((t * 2 + s) * 64 + lane) * 16);
    f32x16 num[2] = {zero16(), zero16()};
    for (int t = 0; t < 2; ++t) for (int s = 0; s < 2; ++s) for (int r = 0; r < 2; ++r) num[r] = mfma(kf[t][s], pq[r][t][s], num[r]);
    for (int r = 0; r < 2; ++r) {
      const float rc = __builtin_amdgcn_rcpf(both_halves(num[r][12]).x + 1e-6f);
      for (int i = 0; i < 16; i += 2) { const f32x2 v = f32x2{num[r][i], num[r][i + 1]} * rc; num[r][i] = v.x; num[r][i + 1] = v.y; }
      if (!h) { num[r][12] = 0.f; num[r][13] = 0.f; num[r][14] = 0.f; num[r][15] = 0.f; }
      const u32 so = WS_O + ((8 * rga + 2 * w + r) * 32 + 2 * head) * 1024;
      st128_sc1(pack8(num[r], 0), rws, lane * 16, so);
      st128_sc1(pack8(num[r], 1), rws, lane * 16, so + 1024);
    }
  };
  if (att) {
    if (tid < 16) *(LAS u32 *)(L + L_ZERO + 4 * tid) = 0;
    u32x4 idf[2];
    for (int s = 0; s < 2; ++s)
      for (int d = 0; d < 4; ++d) {
        const int k0 = 16 * s + 8 * (d >> 1) + 4 * h + 2 * (d & 1), n = lane & 31;
        idf[s][d] = (n == k0 ? 0x3F80u : 0u) | (n == k0 + 1 ? 0x3F800000u : 0u);
      }
    for (int s = 0; s < 2; ++s) asm volatile("" : "+v"(idf[s]));
    f32x16 acc[2][3]; u32x4 stat[2];
    const u32 vostat = WS_STAT + (256 * rga + 64 * w + (lane & 31)) * 16;
    gemmA_compute<true>(L, w, lane, acc, first, [&]() { if (TOUCH_A == 3) touches(); }, [&]() { for (int r = 0; r < 2; ++r) stat[r] = ld128_sc1(rws, vostat + 512 * r, 0); } STAMP_PASS);
    RowStat st[2];
    for (int r = 0; r < 2; ++r) {
      st[r] = row_stats(stat_settle(stat[r], rws, vostat + 512 * r, E), h);
      *(LAS float *)(L + L_TOK + w * 256 + r * 128 + 4 * (lane & 31)) = st[r].rstd;
    }
    BAR();
    STAMP(3);
    {
      const u32x4 wk = waug(cs_read(L, 0, lane), 0), wv = waug(cs_read(L, 1, lane), 0), wq = waug(cs_read(L, 2, lane), 0);
      for (int r = 0; r < 2; ++r) { acc[r][0] = mfma(wk, st[r].xaug, acc[r][0]); acc[r][1] = mfma(st[r].xaug, wv, acc[r][1]); acc[r][2] = mfma(wq, st[r].xaug, acc[r][2]); }
    }
    u32x4 wp[2];
    {
      const int blk = (lane >> 4) & 1, q = (lane & 15) >> 2, p = lane & 3;
      const u32 col = (blk && p == 3) ? 24 : 16 * blk + 4 * p;
      for (int s = 0; s < 2; ++s) {
        u32x2 r[2];
        for (int t = 0; t < 2; ++t) {
          const int dp = 16 * s + 8 * t + 4 * h + q;
          const u32 a = (dp >= 24 && dp < 28) ? L_ZERO + 8 * p : L_WPHI + (dp >= 28 ? dp - 4 : dp) * 56 + col * 2;
          r[t] = trd(L + a);
        }
        wp[s] = u32x4{r[0].x, r[0].y, r[1].x, r[1].y};
      }
    }
    f32x16 kvp[2] = {zero16(), zero16()};
    {
      f32x16 P[2];
      for (int r = 0; r < 2; ++r) P[r] = mfma(wp[0], pack8(acc[r][0], 0), zero16());
      for (int r = 0; r < 2; ++r) P[r] = mfma(wp[1], pack8(acc[r][0], 1), P[r]);
      u32x4 vf[2][2];
      for (int r = 0; r < 2; ++r) {
        f32x16 v = acc[r][1];
        for (int g = 0; g < 4; ++g) {
          const f32x4 rs = *(LAS f32x4 *)(L + L_TOK + w * 256 + r * 128 + (8 * g + 4 * h) * 4);
          for (int e = 0; e < 4; e += 2) { const f32x2 t2 = f32x2{v[4 * g + e], v[4 * g + e + 1]} * f32x2{rs[e], rs[e + 1]}; v[4 * g + e] = t2.x; v[4 * g + e + 1] = t2.y; }
        }
        for (int s = 0; s < 2; ++s) { vf[r][s] = pack8(v, s); if ((lane & 31) == 24) vf[r][s] = u32x4{0x3F803F80u, 0x3F803F80u, 0x3F803F80u, 0x3F803F80u}; }
      }
      u32x4 pf[2][2][2];
      for (int r = 0; r < 2; ++r) feature_map(P[r], st[r].rstd, h, pf[r]);
      f32x16 Z[2][2];
      for (int r = 0; r < 2; ++r) for (int t = 0; t < 2; ++t) Z[r][t] = mfma(pf[r][t][0], idf[0], zero16());
      for (int r = 0; r < 2; ++r) for (int t = 0; t < 2; ++t) Z[r][t] = mfma(pf[r][t][1], idf[1], Z[r][t]);
      for (int r = 0; r < 2; ++r) for (int s = 0; s < 2; ++s) for (int t = 0; t < 2; ++t) kvp[t] = mfma(pack8(Z[r][t], s), vf[r][s], kvp[t]);
    }
    for (int t = 0; t < 2; ++t)
      for (int g = 0; g < 4; ++g)
        *(LAS f32x4 *)(L + (2 + t) * L_SLOT + (g >> 1) * 8192 + (g & 1) * 4096 + w * 1024 + lane * 16) = f32x4{kvp[t][4 * g], kvp[t][4 * g + 1], kvp[t][4 * g + 2], kvp[t][4 * g + 3]};
    STAMP(4);
    BAR();
    {
      f32x16 P[2];
      for (int r = 0; r < 2; ++r) P[r] = mfma(wp[0], pack8(acc[r][2], 0), zero16());
      for (int r = 0; r < 2; ++r) P[r] = mfma(wp[1], pack8(acc[r][2], 1), P[r]);
      for (int r = 0; r < 2; ++r) {
        feature_map(P[r], st[r].rstd, h, pq[r]);
        for (int t = 0; t < 2; ++t) for (int s = 0; s < 2; ++s) asm volatile("" : "+v"(pq[r][t][s]));
        if (r == 0 && has_tile) hlook = ld_flag(gws, hfa);
        __builtin_amdgcn_sched_barrier(0);
      }
    }
    STAMP(6);
    if (!has_tile) {
      BAR();
      o_stage();
      vmwait<0>();
      if (lane < 2) st_flag(gws, WS_OF + ((8 * rga + 2 * w + lane) * 16 + head) * 64, E);
      STAMP_OUT();
      return;
    }
  } else if (ffn) {
    u32 bias[4];
    for (int t = 0; t < 4; ++t) bias[t] = *(const GAS unsigned short *)(gb1 + (128 * ct + 32 * t + (lane & 31)) * 2);
    f32x16 acc[2][4]; u32x4 stat[2];
    const u32 vostat = WS_STAT + (256 * rga + 64 * w + (lane & 31)) * 16;
    gemmA_compute<false>(L, w, lane, acc, first, [&]() { if (TOUCH_A == 3) touches(); }, [&]() { for (int r = 0; r < 2; ++r) stat[r] = ld128_sc1(rws, vostat + 512 * r, 0); } STAMP_PASS);
    RowStat st[2];
    for (int r = 0; r < 2; ++r) st[r] = row_stats(stat_settle(stat[r], rws, vostat + 512 * r, E), h);
    BAR();
    STAMP(3);
    if (HOLD == 2) __builtin_amdgcn_s_setprio(3);
    for (int t = 0; t < 4; ++t) {
      const u32x4 wa = waug(cs_read(L, t, lane), bias[t] | (bias[t] << 16));
      for (int r = 0; r < 2; ++r) {
        f32x16 a = mfma(wa, st[r].xaug, acc[r][t]);
        for (int i = 0; i < 16; i += 2) { f32x2 v = {relu(a[i]), relu(a[i + 1])}; v *= v; a[i] = v.x; a[i + 1] = v.y; }
        const u32 so = WS_H + ((8 * rga + 2 * w + r) * 112 + 8 * ct + 2 * t) * 1024;
        st128_sc1(pack8(a, 0), rws, lane * 16, so);
        st128_sc1(pack8(a, 1), rws, lane * 16, so + 1024);
      }
      if (HOLD == 3 && t == 1) asm volatile("s_barrier" ::: "memory");
      if (t == 2) {
        vmwait<4>();
        if (lane < 2) st_flag(gws, WS_HFA + ((8 * rga + 2 * w + lane) * 14 + ct) * 64, E);
      }
    }
    if (HOLD == 1) asm volatile("s_barrier" ::: "memory");
    if (HOLD == 2) __builtin_amdgcn_s_setprio(0);
    STAMP(4);
    vmwait<0>();
    if (lane < 2) st_flag(gws, WS_HFB + ((8 * rga + 2 * w + lane) * 14 + ct) * 64, E);
    STAMP(5);
    for (int t = 0; t < 2; ++t) for (int r = 0; r < 2; ++r) for (int s = 0; s < 2; ++s) pq[t][r][s] = u32x4{0, 0, 0, 0};
  } else {
    warm_up(pc0, rx, rws, gwqkv, gw1, gw2, gwo, xcd, (slot - 30) * 8 + w, lane, (u32)(size_t)L);
    first();
    for (int t = 0; t < 2; ++t) for (int r = 0; r < 2; ++r) for (int s = 0; s < 2; ++s) pq[t][r][s] = u32x4{0, 0, 0, 0};
  }

  {
    constexpr int RD = RING;
    const u32 soh = WS_H + (rb * 112 + 56 * kg) * 1024, soo = WS_O + (rb * 32 + 16 * kg) * 1024;
    u32x4 ring[RD];
    u32x2 b2v[4]; u32x4 sg0;
    const int blk = (lane >> 4) & 1, q = (lane & 15) >> 2, p = lane & 3;
    u32 bb0[2], bb[2];
    for (int c2 = 0; c2 < 2; ++c2) bb0[c2] = (u32)(size_t)L + kg * 8192 + (4 * h + q) * 128 + ((c2 ^ kg ^ (q >> 1)) * 64) + 32 * blk + 8 * p;
    f32x16 af[2] = {zero16(), zero16()}, aa[2] = {zero16(), zero16()};
    constexpr int PD = BPD;
    u32x4 bf[PD + 1][2];
    const u32 hfb = WS_HFB + (rb * 14 + 7 * kg + ((lane & 7) < 7 ? (lane & 7) : 0)) * 64, ofo = WS_OF + (rb * 16 + 8 * kg + (lane & 7)) * 64;
    u32 look = 0, soe = 0;
    bool o_early = false;
    float rs2; f32x2 bx[8], sg[8], xx[8];
    u32x4 oa[16];
    if (!__all(hlook == E)) poll_all(gws, hfa, E);
    STAMP(10);
    if (!att) BAR();
    sfor<0, 72>([&](auto I) {
      constexpr int i = I;
      if constexpr (i == 56) {
        for (int g = 0; g < 4; ++g)
          *(LAS f32x4 *)(L + 6 * L_SLOT + w * 4096 + g * 1024 + lane * 16) = f32x4{af[1][4 * g], af[1][4 * g + 1], af[1][4 * g + 2], af[1][4 * g + 3]};
        BAR();
        for (int g = 0; g < 4; ++g) {
          const f32x4 pf = *(LAS f32x4 *)(L + 6 * L_SLOT + (w ^ 2) * 4096 + g * 1024 + lane * 16);
          for (int e = 0; e < 2; ++e) {
            const int j = 4 * g + 2 * e;
            const f32x2 y = (f32x2{af[0][j], af[0][j + 1]} + f32x2{pf[2 * e], pf[2 * e + 1]}) * rs2 + bx[2 * g + e];
            const f32x2 t = y * -LOG2E;
            const f32x2 d = f32x2{__builtin_amdgcn_exp2f(t.x), __builtin_amdgcn_exp2f(t.y)} + 1.0f;
            sg[2 * g + e] = y * f32x2{__builtin_amdgcn_rcpf(d.x), __builtin_amdgcn_rcpf(d.y)};
            asm volatile("" : "+v"(sg[2 * g + e]));
          }
        }
        STAMP(14);
        if (!o_early) {
          if (!__all(look == E)) poll_all(gws, ofo, E);
          for (int j = 0; j < OB; ++j) oa[j] = ld128_sc1(rws, lane * 16, soo + j * 1024);
        }
        __builtin_amdgcn_sched_barrier(0);
      }
      if constexpr (i >= 16 && (i & 7) == 0 && i != 56) asm volatile("s_barrier" ::: "memory");
      if constexpr (i == 0) STAMP(11);
      if constexpr (i == 0) {
        for (int j = 0; j < RD; ++j) ring[j] = ld128_sc1(rws, lane * 16, soh + h_step(j) * 1024);
        __builtin_amdgcn_sched_barrier(0);
        for (int c2 = 0; c2 < 2; ++c2) { bb[c2] = bb0[c2] + 4 * L_SLOT; asm volatile("" : "+v"(bb[c2])); for (int j = 0; j < PD; ++j) bf[j][c2] = trfrag((lchar *)(size_t)bb[c2] + j * 2048, 1024); }
      }
      static_assert(28 - RD - 6 < 16, "no look between the o stores and their counted wait");
      if constexpr (i == 28 - RD - 6) look = ld_flag(gws, hfb);
      if constexpr (i == 28 - RD) { if (!__all(look == E)) poll_all(gws, hfb, E); }
      if constexpr (i == 44) {
        for (int g = 0; g < 4; ++g) b2v[g] = *(const GAS u32x2 *)(gb2 + (col0 + 8 * g + 4 * h) * 2);
        sg0 = ld128_sc1(rws, WS_STAT + row * 16, 0);
      }
      if constexpr (i == 40) look = ld_flag(gws, ofo);
      if constexpr (i == 47) { o_early = __all(look == E); soe = o_early ? soo : WS_END; }
      if constexpr (i >= 48 && i < 48 + OB) oa[i - 48] = ld128_sc1(rws, lane * 16, soe + (i - 48) * 1024);
      if constexpr (i == 50) look = ld_flag(gws, ofo);
      if constexpr (i == 52) {
        u32x4 g4 = sg0;
        if (!__all(g4[1] == E && g4[3] == E)) g4 = stat_settle(g4, rws, WS_STAT + row * 16, E);
        const float rs1 = __builtin_amdgcn_rcpf(asf(g4[2]));
        rs2 = rs1 * rs1;
        for (int g = 0; g < 4; ++g) for (int e = 0; e < 2; ++e) { bx[2 * g + e] = f32x2{bf2f(b2v[g][e]), bfhi(b2v[g][e])}; asm volatile("" : "+v"(bx[2 * g + e])); }
      }
      if constexpr (i == 60) {
        for (int g = 0; g < 4; ++g) {
          const u32 r64 = 32 * rh + (lane & 31);
          const u32x2 xr = *(LAS u32x2 *)(L + L_XT + r64 * 128 + 16 * ((4 * kg + g) ^ ((r64 >> 1) & 7)) + 8 * h);
          for (int e = 0; e < 2; ++e) { xx[2 * g + e] = f32x2{bf2f(xr[e]), bfhi(xr[e])}; asm volatile("" : "+v"(xx[2 * g + e])); }
        }
      }
      if constexpr (i == 16) { if (att) o_stage(); }
      if constexpr (i == 16 + OFD) {
        if (att) { vmwait<OFD>(); if (lane < 2) st_flag(gws, WS_OF + ((8 * rga + 2 * w + lane) * 16 + head) * 64, E); }
      }
      u32x4 a;
      if constexpr (i < 56) a = ring[i % RD]; else a = oa[i - 56];
      if constexpr (i + PD < 72) {
        constexpr int n = i + PD;
        if constexpr ((n & 3) == 0) for (int c2 = 0; c2 < 2; ++c2) { bb[c2] = bb0[c2] + (((n >> 2) + 4) % NSLOT) * L_SLOT; asm volatile("" : "+v"(bb[c2])); }
        for (int c2 = 0; c2 < 2; ++c2) bf[n % (PD + 1)][c2] = trfrag((lchar *)(size_t)bb[c2] + (n & 3) * 2048, 1024);
      }
      if constexpr (i < 56) { af[0] = mfma(bf[i % (PD + 1)][0], a, af[0]); af[1] = mfma(bf[i % (PD + 1)][1], a, af[1]); asm volatile("" : "+v"(af[0]), "+v"(af[1])); }
      else { aa[0] = mfma(bf[i % (PD + 1)][0], a, aa[0]); aa[1] = mfma(bf[i % (PD + 1)][1], a, aa[1]); asm volatile("" : "+v"(aa[0]), "+v"(aa[1])); }
      if constexpr (i + RD < 56) ring[i % RD] = ld128_sc1(rws, lane * 16, soh + h_step(i + RD) * 1024);
      if constexpr (i >= 56 && i - 56 + OB < 16) oa[i - 56 + OB] = ld128_sc1(rws, lane * 16, soo + (i - 56 + OB) * 1024);
      __builtin_amdgcn_sched_barrier(0);
    });
    STAMP(12);
    for (int g = 0; g < 4; ++g)
      *(LAS f32x4 *)(L + 8 * L_SLOT + w * 4096 + g * 1024 + lane * 16) = f32x4{aa[1][4 * g], aa[1][4 * g + 1], aa[1][4 * g + 2], aa[1][4 * g + 3]};
    BAR();
    u32x2 o2[4];
    for (int g = 0; g < 4; ++g) {
      const f32x4 pa = *(LAS f32x4 *)(L + 8 * L_SLOT + (w ^ 2) * 4096 + g * 1024 + lane * 16);
      for (int e = 0; e < 2; ++e) {
        const int i = 4 * g + 2 * e;
        const f32x2 r = (f32x2{aa[0][i], aa[0][i + 1]} + f32x2{pa[2 * e], pa[2 * e + 1]}) * sg[2 * g + e] + xx[2 * g + e];
        o2[g][e] = pk2(r.x, r.y);
      }
    }
    const rsrc_t rout = __builtin_amdgcn_make_buffer_rsrc((void *)gout, 0, 2048 * XROW, 0x00020000);
    for (int g = 0; g < 4; g += 2) {
      auto rx0 = __builtin_amdgcn_permlane32_swap(o2[g].x, o2[g + 1].x, false, false);
      auto ry0 = __builtin_amdgcn_permlane32_swap(o2[g].y, o2[g + 1].y, false, false);
      __builtin_amdgcn_raw_buffer_store_b128(u32x4{rx0[0], ry0[0], rx0[1], ry0[1]}, rout, row * XROW + (col0 + 8 * (g + h)) * 2, 0, OUT_AUX);
    }
    STAMP(13);
    STAMP(15);
    STAMP_OUT();
  }
}

extern "C" void kernel_launch(void *const *d_in, const int *in_sizes, int n_in, void *d_out, int out_size, void *d_ws, size_t ws_size, hipStream_t stream) {
  fused_hedgehog_ffn<<<256, 512, 0, stream>>>((const char *)d_in[0], (const char *)d_in[1], (const char *)d_in[2], (const char *)d_in[3], (const char *)d_in[5],
                                              (const char *)d_in[6], (const char *)d_in[7], (const char *)d_in[8], (char *)d_out, (char *)d_ws);
}
```

```cpp
#include <hip/hip_runtime.h>
#include <hip/hip_bf16.h>
#include <stdint.h>
#include <type_traits>

typedef unsigned u32;
typedef u32 u32x4 __attribute__((ext_vector_type(4)));
typedef u32 u32x2 __attribute__((ext_vector_type(2)));
typedef float f32x16 __attribute__((ext_vector_type(16)));
typedef float f32x4 __attribute__((ext_vector_type(4)));
typedef float f32x2 __attribute__((ext_vector_type(2)));
typedef short s16x8 __attribute__((ext_vector_type(8)));
typedef short s16x4 __attribute__((ext_vector_type(4)));
typedef __bf16 bf16x2 __attribute__((ext_vector_type(2)));
typedef __bf16 bf16x8 __attribute__((ext_vector_type(8)));
#define LAS __attribute__((address_space(3)))
#define GAS __attribute__((address_space(1)))
#define DI __device__ __forceinline__
typedef __amdgpu_buffer_rsrc_t rsrc_t;
typedef LAS char lchar;

namespace {
constexpr int DM = 448, DF = 1792;
constexpr u32 XROW = DM * 2;
constexpr u32 QKVROW = 3 * DM * 2;
constexpr u32 W1ROW = DF * 2;
constexpr u32 WS_EPOCH = 0;
constexpr u32 WS_STAT = 4096;
constexpr u32 WS_KF = 65536;
constexpr u32 WS_HFA = 98304;
constexpr u32 WS_HFB = WS_HFA + 57344;
constexpr u32 WS_OF = WS_HFB + 57344;
constexpr u32 WS_KVP = 294912;
constexpr u32 WS_H = WS_KVP + (1u << 20);
constexpr u32 WS_O = WS_H + 64 * 112 * 1024;
constexpr u32 WS_END = WS_O + 64 * 32 * 1024;
constexpr u32 L_SLOT = 16384, NSLOT = 9;
constexpr u32 A_SLOT = 49152, A_WOFF = 32768;
constexpr u32 L_MISC = NSLOT * L_SLOT;
constexpr u32 L_WPHI = L_MISC;
constexpr u32 L_ZERO = L_MISC + 2048;
constexpr u32 L_E = L_MISC + 2112;
constexpr u32 L_DUMMY = L_MISC + 2304;
constexpr u32 L_CSP = L_MISC + 2560;
constexpr u32 L_TOK = L_CSP + 512;
constexpr u32 L_KVF = L_TOK + 1024;
constexpr u32 L_XT = L_KVF + 4096;
constexpr u32 L_END = L_XT + 8192;
constexpr u32 LDS_BYTES = 163840;
static_assert(L_END <= LDS_BYTES, "lds");
constexpr int SPIN_CAP = 1 << 16;
constexpr float LOG2E = 1.4426950408889634f;
#define CS_DOT2 0
#define SGB 1
#define LPRIO 0
#define FIRST_AT -1
#define TOUCH_A 0
#define POLL_SLEEP 3
#define HOLD 1
#define XPRIO 3
#define WARM 7
#define BPD 3
#define OB 8
#define FFN_ALL 0
#define OFD 10
#define OUT_AUX 0
#define SPREAD 0
#define PIN_C 0
#define RING 10

#define STAMP(i) do {} while (0)
#define STAMP_DECL do {} while (0)
#define STAMP_OUT() do {} while (0)
#define STAMP_ARG
#define STAMP_PASS
template <int B, int E, class F> DI void sfor(F &&f) {
  if constexpr (B < E) { f(std::integral_constant<int, B>{}); sfor<B + 1, E>(f); }
}
DI u32 rfl(u32 v) { return __builtin_amdgcn_readfirstlane(v); }
DI f32x16 mfma(u32x4 a, u32x4 b, f32x16 c) {
  return __builtin_amdgcn_mfma_f32_32x32x16_bf16(__builtin_bit_cast(bf16x8, a), __builtin_bit_cast(bf16x8, b), c, 0, 0, 0);
}
DI u32 pk2(float lo, float hi) { f32x2 p = {lo, hi}; return __builtin_bit_cast(u32, __builtin_convertvector(p, bf16x2)); }
DI float dot2(u32 a, u32 b, float c) { return __builtin_amdgcn_fdot2_f32_bf16(__builtin_bit_cast(bf16x2, a), __builtin_bit_cast(bf16x2, b), c, false); }
DI float bf2f(u32 lo16) { return __builtin_bit_cast(float, lo16 << 16); }
DI float bfhi(u32 v) { return __builtin_bit_cast(float, v & 0xffff0000u); }
DI float asf(u32 v) { return __builtin_bit_cast(float, v); }
DI f32x2 both_halves(float v) {
  const u32 a = __builtin_bit_cast(u32, v);
  auto r = __builtin_amdgcn_permlane32_swap(a, a, false, false);
  return f32x2{asf(r[0]), asf(r[1])};
}
DI float half_lo(float v) { return both_halves(v).x; }
DI u32x4 pack8(const f32x16 &x, int s) { return u32x4{pk2(x[8 * s], x[8 * s + 1]), pk2(x[8 * s + 2], x[8 * s + 3]), pk2(x[8 * s + 4], x[8 * s + 5]), pk2(x[8 * s + 6], x[8 * s + 7])}; }
DI u32x2 trd(lchar *p) {
  typedef short v4i16 __attribute__((ext_vector_type(4)));
  return __builtin_bit_cast(u32x2, __builtin_amdgcn_ds_read_tr16_b64_v4i16((LAS v4i16 *)p));
}
DI u32x4 trfrag(lchar *p, int tstride) { u32x2 a = trd(p), b = trd(p + tstride); return u32x4{a.x, a.y, b.x, b.y}; }
DI float relu(float v) { return __builtin_amdgcn_fmed3f(v, 0.f, __builtin_inff()); }
DI f32x16 zero16() { f32x16 z; for (int i = 0; i < 16; ++i) z[i] = 0.f; return z; }
#define BAR() asm volatile("s_waitcnt lgkmcnt(0)\n\ts_barrier" ::: "memory")
template <int N> DI void vmwait() { asm volatile("s_waitcnt vmcnt(%0)" ::"i"(N) : "memory"); }
DI void dma16(rsrc_t r, u32 voff, u32 soff, u32 ldsaddr) {
  asm volatile("s_mov_b32 m0, %0\n\ts_nop 0\n\tbuffer_load_dwordx4 %1, %2, %3 offen lds" ::"s"(ldsaddr), "v"(voff), "s"(r), "s"(soff) : "memory", "m0");
}
template <int OFF> DI void ald128(u32x4 &d, rsrc_t r, u32 voff, u32 soff) {
  asm volatile("buffer_load_dwordx4 %0, %1, %2, %3 offen offset:%4" : "=v"(d) : "v"(voff), "s"(r), "s"(soff), "i"(OFF) : "memory");
}
DI u32x4 ld128_sc1(rsrc_t r, u32 voff, u32 soff) { return __builtin_amdgcn_raw_buffer_load_b128(r, voff, soff, 16); }
DI void st128_sc1(u32x4 v, rsrc_t r, u32 voff, u32 soff) {
  __builtin_amdgcn_raw_buffer_store_b128(v, r, voff, soff, 16);
  asm volatile("s_nop 1" ::"v"(v) : "memory");
}
typedef GAS u32 gu32;
DI u32 ld_flag(const char *ws, u32 off) { return __hip_atomic_load((gu32 *)(ws + off), __ATOMIC_RELAXED, __HIP_MEMORY_SCOPE_AGENT); }
DI void st_flag(char *ws, u32 off, u32 v) { __hip_atomic_store((gu32 *)(ws + off), v, __ATOMIC_RELAXED, __HIP_MEMORY_SCOPE_AGENT); }
DI void poll_all(const char *ws, u32 off, u32 E) {
#pragma unroll 1
  for (int spin = 0; spin < SPIN_CAP; ++spin) {
    if (__all(ld_flag(ws, off) == E)) break;
    __builtin_amdgcn_s_sleep(POLL_SLEEP);
  }
  __builtin_amdgcn_fence(__ATOMIC_ACQUIRE, "wavefront");
}
template <u32 LI, u32 SI> DI void dmai(rsrc_t r, u32 voff, u32 sob, u32 ldsb) {
  u32 t;
  asm volatile("s_add_u32 m0, %2, %5\n\ts_add_u32 %0, %1, %6\n\tbuffer_load_dwordx4 %3, %4, %0 offen lds"
               : "=&s"(t) : "s"(sob), "s"(ldsb), "v"(voff), "s"(r), "i"(LI), "i"(SI) : "memory", "m0", "scc");
}
DI void dmar(rsrc_t r, u32 voff, u32 soff, u32 lds) {
  asm volatile("s_mov_b32 m0, %0\n\ts_nop 0\n\tbuffer_load_dwordx4 %1, %2, %3 offen lds" ::"s"(lds), "v"(voff), "s"(r), "s"(soff) : "memory", "m0");
}
template <int CTRL, int RMASK> DI float dpp_add(float v) {
  return v + asf(__builtin_amdgcn_update_dpp(0u, __builtin_bit_cast(u32, v), CTRL, RMASK, 0xf, true));
}
DI float wave_sum63(float v) {
  v = dpp_add<0x111, 0xf>(v); v = dpp_add<0x112, 0xf>(v); v = dpp_add<0x114, 0xf>(v); v = dpp_add<0x118, 0xf>(v);
  v = dpp_add<0x142, 0xa>(v); v = dpp_add<0x143, 0xc>(v);
  return v;
}
DI void st64_sc1(char *ws, u32 off, u32 lo, u32 hi) {
  __hip_atomic_store((GAS unsigned long long *)(ws + off), (unsigned long long)lo | ((unsigned long long)hi << 32), __ATOMIC_RELAXED, __HIP_MEMORY_SCOPE_AGENT);
}
struct Early { u32 ev; u32x4 xa, xb; };
DI Early early_issue(char *ws, rsrc_t rx, int row0, int lane) {
  Early e;
  e.ev = ld_flag(ws, WS_EPOCH);
  const u32 vo = lane < 56 ? (u32)lane * 16 : 0x7ffffff0u;
  e.xa = __builtin_amdgcn_raw_buffer_load_b128(rx, vo, row0 * XROW, 0); e.xb = __builtin_amdgcn_raw_buffer_load_b128(rx, vo, (row0 + 1) * XROW, 0);
  return e;
}
DI u32 early_finish(const Early &e, rsrc_t rws, int row0, int lane) {
  const u32 E = rfl(e.ev) + 1, ONES = 0x3F803F80u;
  float s[2], q[2];
  for (int r = 0; r < 2; ++r) {
    const u32x4 &x = r ? e.xb : e.xa;
    s[r] = dot2(x[0], ONES, 0.f); q[r] = dot2(x[0], x[0], 0.f);
    for (int d = 1; d < 4; ++d) { s[r] = dot2(x[d], ONES, s[r]); q[r] = dot2(x[d], x[d], q[r]); }
    s[r] = wave_sum63(s[r]); q[r] = wave_sum63(q[r]);
    const float mean = s[r] * (1.0f / DM);
    const float sig = __builtin_amdgcn_sqrtf(fmaxf(q[r] * (1.0f / DM) - mean * mean, 0.f) + 1e-5f);
    const u32 vo = lane == 63 ? WS_STAT + (row0 + r) * 16 : 0x7ffffff0u;
    __builtin_amdgcn_raw_buffer_store_b64(u32x2{__builtin_bit_cast(u32, mean), E}, rws, vo, 0, 16);
    __builtin_amdgcn_raw_buffer_store_b64(u32x2{__builtin_bit_cast(u32, sig), E}, rws, vo + 8, 0, 16);
  }
  return E;
}
DI u32x4 stat_settle(u32x4 g, rsrc_t rws, u32 vostat, u32 E) {
#pragma unroll 1
  for (int spin = 0; spin < SPIN_CAP && !__all(g[1] == E && g[3] == E); ++spin) { __builtin_amdgcn_s_sleep(1); g = ld128_sc1(rws, vostat, 0); }
  return g;
}
struct RowStat { float rstd; u32x4 xaug; };
DI RowStat row_stats(u32x4 g, int h) {
  const float m = -asf(g[0]), sig = asf(g[2]);
  RowStat r; r.rstd = __builtin_amdgcn_rcpf(sig);
  const float ml = m - bfhi(pk2(m, m)), sl = sig - bfhi(pk2(sig, sig));
  const u32 mm = pk2(m, ml), ss = pk2(sig, sl);
  r.xaug = h ? u32x4{0, 0, 0, 0} : u32x4{mm, mm, ss, 0};
  return r;
}
DI u32x4 waug(float cs, u32 bb) {
  const u32 hh = pk2(cs, cs); const float cl = cs - bfhi(hh);
  return u32x4{hh, pk2(cl, cl), bb, 0};
}
DI float cs_read(lchar *L, int t, int lane) { return *(LAS float *)(L + L_CSP + (t * 32 + (lane & 31)) * 4); }

DI void touch(rsrc_t r, u32 voff, u32 lb) {
  asm volatile("s_mov_b32 m0, %0\n\ts_nop 0\n\tbuffer_load_dword %1, %2, 0 offen lds" ::"s"(lb + L_DUMMY), "v"(voff), "s"(r) : "memory", "m0");
}
constexpr u32 CODE_BYTES = 50 * 1024, CODE_PIECES = (CODE_BYTES + 8191) / 8192;
DI void warm_up(unsigned long long pc, rsrc_t rws, const char *gw2, const char *gwo, int xcd, int wv, int lane, u32 lb) {
  if (WARM & 1) {
    const rsrc_t rc = __builtin_amdgcn_make_buffer_rsrc((void *)(pc & ~127ull), 0, CODE_BYTES, 0x00020000);
    if (wv < (int)CODE_PIECES) touch(rc, wv * 8192 + lane * 128, lb);
  }
  const int g = xcd * 16 + wv;
  if (WARM & 2) { if (g < 27) touch(rws, WS_KF + g * 8192 + lane * 128, lb); }
  if (WARM & 4) {
    const rsrc_t r2 = __builtin_amdgcn_make_buffer_rsrc((void *)gw2, 0, DF * XROW, 0x00020000), ro = __builtin_amdgcn_make_buffer_rsrc((void *)gwo, 0, DM * XROW, 0x00020000);
    touch(r2, g * 8192 + lane * 128, lb);
    if (g < 68) touch(r2, (128 + g) * 8192 + lane * 128, lb);
    else if (g < 117) touch(ro, (g - 68) * 8192 + lane * 128, lb);
  }
}
template <bool ATT, class First, class After0, class Mid>
DI void gemmA_compute(lchar *L, int w, int lane, f32x16 (&acc)[2][ATT ? 3 : 4], First &&first, After0 &&after0, Mid &&mid STAMP_ARG) {
  constexpr int T = ATT ? 3 : 4, ROWB = ATT ? 64 : 256;
  const int h = lane >> 5, blk = (lane >> 4) & 1, q = (lane & 15) >> 2, p = lane & 3;
  const u32 lb = (u32)(size_t)L;
  u32 wb0[T], xb0[4], wb[T], xb[4], cb0, cb;
  for (int t = 0; t < T; ++t) wb0[t] = lb + A_WOFF + (ATT ? t * 4096 + (8 * h + q) * 64 : (8 * h + q) * 256 + 64 * (t ^ q)) + 32 * blk + 8 * p;
  for (int u = 0; u < 4; ++u) xb0[u] = lb + w * 8192 + (lane & 31) * 128 + 16 * ((2 * u + h) ^ ((lane >> 1) & 7));
  const int tw = w < T ? w : T - 1;
  cb0 = lb + A_WOFF + (ATT ? tw * 4096 + (8 * h + q) * 64 : (8 * h + q) * 256 + 64 * (tw ^ q)) + 32 * blk + 8 * p;
  f32x2 ca = {0.f, 0.f};
  for (int r = 0; r < 2; ++r) for (int t = 0; t < T; ++t) acc[r][t] = zero16();
  u32x4 xf[2][2], wf[2][T], cf[2];
  sfor<0, 29>([&](auto I) {
    constexpr int i = I, c = i >> 2, u = i & 3;
    if constexpr (i == 0 && FIRST_AT < 0) { first(); STAMP(14); }
    if constexpr (u == 0 && i < 28) {
      BAR();
      if constexpr (c == 0) STAMP(1);
      constexpr u32 so = (c % 3) * A_SLOT;
      for (int t = 0; t < T; ++t) { wb[t] = wb0[t] + so; asm volatile("" : "+v"(wb[t])); }
      for (int j = 0; j < 4; ++j) { xb[j] = xb0[j] + so; asm volatile("" : "+v"(xb[j])); }
      cb = cb0 + so; asm volatile("" : "+v"(cb));
    }
    if constexpr (i < 28) {
      cf[i & 1] = trfrag((lchar *)(size_t)cb + 16 * u * ROWB, 4 * ROWB);
      for (int r = 0; r < 2; ++r) xf[i & 1][r] = *(LAS u32x4 *)((lchar *)(size_t)xb[u] + r * 4096);
      for (int t = 0; t < T; ++t) wf[i & 1][t] = trfrag((lchar *)(size_t)wb[t] + 16 * u * ROWB, 4 * ROWB);
    }
    if constexpr (i == FIRST_AT) first();
    if constexpr (i == 1) after0();
    if constexpr (i == 20) mid();
    if constexpr (i > 0) {
      constexpr int j = (i - 1) & 1;
      for (int t = 0; t < T; ++t)
        for (int r = 0; r < 2; ++r) acc[r][t] = (ATT && t == 1) ? mfma(xf[j][r], wf[j][t], acc[r][t]) : mfma(wf[j][t], xf[j][r], acc[r][t]);
      for (int d = 0; d < 4; ++d) ca += f32x2{bf2f(cf[j][d]), bfhi(cf[j][d])};
      asm volatile("" : "+v"(ca));
      for (int m = 0; m < 2 * T; ++m) { __builtin_amdgcn_sched_group_barrier(0x8, 1, 0); __builtin_amdgcn_sched_group_barrier(0x100, 2, 0); __builtin_amdgcn_sched_group_barrier(0x2, 2, 0); }
    }
    __builtin_amdgcn_sched_barrier(0);
  });
  { const f32x2 a = both_halves(ca.x + ca.y); if (lane < 32 && w < T) *(LAS float *)(L + L_CSP + (w * 32 + lane) * 4) = a.x + a.y; }
  STAMP(2);
}
constexpr int w2_row(int st) { return st < 7 ? 128 * st : 128 * (st - 7) + 64; }
constexpr int h_step(int i) { return i < 28 ? 8 * (i >> 2) + (i & 3) : 8 * ((i - 28) >> 2) + 4 + (i & 3); }
struct StageCtx { rsrc_t rw2, rwo; u32 vob, so2, soo, ld; };
template <int ST> DI void stage_dma(const StageCtx &c) {
  sfor<0, 4>([&](auto I) {
    constexpr u32 i = I, dst = ((ST + 4) % NSLOT) * L_SLOT + i * 1024;
    dmai<dst, (w2_row(ST) + 8 * i) * XROW>(c.rw2, c.vob, c.so2, c.ld);
  });
}
template <int ST> DI void stage_load(const StageCtx &c, u32x4 (&br)[36]) {
  sfor<0, 4>([&](auto I) {
    constexpr u32 i = I;
    if constexpr (ST < 14) br[4 * (ST - 9) + i] = __builtin_amdgcn_raw_buffer_load_b128(c.rw2, c.vob, c.so2 + (w2_row(ST) + 8 * i) * XROW, 0);
    else br[4 * (ST - 9) + i] = __builtin_amdgcn_raw_buffer_load_b128(c.rwo, c.vob, c.soo + (56 * (ST - 14) + (i == 3 ? 20 : 8 * i)) * XROW, 0);
  });
}
template <bool ATT>
DI void gemmA_loader(lchar *L, rsrc_t rx, rsrc_t rw, u32 sox, u32 sow, int lw, int lane, const StageCtx &sc, u32x4 (&br)[36], rsrc_t rp, u32 wpo STAMP_ARG) {
  constexpr int NW = ATT ? 3 : 4, N = 8 + NW;
  const u32 lb = (u32)(size_t)L;
  const u32 vox0 = (lane >> 3) * XROW + 16 * ((lane & 7) ^ ((lane >> 4) & 3)), vox1 = vox0 ^ 64;
  const u32 ldx = rfl(lb + lw * 8192);
  const u32 vow = ATT ? (lane >> 2) * QKVROW + ((lane & 3) == 3 ? 40 : 16 * (lane & 3)) : (lane >> 4) * W1ROW + 16 * ((lane & 15) ^ ((lane >> 4) << 2));
  const u32 swb = rfl(sow + 16 * lw * (ATT ? QKVROW : W1ROW)), ldw = rfl(lb + A_WOFF + lw * (ATT ? 1024 : 4096));
  auto issue = [&](auto C) {
    constexpr u32 c = C, so = (c % 3) * A_SLOT;
    sfor<0, NW>([&](auto I) {
      constexpr u32 i = I;
      if constexpr (ATT) dmai<so + i * 4096, 64 * c * QKVROW + (i == 0 ? 896 : i == 1 ? 1792 : 0)>(rw, vow, swb, ldw);
      else dmai<so + i * 1024, (64 * c + 4 * i) * W1ROW>(rw, vow, swb, ldw);
    });
    sfor<0, 8>([&](auto I) { constexpr u32 i = I; dmai<so + i * 1024, 8 * i * XROW + 128 * c>(rx, (i & 1) ? vox1 : vox0, sox, ldx); });
  };
  sfor<0, 7>([&](auto C) {
    constexpr int c = C;
    if constexpr (ATT && c == 1) { if (lw < 2) vmwait<N + 1>(); else vmwait<N>(); }
    else vmwait<(c == 6 ? 12 : N)>();
    asm volatile("s_barrier" ::: "memory");
    if constexpr (c == 0) STAMP(1);
    if constexpr (ATT && c == 0) {
      if (lw < 2) dmar(rp, lane * 16 + lw * 1024, wpo, lb + L_WPHI + lw * 1024);
    }
    if constexpr (c + 2 < 7) issue(std::integral_constant<int, c + 2>{});
    else { stage_load<9 + 3 * (c - 5)>(sc, br); stage_load<10 + 3 * (c - 5)>(sc, br); stage_load<11 + 3 * (c - 5)>(sc, br); }
  });
  STAMP(2);
}

DI void feature_map(const f32x16 &P, float rstd, int h, u32x4 (&pf)[2][2]) {
  const float c = rstd * LOG2E;
  float mx = fmaxf(fmaxf(P[0], P[1]), P[2]), mn = fminf(fminf(P[0], P[1]), P[2]);
  for (int i = 3; i < 15; i += 2) { mx = fmaxf(fmaxf(mx, P[i]), P[i + 1]); mn = fminf(fminf(mn, P[i]), P[i + 1]); }
  mx = fmaxf(mx, P[15]); mn = fminf(mn, P[15]);
  { f32x2 a = both_halves(mx), b = both_halves(mn); mx = fmaxf(a.x, a.y); mn = fminf(b.x, b.y); }
  const float mxc = -mx * c, mnc = mn * c;
  f32x16 ep, en;
  for (int i = 0; i < 16; i += 2) {
    const f32x2 p2 = {P[i], P[i + 1]}, ap = p2 * c + mxc, an = mnc - p2 * c;
    ep[i] = __builtin_amdgcn_exp2f(ap.x); ep[i + 1] = __builtin_amdgcn_exp2f(ap.y);
    en[i] = __builtin_amdgcn_exp2f(an.x); en[i + 1] = __builtin_amdgcn_exp2f(an.y);
  }
  f32x2 sp2 = {ep[0], ep[1]}, sn2 = {en[0], en[1]};
  for (int i = 2; i < 12; i += 2) { sp2 += f32x2{ep[i], ep[i + 1]}; sn2 += f32x2{en[i], en[i + 1]}; }
  const float keep = h ? 0.f : 1.f;
  sp2 += (f32x2{ep[12], ep[13]} + f32x2{ep[14], ep[15]}) * keep;
  sn2 += (f32x2{en[12], en[13]} + f32x2{en[14], en[15]}) * keep;
  const f32x2 a = both_halves(sp2.x + sp2.y), b = both_halves(sn2.x + sn2.y);
  const float rp = __builtin_amdgcn_rcpf(a.x + a.y), rn = __builtin_amdgcn_rcpf(b.x + b.y);
  for (int i = 0; i < 16; i += 2) {
    const f32x2 u = f32x2{ep[i], ep[i + 1]} * rp, v = f32x2{en[i], en[i + 1]} * rn;
    ep[i] = u.x; ep[i + 1] = u.y; en[i] = v.x; en[i + 1] = v.y;
  }
  pf[0][0] = pack8(ep, 0); pf[0][1] = pack8(ep, 1); pf[1][0] = pack8(en, 0); pf[1][1] = pack8(en, 1);
}

constexpr int refill_end(int k) { return k < 0 ? 0 : k == 0 ? 7 : k == 1 ? 14 : k == 2 ? 20 : k == 3 ? 26 : k == 4 ? 32 : 36; }
constexpr bool refill_ok() { for (int k = 0; k < 6; ++k) for (int p = refill_end(k - 1); p < refill_end(k); ++p) { const int g = 9 + p / 4, s = 4 + 2 * k; if (s < g - 8 || 4 * (s + 2) > 4 * g - 2) return false; } return true; }
static_assert(refill_ok(), "refill schedule");
}

__global__ void __launch_bounds__(512, 2) fused_hedgehog_ffn(const char *__restrict__ gx, const char *__restrict__ gwqkv, const char *__restrict__ gwphi, const char *__restrict__ gwo,
                                                              const char *__restrict__ gw1, const char *__restrict__ gb1, const char *__restrict__ gw2, const char *__restrict__ gb2,
                                                              char *__restrict__ gout, char *__restrict__ gws) {
  __shared__ __attribute__((aligned(1024))) char smem[LDS_BYTES];
  lchar *L = (lchar *)smem;
  unsigned long long pc0;
  asm volatile("s_getpc_b64 %0" : "=s"(pc0));
  asm volatile("" ::"s"(gx), "s"(gwqkv), "s"(gwphi), "s"(gwo), "s"(gw1), "s"(gb1), "s"(gw2), "s"(gb2), "s"(gout), "s"(gws));
  const int tid = threadIdx.x, lane = tid & 63, w = rfl(tid >> 6);
  const int b = blockIdx.x, xcd = b & 7;
  int slot = b >> 3;
  STAMP_DECL; STAMP(0);
  const rsrc_t rx = __builtin_amdgcn_make_buffer_rsrc((void *)gx, 0, 2048 * XROW, 0x00020000);
  if (__builtin_expect(w >= 4 && slot < 30, 1)) {
    const bool a = slot < 16;
    const u32 lw = w - 4, lb = (u32)(size_t)L, hi = slot >= 23;
    const u32 rg0 = (xcd & 6) + (a ? slot >> 3 : hi), sox = (256 * rg0 + 64 * lw) * XROW, ldx = lb + lw * 8192;
    const u32 vox = (lane >> 3) * XROW + 16 * ((lane & 7) ^ ((lane >> 4) & 3));
    const rsrc_t rw = __builtin_amdgcn_make_buffer_rsrc((void *)(a ? gwqkv : gw1), 0, a ? DM * QKVROW : DM * W1ROW, 0x00020000);
    const u32 vow = a ? (lane >> 2) * QKVROW + ((lane & 3) == 3 ? 40 : 16 * (lane & 3)) : (lane >> 4) * W1ROW + 16 * ((lane & 15) ^ ((lane >> 4) << 2));
    const u32 swb = a ? 56 * (8 * (xcd & 1) + (slot & 7)) + 16 * lw * QKVROW : 256 * (7 * (xcd & 1) + slot - 16 - 7 * hi) + 16 * lw * W1ROW;
    const u32 ldw = lb + A_WOFF + lw * (a ? 1024 : 4096), ls = a ? 4096 : 1024, cs = a ? 64 * QKVROW : 64 * W1ROW;
    sfor<0, 2>([&](auto C) {
      constexpr u32 c = C;
      dmar(rw, vow, swb + c * cs + (a ? 896 : 0), ldw + c * A_SLOT);
      dmar(rw, vow, swb + c * cs + (a ? 1792 : 4 * W1ROW), ldw + c * A_SLOT + ls);
      dmar(rw, vow, swb + c * cs + (a ? 0 : 8 * W1ROW), ldw + c * A_SLOT + 2 * ls);
      if (!a) dmar(rw, vow, swb + c * cs + 12 * W1ROW, ldw + c * A_SLOT + 3 * ls);
      sfor<0, 8>([&](auto I) { constexpr u32 i = I; dmai<c * A_SLOT + i * 1024, 8 * i * XROW + 128 * c>(rx, (i & 1) ? vox ^ 64 : vox, sox, ldx); });
    });
  }
  asm volatile("" : "+s"(slot));
  const int h = lane >> 5;
  const rsrc_t rws = __builtin_amdgcn_make_buffer_rsrc((void *)gws, 0, WS_END, 0x00020000);
  const int tc = slot >= 16 ? slot - 16 : slot + 16;
  const bool has_tile = tc < 28;
  const u32 rt = tc / 7, cc = tc % 7;
  const bool att = slot < 16, ffn = !att && slot < 30;
  const int head = 8 * (xcd & 1) + (slot & 7);
  const int jf = slot - 16, ct = 7 * (xcd & 1) + (jf >= 7 ? jf - 7 : jf);
  const int rga = 2 * (xcd >> 1) + (att ? (slot >> 3) : (jf >= 7));

  if (w >= 4) {
    const int lw = w - 4;
    StageCtx sc;
    sc.rw2 = __builtin_amdgcn_make_buffer_rsrc((void *)gw2, 0, DF * XROW, 0x00020000);
    sc.rwo = __builtin_amdgcn_make_buffer_rsrc((void *)gwo, 0, DM * XROW, 0x00020000);
    sc.vob = (lane >> 3) * XROW + 16 * ((lane & 7) ^ (((lane >> 4) & 1) << 2));
    sc.so2 = rfl((896 * (lw >> 1) + 32 * (lw & 1)) * XROW + 128 * cc);
    sc.soo = rfl(28 * (8 * (lw >> 1) + (lw & 1)) * XROW + 128 * cc);
    sc.ld = rfl((u32)(size_t)L + (lw >> 1) * 8192 + (lw & 1) * 4096);
    u32x4 br[36];
    auto resid_dma = [&]() {
      const u32 vo = (lane >> 3) * XROW + 16 * ((lane & 7) ^ ((lane >> 4) & 3)), so = rfl((256 * xcd + 64 * rt + 16 * lw) * XROW + 128 * cc), ld = rfl((u32)(size_t)L + L_XT + lw * 2048);
      dmai<0, 0>(rx, vo, so, ld);
      dmai<1024, 8 * XROW>(rx, vo ^ 64, so, ld);
    };
    if (att) {
      const rsrc_t rw = __builtin_amdgcn_make_buffer_rsrc((void *)gwqkv, 0, DM * QKVROW, 0x00020000);
      gemmA_loader<true>(L, rx, rw, rfl((256 * rga + 64 * lw) * XROW), rfl(56 * head), lw, lane, sc, br, __builtin_amdgcn_make_buffer_rsrc((void *)gwphi, 0, 16 * 28 * 28 * 2, 0x00020000), rfl(head * 1568) STAMP_PASS);
      BAR();
      const u32 E = rfl(*(LAS u32 *)(L + L_E));
      if (has_tile) { sfor<0, 5>([&](auto S) { stage_dma<S>(sc); }); sfor<5, 7>([&](auto S) { stage_dma<S>(sc); }); resid_dma(); }
      sfor<15, 18>([&](auto S) { stage_load<S>(sc, br); });
      if (has_tile) vmwait<22>();
      BAR();
      __builtin_amdgcn_s_setprio(XPRIO);
      STAMP(4);
      for (int t = 0; t < 2; ++t) {
        lchar *sp = (lchar *)(size_t)(sc.ld + (2 + t) * L_SLOT + lane * 16);
        f32x4 s = *(LAS f32x4 *)sp;
        for (int ww = 1; ww < 4; ++ww) s += *(LAS f32x4 *)(sp + ww * 1024);
        st128_sc1(u32x4{pk2(s.x, s.y), E, pk2(s.z, s.w), E}, rws, lane * 16, WS_KVP + (head * 8 + rga) * 8192 + (t * 4 + lw) * 1024);
      }
      STAMP(5);
      poll_all(gws, WS_KVP + (head * 8 + (lane & 7)) * 8192 + (4 + lw) * 1024 + 63 * 16 + 12, E);
      STAMP(7);
      f32x2 ks[2][2];
#pragma unroll 1
      for (int spin = 0; spin < SPIN_CAP; ++spin) {
        u32x4 part[2][8];
        for (int t = 0; t < 2; ++t) for (int r = 0; r < 8; ++r) part[t][r] = ld128_sc1(rws, lane * 16, WS_KVP + (head * 8 + r) * 8192 + (t * 4 + lw) * 1024);
        u32 ta = E, to = E;
        for (int t = 0; t < 2; ++t) {
          ks[t][0] = f32x2{0.f, 0.f}; ks[t][1] = f32x2{0.f, 0.f};
          for (int r = 0; r < 8; ++r) {
            ks[t][0] += f32x2{bf2f(part[t][r][0]), bfhi(part[t][r][0])}; ks[t][1] += f32x2{bf2f(part[t][r][2]), bfhi(part[t][r][2])};
            ta &= part[t][r][1] & part[t][r][3]; to |= part[t][r][1] | part[t][r][3];
          }
        }
        if (__all(ta == E && to == E)) break;
        __builtin_amdgcn_s_sleep(POLL_SLEEP);
      }
      for (int t = 0; t < 2; ++t) {
        u32x2 pk = {pk2(ks[t][0].x, ks[t][0].y), pk2(ks[t][1].x, ks[t][1].y)};
        if (lw == 3 && h) pk = u32x2{0, 0};
        *(LAS u32x2 *)(L + L_KVF + ((t * 2 + (lw >> 1)) * 64 + lane) * 16 + (lw & 1) * 8) = pk;
      }
      __builtin_amdgcn_s_setprio(0);
      STAMP(8);
      if (!has_tile) {
        BAR();
        if (xcd == 0 && slot == 12 && lw == 0) {
#pragma unroll 1
          for (int spin = 0; spin < SPIN_CAP; ++spin) {
            bool ok = true;
            for (int i = 0; i < 32; ++i) ok &= ld_flag(gws, WS_STAT + (64 * i + lane) * 16 + 4) == E;
            if (__all(ok)) break;
            __builtin_amdgcn_s_sleep(8);
          }
          if (lane == 0) st_flag(gws, WS_EPOCH, E);
        }
        STAMP_OUT();
        return;
      }
    } else if (ffn) {
      const rsrc_t rw = __builtin_amdgcn_make_buffer_rsrc((void *)gw1, 0, DM * W1ROW, 0x00020000);
      gemmA_loader<false>(L, rx, rw, rfl((256 * rga + 64 * lw) * XROW), rfl(256 * ct), lw, lane, sc, br, rx, 0 STAMP_PASS);
      BAR();
      if (HOLD == 1 || HOLD == 3) asm volatile("s_barrier" ::: "memory");
      sfor<0, 5>([&](auto S) { stage_dma<S>(sc); });
      vmwait<0>();
      BAR();
      sfor<5, 9>([&](auto S) { stage_dma<S>(sc); });
      resid_dma();
      sfor<15, 18>([&](auto S) { stage_load<S>(sc, br); });
    } else {
      warm_up(pc0, rws, gw2, gwo, xcd, (slot - 30) * 8 + w, lane, (u32)(size_t)L);
      sfor<0, 9>([&](auto S) { stage_dma<S>(sc); });
      resid_dma();
      sfor<9, 18>([&](auto S) { stage_load<S>(sc, br); });
      vmwait<36>();
      BAR();
    }
    STAMP(10);
    const u32 ldv = sc.ld + lane * 16;
    sfor<0, 7>([&](auto K) {
      constexpr int k = K;
      if constexpr (k == 0) vmwait<12>();
      if constexpr (k == 1) vmwait<0>();
      BAR();
      if constexpr (k == 0) { if (att) sfor<7, 9>([&](auto S) { stage_dma<S>(sc); }); }
      sfor<refill_end(k - 1), refill_end(k)>([&](auto Pc) {
        constexpr int pc = Pc;
        *(LAS u32x4 *)((lchar *)(size_t)ldv + ((9 + pc / 4 + 4) % NSLOT) * L_SLOT + (pc & 3) * 1024) = br[pc];
        if (SPREAD) asm volatile("s_nop %0" ::"i"(SPREAD ? SPREAD - 1 : 0) : "memory");
      });
    });
    STAMP(12);
    asm volatile("s_barrier" ::: "memory");
    STAMP_OUT();
    return;
  }

  const Early early = early_issue(gws, rx, 8 * b + 2 * w, lane);
  u32 E;
  auto touches = [&]() {
    if (att || ffn) {
      constexpr u32 OOB = 0x7ffffff0u;
      const u32 lb = (u32)(size_t)L;
      { const u32 g15 = att ? (slot & 7) : 8 + (jf >= 7 ? jf - 7 : jf), i = (g15 * 4 + w) * 22 + lane;
        touch(rx, (lane < 22 && i < 1280) ? (256 * rga + (i & 255)) * XROW + 128 * (2 + (i >> 8)) : OOB, lb); }
      if (att) {
        const rsrc_t rq = __builtin_amdgcn_make_buffer_rsrc((void *)gwqkv, 0, DM * QKVROW, 0x00020000);
        const u32 i = slot * 240 + w * 60 + lane, row = (i * 43691u) >> 19, rem = i - 12 * row;
        touch(rq, lane < 60 ? (128 + row) * QKVROW + 896 * (rem >> 2) + ((448 * (xcd & 1)) & ~127u) + 128 * (rem & 3) : OOB, lb);
      } else {
        const rsrc_t r1 = __builtin_amdgcn_make_buffer_rsrc((void *)gw1, 0, DM * W1ROW, 0x00020000);
        const u32 l8 = 4 * (jf >= 7) + w, i = 64 * l8 + lane, j = 512 + 16 * l8 + lane;
        touch(r1, (128 + (i >> 1)) * W1ROW + 256 * ct + 128 * (i & 1), lb);
        touch(r1, lane < 16 ? (128 + (j >> 1)) * W1ROW + 256 * ct + 128 * (j & 1) : OOB, lb);
      }
    }
  };
  if (TOUCH_A == 2) touches();
  auto first = [&]() {
    E = early_finish(early, rws, 8 * b + 2 * w, lane); if (tid == 0) *(LAS u32 *)(L + L_E) = E;
    if (TOUCH_A == 1) touches();
  };
  const int rh = w & 1, kg = w >> 1;
  const u32 rb = 8 * xcd + 2 * rt + rh;
  const u32 row = 32 * rb + (lane & 31), col0 = 64 * cc + 32 * kg;
  u32x4 pq[2][2][2];
  const u32 hfa = WS_HFA + (rb * 14 + 7 * kg + ((lane & 7) < 7 ? (lane & 7) : 0)) * 64;
  u32 hlook = 0;
  auto o_stage = [&]() {
    u32x4 kf[2][2];
    for (int t = 0; t < 2; ++t) for (int s = 0; s < 2; ++s) kf[t][s] = *(LAS u32x4 *)(L + L_KVF + ((t * 2 + s) * 64 + lane) * 16);
    f32x16 num[2] = {zero16(), zero16()};
    for (int t = 0; t < 2; ++t) for (int s = 0; s < 2; ++s) for (int r = 0; r < 2; ++r) num[r] = mfma(kf[t][s], pq[r][t][s], num[r]);
    for (int r = 0; r < 2; ++r) {
      const float rc = __builtin_amdgcn_rcpf(both_halves(num[r][12]).x + 1e-6f);
      for (int i = 0; i < 16; i += 2) { const f32x2 v = f32x2{num[r][i], num[r][i + 1]} * rc; num[r][i] = v.x; num[r][i + 1] = v.y; }
      if (!h) { num[r][12] = 0.f; num[r][13] = 0.f; num[r][14] = 0.f; num[r][15] = 0.f; }
      const u32 so = WS_O + ((8 * rga + 2 * w + r) * 32 + 2 * head) * 1024;
      st128_sc1(pack8(num[r], 0), rws, lane * 16, so);
      st128_sc1(pack8(num[r], 1), rws, lane * 16, so + 1024);
    }
  };
  if (att) {
    if (tid < 16) *(LAS u32 *)(L + L_ZERO + 4 * tid) = 0;
    u32x4 idf[2];
    for (int s = 0; s < 2; ++s)
      for (int d = 0; d < 4; ++d) {
        const int k0 = 16 * s + 8 * (d >> 1) + 4 * h + 2 * (d & 1), n = lane & 31;
        idf[s][d] = (n == k0 ? 0x3F80u : 0u) | (n == k0 + 1 ? 0x3F800000u : 0u);
      }
    for (int s = 0; s < 2; ++s) asm volatile("" : "+v"(idf[s]));
    f32x16 acc[2][3]; u32x4 stat[2];
    const u32 vostat = WS_STAT + (256 * rga + 64 * w + (lane & 31)) * 16;
    gemmA_compute<true>(L, w, lane, acc, first, [&]() { if (TOUCH_A == 3) touches(); }, [&]() { for (int r = 0; r < 2; ++r) stat[r] = ld128_sc1(rws, vostat + 512 * r, 0); } STAMP_PASS);
    RowStat st[2];
    for (int r = 0; r < 2; ++r) {
      st[r] = row_stats(stat_settle(stat[r], rws, vostat + 512 * r, E), h);
      *(LAS float *)(L + L_TOK + w * 256 + r * 128 + 4 * (lane & 31)) = st[r].rstd;
    }
    BAR();
    STAMP(3);
    {
      const u32x4 wk = waug(cs_read(L, 0, lane), 0), wv = waug(cs_read(L, 1, lane), 0), wq = waug(cs_read(L, 2, lane), 0);
      for (int r = 0; r < 2; ++r) { acc[r][0] = mfma(wk, st[r].xaug, acc[r][0]); acc[r][1] = mfma(st[r].xaug, wv, acc[r][1]); acc[r][2] = mfma(wq, st[r].xaug, acc[r][2]); }
    }
    u32x4 wp[2];
    {
      const int blk = (lane >> 4) & 1, q = (lane & 15) >> 2, p = lane & 3;
      const u32 col = (blk && p == 3) ? 24 : 16 * blk + 4 * p;
      for (int s = 0; s < 2; ++s) {
        u32x2 r[2];
        for (int t = 0; t < 2; ++t) {
          const int dp = 16 * s + 8 * t + 4 * h + q;
          const u32 a = (dp >= 24 && dp < 28) ? L_ZERO + 8 * p : L_WPHI + (dp >= 28 ? dp - 4 : dp) * 56 + col * 2;
          r[t] = trd(L + a);
        }
        wp[s] = u32x4{r[0].x, r[0].y, r[1].x, r[1].y};
      }
    }
    f32x16 kvp[2] = {zero16(), zero16()};
    {
      f32x16 P[2];
      for (int r = 0; r < 2; ++r) P[r] = mfma(wp[0], pack8(acc[r][0], 0), zero16());
      for (int r = 0; r < 2; ++r) P[r] = mfma(wp[1], pack8(acc[r][0], 1), P[r]);
      u32x4 vf[2][2];
      for (int r = 0; r < 2; ++r) {
        f32x16 v = acc[r][1];
        for (int g = 0; g < 4; ++g) {
          const f32x4 rs = *(LAS f32x4 *)(L + L_TOK + w * 256 + r * 128 + (8 * g + 4 * h) * 4);
          for (int e = 0; e < 4; e += 2) { const f32x2 t2 = f32x2{v[4 * g + e], v[4 * g + e + 1]} * f32x2{rs[e], rs[e + 1]}; v[4 * g + e] = t2.x; v[4 * g + e + 1] = t2.y; }
        }
        for (int s = 0; s < 2; ++s) { vf[r][s] = pack8(v, s); if ((lane & 31) == 24) vf[r][s] = u32x4{0x3F803F80u, 0x3F803F80u, 0x3F803F80u, 0x3F803F80u}; }
      }
      u32x4 pf[2][2][2];
      for (int r = 0; r < 2; ++r) feature_map(P[r], st[r].rstd, h, pf[r]);
      f32x16 Z[2][2];
      for (int r = 0; r < 2; ++r) for (int t = 0; t < 2; ++t) Z[r][t] = mfma(pf[r][t][0], idf[0], zero16());
      for (int r = 0; r < 2; ++r) for (int t = 0; t < 2; ++t) Z[r][t] = mfma(pf[r][t][1], idf[1], Z[r][t]);
      for (int r = 0; r < 2; ++r) for (int s = 0; s < 2; ++s) for (int t = 0; t < 2; ++t) kvp[t] = mfma(pack8(Z[r][t], s), vf[r][s], kvp[t]);
    }
    for (int t = 0; t < 2; ++t)
      for (int g = 0; g < 4; ++g)
        *(LAS f32x4 *)(L + (2 + t) * L_SLOT + (g >> 1) * 8192 + (g & 1) * 4096 + w * 1024 + lane * 16) = f32x4{kvp[t][4 * g], kvp[t][4 * g + 1], kvp[t][4 * g + 2], kvp[t][4 * g + 3]};
    STAMP(4);
    BAR();
    {
      f32x16 P[2];
      for (int r = 0; r < 2; ++r) P[r] = mfma(wp[0], pack8(acc[r][2], 0), zero16());
      for (int r = 0; r < 2; ++r) P[r] = mfma(wp[1], pack8(acc[r][2], 1), P[r]);
      for (int r = 0; r < 2; ++r) {
        feature_map(P[r], st[r].rstd, h, pq[r]);
        for (int t = 0; t < 2; ++t) for (int s = 0; s < 2; ++s) asm volatile("" : "+v"(pq[r][t][s]));
        if (r == 0 && has_tile) hlook = ld_flag(gws, hfa);
        __builtin_amdgcn_sched_barrier(0);
      }
    }
    STAMP(6);
    if (!has_tile) {
      BAR();
      o_stage();
      vmwait<0>();
      if (lane < 2) st_flag(gws, WS_OF + ((8 * rga + 2 * w + lane) * 16 + head) * 64, E);
      STAMP_OUT();
      return;
    }
  } else if (ffn) {
    u32 bias[4];
    for (int t = 0; t < 4; ++t) bias[t] = *(const GAS unsigned short *)(gb1 + (128 * ct + 32 * t + (lane & 31)) * 2);
    f32x16 acc[2][4]; u32x4 stat[2];
    const u32 vostat = WS_STAT + (256 * rga + 64 * w + (lane & 31)) * 16;
    gemmA_compute<false>(L, w, lane, acc, first, [&]() { if (TOUCH_A == 3) touches(); }, [&]() { for (int r = 0; r < 2; ++r) stat[r] = ld128_sc1(rws, vostat + 512 * r, 0); } STAMP_PASS);
    RowStat st[2];
    for (int r = 0; r < 2; ++r) st[r] = row_stats(stat_settle(stat[r], rws, vostat + 512 * r, E), h);
    BAR();
    STAMP(3);
    if (HOLD == 2) __builtin_amdgcn_s_setprio(3);
    for (int t = 0; t < 4; ++t) {
      const u32x4 wa = waug(cs_read(L, t, lane), bias[t] | (bias[t] << 16));
      for (int r = 0; r < 2; ++r) {
        f32x16 a = mfma(wa, st[r].xaug, acc[r][t]);
        for (int i = 0; i < 16; i += 2) { f32x2 v = {relu(a[i]), relu(a[i + 1])}; v *= v; a[i] = v.x; a[i + 1] = v.y; }
        const u32 so = WS_H + ((8 * rga + 2 * w + r) * 112 + 8 * ct + 2 * t) * 1024;
        st128_sc1(pack8(a, 0), rws, lane * 16, so);
        st128_sc1(pack8(a, 1), rws, lane * 16, so + 1024);
      }
      if (HOLD == 3 && t == 1) asm volatile("s_barrier" ::: "memory");
      if (t == 2) {
        vmwait<4>();
        if (lane < 2) st_flag(gws, WS_HFA + ((8 * rga + 2 * w + lane) * 14 + ct) * 64, E);
      }
    }
    if (HOLD == 1) asm volatile("s_barrier" ::: "memory");
    if (HOLD == 2) __builtin_amdgcn_s_setprio(0);
    STAMP(4);
    vmwait<0>();
    if (lane < 2) st_flag(gws, WS_HFB + ((8 * rga + 2 * w + lane) * 14 + ct) * 64, E);
    STAMP(5);
    for (int t = 0; t < 2; ++t) for (int r = 0; r < 2; ++r) for (int s = 0; s < 2; ++s) pq[t][r][s] = u32x4{0, 0, 0, 0};
  } else {
    warm_up(pc0, rws, gw2, gwo, xcd, (slot - 30) * 8 + w, lane, (u32)(size_t)L);
    first();
    for (int t = 0; t < 2; ++t) for (int r = 0; r < 2; ++r) for (int s = 0; s < 2; ++s) pq[t][r][s] = u32x4{0, 0, 0, 0};
  }

  {
    constexpr int RD = RING;
    const u32 soh = WS_H + (rb * 112 + 56 * kg) * 1024, soo = WS_O + (rb * 32 + 16 * kg) * 1024;
    u32x4 ring[RD];
    u32x2 b2v[4]; u32x4 sg0;
    const int blk = (lane >> 4) & 1, q = (lane & 15) >> 2, p = lane & 3;
    u32 bb0[2], bb[2];
    for (int c2 = 0; c2 < 2; ++c2) bb0[c2] = (u32)(size_t)L + kg * 8192 + (4 * h + q) * 128 + ((c2 ^ kg ^ (q >> 1)) * 64) + 32 * blk + 8 * p;
    f32x16 af[2] = {zero16(), zero16()}, aa[2] = {zero16(), zero16()};
    constexpr int PD = BPD;
    u32x4 bf[PD + 1][2];
    const u32 hfb = WS_HFB + (rb * 14 + 7 * kg + ((lane & 7) < 7 ? (lane & 7) : 0)) * 64, ofo = WS_OF + (rb * 16 + 8 * kg + (lane & 7)) * 64;
    u32 look = 0, soe = 0;
    bool o_early = false;
    float rs2; f32x2 bx[8], sg[8], xx[8];
    u32x4 oa[16];
    if (!__all(hlook == E)) poll_all(gws, hfa, E);
    STAMP(10);
    if (!att) BAR();
    sfor<0, 72>([&](auto I) {
      constexpr int i = I;
      if constexpr (i == 56) {
        for (int g = 0; g < 4; ++g)
          *(LAS f32x4 *)(L + 6 * L_SLOT + w * 4096 + g * 1024 + lane * 16) = f32x4{af[1][4 * g], af[1][4 * g + 1], af[1][4 * g + 2], af[1][4 * g + 3]};
        BAR();
        for (int g = 0; g < 4; ++g) {
          const f32x4 pf = *(LAS f32x4 *)(L + 6 * L_SLOT + (w ^ 2) * 4096 + g * 1024 + lane * 16);
          for (int e = 0; e < 2; ++e) {
            const int j = 4 * g + 2 * e;
            const f32x2 y = (f32x2{af[0][j], af[0][j + 1]} + f32x2{pf[2 * e], pf[2 * e + 1]}) * rs2 + bx[2 * g + e];
            const f32x2 t = y * -LOG2E;
            const f32x2 d = f32x2{__builtin_amdgcn_exp2f(t.x), __builtin_amdgcn_exp2f(t.y)} + 1.0f;
            sg[2 * g + e] = y * f32x2{__builtin_amdgcn_rcpf(d.x), __builtin_amdgcn_rcpf(d.y)};
            asm volatile("" : "+v"(sg[2 * g + e]));
          }
        }
        STAMP(14);
        if (!o_early) {
          if (!__all(look == E)) poll_all(gws, ofo, E);
          for (int j = 0; j < OB; ++j) oa[j] = ld128_sc1(rws, lane * 16, soo + j * 1024);
        }
        __builtin_amdgcn_sched_barrier(0);
      }
      if constexpr (i >= 16 && (i & 7) == 0 && i != 56) asm volatile("s_barrier" ::: "memory");
      if constexpr (i == 0) STAMP(11);
      if constexpr (i == 0) {
        for (int j = 0; j < RD; ++j) ring[j] = ld128_sc1(rws, lane * 16, soh + h_step(j) * 1024);
        __builtin_amdgcn_sched_barrier(0);
        for (int c2 = 0; c2 < 2; ++c2) { bb[c2] = bb0[c2] + 4 * L_SLOT; asm volatile("" : "+v"(bb[c2])); for (int j = 0; j < PD; ++j) bf[j][c2] = trfrag((lchar *)(size_t)bb[c2] + j * 2048, 1024); }
      }
      static_assert(28 - RD - 6 < 16, "no look between the o stores and their counted wait");
      if constexpr (i == 28 - RD - 6) look = ld_flag(gws, hfb);
      if constexpr (i == 28 - RD) { if (!__all(look == E)) poll_all(gws, hfb, E); }
      if constexpr (i == 44) {
        for (int g = 0; g < 4; ++g) b2v[g] = *(const GAS u32x2 *)(gb2 + (col0 + 8 * g + 4 * h) * 2);
        sg0 = ld128_sc1(rws, WS_STAT + row * 16, 0);
      }
      if constexpr (i == 40) look = ld_flag(gws, ofo);
      if constexpr (i == 47) { o_early = __all(look == E); soe = o_early ? soo : WS_END; }
      if constexpr (i >= 48 && i < 48 + OB) oa[i - 48] = ld128_sc1(rws, lane * 16, soe + (i - 48) * 1024);
      if constexpr (i == 50) look = ld_flag(gws, ofo);
      if constexpr (i == 52) {
        u32x4 g4 = sg0;
        if (!__all(g4[1] == E && g4[3] == E)) g4 = stat_settle(g4, rws, WS_STAT + row * 16, E);
        const float rs1 = __builtin_amdgcn_rcpf(asf(g4[2]));
        rs2 = rs1 * rs1;
        for (int g = 0; g < 4; ++g) for (int e = 0; e < 2; ++e) { bx[2 * g + e] = f32x2{bf2f(b2v[g][e]), bfhi(b2v[g][e])}; asm volatile("" : "+v"(bx[2 * g + e])); }
      }
      if constexpr (i == 60) {
        for (int g = 0; g < 4; ++g) {
          const u32 r64 = 32 * rh + (lane & 31);
          const u32x2 xr = *(LAS u32x2 *)(L + L_XT + r64 * 128 + 16 * ((4 * kg + g) ^ ((r64 >> 1) & 7)) + 8 * h);
          for (int e = 0; e < 2; ++e) { xx[2 * g + e] = f32x2{bf2f(xr[e]), bfhi(xr[e])}; asm volatile("" : "+v"(xx[2 * g + e])); }
        }
      }
      if constexpr (i == 16) { if (att) o_stage(); }
      if constexpr (i == 16 + OFD) {
        if (att) { vmwait<OFD>(); if (lane < 2) st_flag(gws, WS_OF + ((8 * rga + 2 * w + lane) * 16 + head) * 64, E); }
      }
      u32x4 a;
      if constexpr (i < 56) a = ring[i % RD]; else a = oa[i - 56];
      if constexpr (i + PD < 72) {
        constexpr int n = i + PD;
        if constexpr ((n & 3) == 0) for (int c2 = 0; c2 < 2; ++c2) { bb[c2] = bb0[c2] + (((n >> 2) + 4) % NSLOT) * L_SLOT; asm volatile("" : "+v"(bb[c2])); }
        for (int c2 = 0; c2 < 2; ++c2) bf[n % (PD + 1)][c2] = trfrag((lchar *)(size_t)bb[c2] + (n & 3) * 2048, 1024);
      }
      if constexpr (i < 56) { af[0] = mfma(bf[i % (PD + 1)][0], a, af[0]); af[1] = mfma(bf[i % (PD + 1)][1], a, af[1]); asm volatile("" : "+v"(af[0]), "+v"(af[1])); }
      else { aa[0] = mfma(bf[i % (PD + 1)][0], a, aa[0]); aa[1] = mfma(bf[i % (PD + 1)][1], a, aa[1]); asm volatile("" : "+v"(aa[0]), "+v"(aa[1])); }
      if constexpr (i + RD < 56) ring[i % RD] = ld128_sc1(rws, lane * 16, soh + h_step(i + RD) * 1024);
      if constexpr (i >= 56 && i - 56 + OB < 16) oa[i - 56 + OB] = ld128_sc1(rws, lane * 16, soo + (i - 56 + OB) * 1024);
      __builtin_amdgcn_sched_barrier(0);
    });
    STAMP(12);
    for (int g = 0; g < 4; ++g)
      *(LAS f32x4 *)(L + 8 * L_SLOT + w * 4096 + g * 1024 + lane * 16) = f32x4{aa[1][4 * g], aa[1][4 * g + 1], aa[1][4 * g + 2], aa[1][4 * g + 3]};
    BAR();
    u32x2 o2[4];
    for (int g = 0; g < 4; ++g) {
      const f32x4 pa = *(LAS f32x4 *)(L + 8 * L_SLOT + (w ^ 2) * 4096 + g * 1024 + lane * 16);
      for (int e = 0; e < 2; ++e) {
        const int i = 4 * g + 2 * e;
        const f32x2 r = (f32x2{aa[0][i], aa[0][i + 1]} + f32x2{pa[2 * e], pa[2 * e + 1]}) * sg[2 * g + e] + xx[2 * g + e];
        o2[g][e] = pk2(r.x, r.y);
      }
    }
    const rsrc_t rout = __builtin_amdgcn_make_buffer_rsrc((void *)gout, 0, 2048 * XROW, 0x00020000);
    for (int g = 0; g < 4; g += 2) {
      auto rx0 = __builtin_amdgcn_permlane32_swap(o2[g].x, o2[g + 1].x, false, false);
      auto ry0 = __builtin_amdgcn_permlane32_swap(o2[g].y, o2[g + 1].y, false, false);
      __builtin_amdgcn_raw_buffer_store_b128(u32x4{rx0[0], ry0[0], rx0[1], ry0[1]}, rout, row * XROW + (col0 + 8 * (g + h)) * 2, 0, OUT_AUX);
    }
    STAMP(13);
    STAMP(15);
    STAMP_OUT();
  }
}

extern "C" void kernel_launch(void *const *d_in, const int *in_sizes, int n_in, void *d_out, int out_size, void *d_ws, size_t ws_size, hipStream_t stream) {
  fused_hedgehog_ffn<<<256, 512, 0, stream>>>((const char *)d_in[0], (const char *)d_in[1], (const char *)d_in[2], (const char *)d_in[3], (const char *)d_in[5],
                                              (const char *)d_in[6], (const char *)d_in[7], (const char *)d_in[8], (char *)d_out, (char *)d_ws);
}
```

```cpp
#include <hip/hip_runtime.h>
#include <hip/hip_bf16.h>
#include <stdint.h>
#include <type_traits>

typedef unsigned u32;
typedef u32 u32x4 __attribute__((ext_vector_type(4)));
typedef u32 u32x2 __attribute__((ext_vector_type(2)));
typedef float f32x16 __attribute__((ext_vector_type(16)));
typedef float f32x4 __attribute__((ext_vector_type(4)));
typedef float f32x2 __attribute__((ext_vector_type(2)));
typedef short s16x8 __attribute__((ext_vector_type(8)));
typedef short s16x4 __attribute__((ext_vector_type(4)));
typedef __bf16 bf16x2 __attribute__((ext_vector_type(2)));
typedef __bf16 bf16x8 __attribute__((ext_vector_type(8)));
#define LAS __attribute__((address_space(3)))
#define GAS __attribute__((address_space(1)))
#define DI __device__ __forceinline__
typedef __amdgpu_buffer_rsrc_t rsrc_t;
typedef LAS char lchar;

namespace {
constexpr int DM = 448, DF = 1792;
constexpr u32 XROW = DM * 2;
constexpr u32 QKVROW = 3 * DM * 2;
constexpr u32 W1ROW = DF * 2;
constexpr u32 WS_EPOCH = 0;
constexpr u32 WS_STAT = 4096;
constexpr u32 WS_KF = 65536;
constexpr u32 WS_HFA = 98304;
constexpr u32 WS_HFB = WS_HFA + 57344;
constexpr u32 WS_OF = WS_HFB + 57344;
constexpr u32 WS_KVP = 294912;
constexpr u32 WS_H = WS_KVP + (1u << 20);
constexpr u32 WS_O = WS_H + 64 * 112 * 1024;
constexpr u32 WS_END = WS_O + 64 * 32 * 1024;
constexpr u32 L_SLOT = 16384, NSLOT = 9;
constexpr u32 A_SLOT = 49152, A_WOFF = 32768;
constexpr u32 L_MISC = NSLOT * L_SLOT;
constexpr u32 L_WPHI = L_MISC;
constexpr u32 L_ZERO = L_MISC + 2048;
constexpr u32 L_E = L_MISC + 2112;
constexpr u32 L_DUMMY = L_MISC + 2304;
constexpr u32 L_CSP = L_MISC + 2560;
constexpr u32 L_TOK = L_CSP + 512;
constexpr u32 L_KVF = L_TOK + 1024;
constexpr u32 L_XT = L_KVF + 4096;
constexpr u32 L_END = L_XT + 8192;
constexpr u32 LDS_BYTES = 163840;
static_assert(L_END <= LDS_BYTES, "lds");
constexpr int SPIN_CAP = 1 << 16;
constexpr float LOG2E = 1.4426950408889634f;
#define CS_DOT2 0
#define SGB 1
#define LPRIO 0
#define FIRST_AT -1
#define TOUCH_A 0
#define POLL_SLEEP 3
#define HOLD 1
#define XPRIO 3
#define WARM 7
#define BPD 3
#define OB 8
#define FFN_ALL 0
#define OFD 10
#define CX_ATT 3
#define CX_FFN 0
#define OUT_AUX 0
#define SPREAD 0
#define PIN_C 0
#define RING 10

#define STAMP(i) do {} while (0)
#define STAMP_DECL do {} while (0)
#define STAMP_OUT() do {} while (0)
#define STAMP_ARG
#define STAMP_PASS
template <int B, int E, class F> DI void sfor(F &&f) {
  if constexpr (B < E) { f(std::integral_constant<int, B>{}); sfor<B + 1, E>(f); }
}
DI u32 rfl(u32 v) { return __builtin_amdgcn_readfirstlane(v); }
DI f32x16 mfma(u32x4 a, u32x4 b, f32x16 c) {
  return __builtin_amdgcn_mfma_f32_32x32x16_bf16(__builtin_bit_cast(bf16x8, a), __builtin_bit_cast(bf16x8, b), c, 0, 0, 0);
}
DI u32 pk2(float lo, float hi) { f32x2 p = {lo, hi}; return __builtin_bit_cast(u32, __builtin_convertvector(p, bf16x2)); }
DI float dot2(u32 a, u32 b, float c) { return __builtin_amdgcn_fdot2_f32_bf16(__builtin_bit_cast(bf16x2, a), __builtin_bit_cast(bf16x2, b), c, false); }
DI float bf2f(u32 lo16) { return __builtin_bit_cast(float, lo16 << 16); }
DI float bfhi(u32 v) { return __builtin_bit_cast(float, v & 0xffff0000u); }
DI float asf(u32 v) { return __builtin_bit_cast(float, v); }
DI f32x2 both_halves(float v) {
  const u32 a = __builtin_bit_cast(u32, v);
  auto r = __builtin_amdgcn_permlane32_swap(a, a, false, false);
  return f32x2{asf(r[0]), asf(r[1])};
}
DI float half_lo(float v) { return both_halves(v).x; }
DI u32x4 pack8(const f32x16 &x, int s) { return u32x4{pk2(x[8 * s], x[8 * s + 1]), pk2(x[8 * s + 2], x[8 * s + 3]), pk2(x[8 * s + 4], x[8 * s + 5]), pk2(x[8 * s + 6], x[8 * s + 7])}; }
DI u32x2 trd(lchar *p) {
  typedef short v4i16 __attribute__((ext_vector_type(4)));
  return __builtin_bit_cast(u32x2, __builtin_amdgcn_ds_read_tr16_b64_v4i16((LAS v4i16 *)p));
}
DI u32x4 trfrag(lchar *p, int tstride) { u32x2 a = trd(p), b = trd(p + tstride); return u32x4{a.x, a.y, b.x, b.y}; }
DI float relu(float v) { return __builtin_amdgcn_fmed3f(v, 0.f, __builtin_inff()); }
DI f32x16 zero16() { f32x16 z; for (int i = 0; i < 16; ++i) z[i] = 0.f; return z; }
#define BAR() asm volatile("s_waitcnt lgkmcnt(0)\n\ts_barrier" ::: "memory")
template <int N> DI void vmwait() { asm volatile("s_waitcnt vmcnt(%0)" ::"i"(N) : "memory"); }
DI void dma16(rsrc_t r, u32 voff, u32 soff, u32 ldsaddr) {
  asm volatile("s_mov_b32 m0, %0\n\ts_nop 0\n\tbuffer_load_dwordx4 %1, %2, %3 offen lds" ::"s"(ldsaddr), "v"(voff), "s"(r), "s"(soff) : "memory", "m0");
}
template <int OFF> DI void ald128(u32x4 &d, rsrc_t r, u32 voff, u32 soff) {
  asm volatile("buffer_load_dwordx4 %0, %1, %2, %3 offen offset:%4" : "=v"(d) : "v"(voff), "s"(r), "s"(soff), "i"(OFF) : "memory");
}
DI u32x4 ld128_sc1(rsrc_t r, u32 voff, u32 soff) { return __builtin_amdgcn_raw_buffer_load_b128(r, voff, soff, 16); }
DI void st128_sc1(u32x4 v, rsrc_t r, u32 voff, u32 soff) {
  __builtin_amdgcn_raw_buffer_store_b128(v, r, voff, soff, 16);
  asm volatile("s_nop 1" ::"v"(v) : "memory");
}
typedef GAS u32 gu32;
DI u32 ld_flag(const char *ws, u32 off) { return __hip_atomic_load((gu32 *)(ws + off), __ATOMIC_RELAXED, __HIP_MEMORY_SCOPE_AGENT); }
DI void st_flag(char *ws, u32 off, u32 v) { __hip_atomic_store((gu32 *)(ws + off), v, __ATOMIC_RELAXED, __HIP_MEMORY_SCOPE_AGENT); }
DI void poll_all(const char *ws, u32 off, u32 E) {
#pragma unroll 1
  for (int spin = 0; spin < SPIN_CAP; ++spin) {
    if (__all(ld_flag(ws, off) == E)) break;
    __builtin_amdgcn_s_sleep(POLL_SLEEP);
  }
  __builtin_amdgcn_fence(__ATOMIC_ACQUIRE, "wavefront");
}
template <u32 LI, u32 SI> DI void dmai(rsrc_t r, u32 voff, u32 sob, u32 ldsb) {
  u32 t;
  asm volatile("s_add_u32 m0, %2, %5\n\ts_add_u32 %0, %1, %6\n\tbuffer_load_dwordx4 %3, %4, %0 offen lds"
               : "=&s"(t) : "s"(sob), "s"(ldsb), "v"(voff), "s"(r), "i"(LI), "i"(SI) : "memory", "m0", "scc");
}
DI void dmar(rsrc_t r, u32 voff, u32 soff, u32 lds) {
  asm volatile("s_mov_b32 m0, %0\n\ts_nop 0\n\tbuffer_load_dwordx4 %1, %2, %3 offen lds" ::"s"(lds), "v"(voff), "s"(r), "s"(soff) : "memory", "m0");
}
template <int CTRL, int RMASK> DI float dpp_add(float v) {
  return v + asf(__builtin_amdgcn_update_dpp(0u, __builtin_bit_cast(u32, v), CTRL, RMASK, 0xf, true));
}
DI float wave_sum63(float v) {
  v = dpp_add<0x111, 0xf>(v); v = dpp_add<0x112, 0xf>(v); v = dpp_add<0x114, 0xf>(v); v = dpp_add<0x118, 0xf>(v);
  v = dpp_add<0x142, 0xa>(v); v = dpp_add<0x143, 0xc>(v);
  return v;
}
DI void st64_sc1(char *ws, u32 off, u32 lo, u32 hi) {
  __hip_atomic_store((GAS unsigned long long *)(ws + off), (unsigned long long)lo | ((unsigned long long)hi << 32), __ATOMIC_RELAXED, __HIP_MEMORY_SCOPE_AGENT);
}
struct Early { u32 ev; u32x4 xa, xb; };
DI Early early_issue(char *ws, rsrc_t rx, int row0, int lane) {
  Early e;
  e.ev = ld_flag(ws, WS_EPOCH);
  const u32 vo = lane < 56 ? (u32)lane * 16 : 0x7ffffff0u;
  e.xa = __builtin_amdgcn_raw_buffer_load_b128(rx, vo, row0 * XROW, 0); e.xb = __builtin_amdgcn_raw_buffer_load_b128(rx, vo, (row0 + 1) * XROW, 0);
  return e;
}
DI u32 early_finish(const Early &e, rsrc_t rws, int row0, int lane) {
  const u32 E = rfl(e.ev) + 1, ONES = 0x3F803F80u;
  float s[2], q[2];
  for (int r = 0; r < 2; ++r) {
    const u32x4 &x = r ? e.xb : e.xa;
    s[r] = dot2(x[0], ONES, 0.f); q[r] = dot2(x[0], x[0], 0.f);
    for (int d = 1; d < 4; ++d) { s[r] = dot2(x[d], ONES, s[r]); q[r] = dot2(x[d], x[d], q[r]); }
    s[r] = wave_sum63(s[r]); q[r] = wave_sum63(q[r]);
    const float mean = s[r] * (1.0f / DM);
    const float sig = __builtin_amdgcn_sqrtf(fmaxf(q[r] * (1.0f / DM) - mean * mean, 0.f) + 1e-5f);
    const u32 vo = lane == 63 ? WS_STAT + (row0 + r) * 16 : 0x7ffffff0u;
    __builtin_amdgcn_raw_buffer_store_b64(u32x2{__builtin_bit_cast(u32, mean), E}, rws, vo, 0, 16);
    __builtin_amdgcn_raw_buffer_store_b64(u32x2{__builtin_bit_cast(u32, sig), E}, rws, vo + 8, 0, 16);
  }
  return E;
}
DI u32x4 stat_settle(u32x4 g, rsrc_t rws, u32 vostat, u32 E) {
#pragma unroll 1
  for (int spin = 0; spin < SPIN_CAP && !__all(g[1] == E && g[3] == E); ++spin) { __builtin_amdgcn_s_sleep(1); g = ld128_sc1(rws, vostat, 0); }
  return g;
}
struct RowStat { float rstd; u32x4 xaug; };
DI RowStat row_stats(u32x4 g, int h) {
  const float m = -asf(g[0]), sig = asf(g[2]);
  RowStat r; r.rstd = __builtin_amdgcn_rcpf(sig);
  const float ml = m - bfhi(pk2(m, m)), sl = sig - bfhi(pk2(sig, sig));
  const u32 mm = pk2(m, ml), ss = pk2(sig, sl);
  r.xaug = h ? u32x4{0, 0, 0, 0} : u32x4{mm, mm, ss, 0};
  return r;
}
DI u32x4 waug(float cs, u32 bb) {
  const u32 hh = pk2(cs, cs); const float cl = cs - bfhi(hh);
  return u32x4{hh, pk2(cl, cl), bb, 0};
}
DI float cs_read(lchar *L, int t, int lane) { return *(LAS float *)(L + L_CSP + (t * 32 + (lane & 31)) * 4); }

DI void touch(rsrc_t r, u32 voff, u32 lb) {
  asm volatile("s_mov_b32 m0, %0\n\ts_nop 0\n\tbuffer_load_dword %1, %2, 0 offen lds" ::"s"(lb + L_DUMMY), "v"(voff), "s"(r) : "memory", "m0");
}
constexpr u32 CODE_BYTES = 50 * 1024, CODE_PIECES = (CODE_BYTES + 8191) / 8192;
DI void warm_up(unsigned long long pc, rsrc_t rws, const char *gw2, const char *gwo, int xcd, int wv, int lane, u32 lb) {
  if (WARM & 1) {
    const rsrc_t rc = __builtin_amdgcn_make_buffer_rsrc((void *)(pc & ~127ull), 0, CODE_BYTES, 0x00020000);
    if (wv < (int)CODE_PIECES) touch(rc, wv * 8192 + lane * 128, lb);
  }
  const int g = xcd * 16 + wv;
  if (WARM & 2) { if (g < 27) touch(rws, WS_KF + g * 8192 + lane * 128, lb); }
  if (WARM & 4) {
    const rsrc_t r2 = __builtin_amdgcn_make_buffer_rsrc((void *)gw2, 0, DF * XROW, 0x00020000), ro = __builtin_amdgcn_make_buffer_rsrc((void *)gwo, 0, DM * XROW, 0x00020000);
    touch(r2, g * 8192 + lane * 128, lb);
    if (g < 68) touch(r2, (128 + g) * 8192 + lane * 128, lb);
    else if (g < 117) touch(ro, (g - 68) * 8192 + lane * 128, lb);
  }
}
template <bool ATT, class First, class After0, class Mid>
DI void gemmA_compute(lchar *L, rsrc_t rx, u32 sox, int w, int lane, f32x16 (&acc)[2][ATT ? 3 : 4], First &&first, After0 &&after0, Mid &&mid STAMP_ARG) {
  constexpr int T = ATT ? 3 : 4, ROWB = ATT ? 64 : 256;
  constexpr int CX = ATT ? CX_ATT : CX_FFN;
  const u32 vox0 = (lane >> 3) * XROW + 16 * ((lane & 7) ^ ((lane >> 4) & 3)), ldx = rfl((u32)(size_t)L + w * 8192);
  const int h = lane >> 5, blk = (lane >> 4) & 1, q = (lane & 15) >> 2, p = lane & 3;
  const u32 lb = (u32)(size_t)L;
  u32 wb0[T], xb0[4], wb[T], xb[4], cb0, cb;
  for (int t = 0; t < T; ++t) wb0[t] = lb + A_WOFF + (ATT ? t * 4096 + (8 * h + q) * 64 : (8 * h + q) * 256 + 64 * (t ^ q)) + 32 * blk + 8 * p;
  for (int u = 0; u < 4; ++u) xb0[u] = lb + w * 8192 + (lane & 31) * 128 + 16 * ((2 * u + h) ^ ((lane >> 1) & 7));
  const int tw = w < T ? w : T - 1;
  cb0 = lb + A_WOFF + (ATT ? tw * 4096 + (8 * h + q) * 64 : (8 * h + q) * 256 + 64 * (tw ^ q)) + 32 * blk + 8 * p;
  f32x2 ca = {0.f, 0.f};
  for (int r = 0; r < 2; ++r) for (int t = 0; t < T; ++t) acc[r][t] = zero16();
  u32x4 xf[2][2], wf[2][T], cf[2];
  sfor<0, 29>([&](auto I) {
    constexpr int i = I, c = i >> 2, u = i & 3;
    if constexpr (i == 0 && FIRST_AT < 0) { first(); STAMP(14); }
    if constexpr (u == 0 && i < 28) {
      if constexpr (CX && c >= 2) vmwait<(c == 6 ? 2 : CX)>();
      BAR();
      if constexpr (c == 0) STAMP(1);
      constexpr u32 so = (c % 3) * A_SLOT;
      for (int t = 0; t < T; ++t) { wb[t] = wb0[t] + so; asm volatile("" : "+v"(wb[t])); }
      for (int j = 0; j < 4; ++j) { xb[j] = xb0[j] + so; asm volatile("" : "+v"(xb[j])); }
      cb = cb0 + so; asm volatile("" : "+v"(cb));
    }
    if constexpr (i < 28) {
      cf[i & 1] = trfrag((lchar *)(size_t)cb + 16 * u * ROWB, 4 * ROWB);
      for (int r = 0; r < 2; ++r) xf[i & 1][r] = *(LAS u32x4 *)((lchar *)(size_t)xb[u] + r * 4096);
      for (int t = 0; t < T; ++t) wf[i & 1][t] = trfrag((lchar *)(size_t)wb[t] + 16 * u * ROWB, 4 * ROWB);
    }
    if constexpr (i == FIRST_AT) first();
    if constexpr (i == 1) after0();
    if constexpr (i == 20) mid();
    if constexpr (i > 0) {
      constexpr int j = (i - 1) & 1;
      for (int t = 0; t < T; ++t)
        for (int r = 0; r < 2; ++r) acc[r][t] = (ATT && t == 1) ? mfma(xf[j][r], wf[j][t], acc[r][t]) : mfma(wf[j][t], xf[j][r], acc[r][t]);
      for (int d = 0; d < 4; ++d) ca += f32x2{bf2f(cf[j][d]), bfhi(cf[j][d])};
      asm volatile("" : "+v"(ca));
      if constexpr (i < 28 && c + 2 < 7 && u >= 1 && u - 1 < CX) dmai<((c + 2) % 3) * A_SLOT + (u - 1) * 1024, 8 * (u - 1) * XROW + 128 * (c + 2)>(rx, ((u - 1) & 1) ? vox0 ^ 64 : vox0, sox, ldx);
      for (int m = 0; m < 2 * T; ++m) { __builtin_amdgcn_sched_group_barrier(0x8, 1, 0); __builtin_amdgcn_sched_group_barrier(0x100, 2, 0); __builtin_amdgcn_sched_group_barrier(0x2, 2, 0); }
    }
    __builtin_amdgcn_sched_barrier(0);
  });
  { const f32x2 a = both_halves(ca.x + ca.y); if (lane < 32 && w < T) *(LAS float *)(L + L_CSP + (w * 32 + lane) * 4) = a.x + a.y; }
  STAMP(2);
}
constexpr int w2_row(int st) { return st < 7 ? 128 * st : 128 * (st - 7) + 64; }
constexpr int h_step(int i) { return i < 28 ? 8 * (i >> 2) + (i & 3) : 8 * ((i - 28) >> 2) + 4 + (i & 3); }
struct StageCtx { rsrc_t rw2, rwo; u32 vob, so2, soo, ld; };
template <int ST> DI void stage_dma(const StageCtx &c) {
  sfor<0, 4>([&](auto I) {
    constexpr u32 i = I, dst = ((ST + 4) % NSLOT) * L_SLOT + i * 1024;
    dmai<dst, (w2_row(ST) + 8 * i) * XROW>(c.rw2, c.vob, c.so2, c.ld);
  });
}
template <int ST> DI void stage_load(const StageCtx &c, u32x4 (&br)[36]) {
  sfor<0, 4>([&](auto I) {
    constexpr u32 i = I;
    if constexpr (ST < 14) br[4 * (ST - 9) + i] = __builtin_amdgcn_raw_buffer_load_b128(c.rw2, c.vob, c.so2 + (w2_row(ST) + 8 * i) * XROW, 0);
    else br[4 * (ST - 9) + i] = __builtin_amdgcn_raw_buffer_load_b128(c.rwo, c.vob, c.soo + (56 * (ST - 14) + (i == 3 ? 20 : 8 * i)) * XROW, 0);
  });
}
template <bool ATT>
DI void gemmA_loader(lchar *L, rsrc_t rx, rsrc_t rw, u32 sox, u32 sow, int lw, int lane, const StageCtx &sc, u32x4 (&br)[36], rsrc_t rp, u32 wpo STAMP_ARG) {
  constexpr int NW = ATT ? 3 : 4, CX = ATT ? CX_ATT : CX_FFN, N0 = 8 + NW, N = N0 - CX;
  const u32 lb = (u32)(size_t)L;
  const u32 vox0 = (lane >> 3) * XROW + 16 * ((lane & 7) ^ ((lane >> 4) & 3)), vox1 = vox0 ^ 64;
  const u32 ldx = rfl(lb + lw * 8192);
  const u32 vow = ATT ? (lane >> 2) * QKVROW + ((lane & 3) == 3 ? 40 : 16 * (lane & 3)) : (lane >> 4) * W1ROW + 16 * ((lane & 15) ^ ((lane >> 4) << 2));
  const u32 swb = rfl(sow + 16 * lw * (ATT ? QKVROW : W1ROW)), ldw = rfl(lb + A_WOFF + lw * (ATT ? 1024 : 4096));
  auto issue = [&](auto C) {
    constexpr u32 c = C, so = (c % 3) * A_SLOT;
    sfor<0, NW>([&](auto I) {
      constexpr u32 i = I;
      if constexpr (ATT) dmai<so + i * 4096, 64 * c * QKVROW + (i == 0 ? 896 : i == 1 ? 1792 : 0)>(rw, vow, swb, ldw);
      else dmai<so + i * 1024, (64 * c + 4 * i) * W1ROW>(rw, vow, swb, ldw);
    });
    sfor<CX, 8>([&](auto I) { constexpr u32 i = I; dmai<so + i * 1024, 8 * i * XROW + 128 * c>(rx, (i & 1) ? vox1 : vox0, sox, ldx); });
  };
  sfor<0, 7>([&](auto C) {
    constexpr int c = C;
    if constexpr (ATT && c == 1) { if (lw < 2) vmwait<N + 1>(); else vmwait<N>(); }
    else vmwait<(c == 6 ? 12 : c == 0 ? N0 : N)>();
    asm volatile("s_barrier" ::: "memory");
    if constexpr (c == 0) STAMP(1);
    if constexpr (ATT && c == 0) {
      if (lw < 2) dmar(rp, lane * 16 + lw * 1024, wpo, lb + L_WPHI + lw * 1024);
    }
    if constexpr (c + 2 < 7) issue(std::integral_constant<int, c + 2>{});
    else { stage_load<9 + 3 * (c - 5)>(sc, br); stage_load<10 + 3 * (c - 5)>(sc, br); stage_load<11 + 3 * (c - 5)>(sc, br); }
  });
  STAMP(2);
}

DI void feature_map(const f32x16 &P, float rstd, int h, u32x4 (&pf)[2][2]) {
  const float c = rstd * LOG2E;
  float mx = fmaxf(fmaxf(P[0], P[1]), P[2]), mn = fminf(fminf(P[0], P[1]), P[2]);
  for (int i = 3; i < 15; i += 2) { mx = fmaxf(fmaxf(mx, P[i]), P[i + 1]); mn = fminf(fminf(mn, P[i]), P[i + 1]); }
  mx = fmaxf(mx, P[15]); mn = fminf(mn, P[15]);
  { f32x2 a = both_halves(mx), b = both_halves(mn); mx = fmaxf(a.x, a.y); mn = fminf(b.x, b.y); }
  const float mxc = -mx * c, mnc = mn * c;
  f32x16 ep, en;
  for (int i = 0; i < 16; i += 2) {
    const f32x2 p2 = {P[i], P[i + 1]}, ap = p2 * c + mxc, an = mnc - p2 * c;
    ep[i] = __builtin_amdgcn_exp2f(ap.x); ep[i + 1] = __builtin_amdgcn_exp2f(ap.y);
    en[i] = __builtin_amdgcn_exp2f(an.x); en[i + 1] = __builtin_amdgcn_exp2f(an.y);
  }
  f32x2 sp2 = {ep[0], ep[1]}, sn2 = {en[0], en[1]};
  for (int i = 2; i < 12; i += 2) { sp2 += f32x2{ep[i], ep[i + 1]}; sn2 += f32x2{en[i], en[i + 1]}; }
  const float keep = h ? 0.f : 1.f;
  sp2 += (f32x2{ep[12], ep[13]} + f32x2{ep[14], ep[15]}) * keep;
  sn2 += (f32x2{en[12], en[13]} + f32x2{en[14], en[15]}) * keep;
  const f32x2 a = both_halves(sp2.x + sp2.y), b = both_halves(sn2.x + sn2.y);
  const float rp = __builtin_amdgcn_rcpf(a.x + a.y), rn = __builtin_amdgcn_rcpf(b.x + b.y);
  for (int i = 0; i < 16; i += 2) {
    const f32x2 u = f32x2{ep[i], ep[i + 1]} * rp, v = f32x2{en[i], en[i + 1]} * rn;
    ep[i] = u.x; ep[i + 1] = u.y; en[i] = v.x; en[i + 1] = v.y;
  }
  pf[0][0] = pack8(ep, 0); pf[0][1] = pack8(ep, 1); pf[1][0] = pack8(en, 0); pf[1][1] = pack8(en, 1);
}

constexpr int refill_end(int k) { return k < 0 ? 0 : k == 0 ? 7 : k == 1 ? 14 : k == 2 ? 20 : k == 3 ? 26 : k == 4 ? 32 : 36; }
constexpr bool refill_ok() { for (int k = 0; k < 6; ++k) for (int p = refill_end(k - 1); p < refill_end(k); ++p) { const int g = 9 + p / 4, s = 4 + 2 * k; if (s < g - 8 || 4 * (s + 2) > 4 * g - 2) return false; } return true; }
static_assert(refill_ok(), "refill schedule");
}

__global__ void __launch_bounds__(512, 2) fused_hedgehog_ffn(const char *__restrict__ gx, const char *__restrict__ gwqkv, const char *__restrict__ gwphi, const char *__restrict__ gwo,
                                                              const char *__restrict__ gw1, const char *__restrict__ gb1, const char *__restrict__ gw2, const char *__restrict__ gb2,
                                                              char *__restrict__ gout, char *__restrict__ gws) {
  __shared__ __attribute__((aligned(1024))) char smem[LDS_BYTES];
  lchar *L = (lchar *)smem;
  unsigned long long pc0;
  asm volatile("s_getpc_b64 %0" : "=s"(pc0));
  asm volatile("" ::"s"(gx), "s"(gwqkv), "s"(gwphi), "s"(gwo), "s"(gw1), "s"(gb1), "s"(gw2), "s"(gb2), "s"(gout), "s"(gws));
  const int tid = threadIdx.x, lane = tid & 63, w = rfl(tid >> 6);
  const int b = blockIdx.x, xcd = b & 7;
  int slot = b >> 3;
  STAMP_DECL; STAMP(0);
  const rsrc_t rx = __builtin_amdgcn_make_buffer_rsrc((void *)gx, 0, 2048 * XROW, 0x00020000);
  if (__builtin_expect(w >= 4 && slot < 30, 1)) {
    const bool a = slot < 16;
    const u32 lw = w - 4, lb = (u32)(size_t)L, hi = slot >= 23;
    const u32 rg0 = (xcd & 6) + (a ? slot >> 3 : hi), sox = (256 * rg0 + 64 * lw) * XROW, ldx = lb + lw * 8192;
    const u32 vox = (lane >> 3) * XROW + 16 * ((lane & 7) ^ ((lane >> 4) & 3));
    const rsrc_t rw = __builtin_amdgcn_make_buffer_rsrc((void *)(a ? gwqkv : gw1), 0, a ? DM * QKVROW : DM * W1ROW, 0x00020000);
    const u32 vow = a ? (lane >> 2) * QKVROW + ((lane & 3) == 3 ? 40 : 16 * (lane & 3)) : (lane >> 4) * W1ROW + 16 * ((lane & 15) ^ ((lane >> 4) << 2));
    const u32 swb = a ? 56 * (8 * (xcd & 1) + (slot & 7)) + 16 * lw * QKVROW : 256 * (7 * (xcd & 1) + slot - 16 - 7 * hi) + 16 * lw * W1ROW;
    const u32 ldw = lb + A_WOFF + lw * (a ? 1024 : 4096), ls = a ? 4096 : 1024, cs = a ? 64 * QKVROW : 64 * W1ROW;
    sfor<0, 2>([&](auto C) {
      constexpr u32 c = C;
      dmar(rw, vow, swb + c * cs + (a ? 896 : 0), ldw + c * A_SLOT);
      dmar(rw, vow, swb + c * cs + (a ? 1792 : 4 * W1ROW), ldw + c * A_SLOT + ls);
      dmar(rw, vow, swb + c * cs + (a ? 0 : 8 * W1ROW), ldw + c * A_SLOT + 2 * ls);
      if (!a) dmar(rw, vow, swb + c * cs + 12 * W1ROW, ldw + c * A_SLOT + 3 * ls);
      sfor<0, 8>([&](auto I) { constexpr u32 i = I; dmai<c * A_SLOT + i * 1024, 8 * i * XROW + 128 * c>(rx, (i & 1) ? vox ^ 64 : vox, sox, ldx); });
    });
  }
  asm volatile("" : "+s"(slot));
  const int h = lane >> 5;
  const rsrc_t rws = __builtin_amdgcn_make_buffer_rsrc((void *)gws, 0, WS_END, 0x00020000);
  const int tc = slot >= 16 ? slot - 16 : slot + 16;
  const bool has_tile = tc < 28;
  const u32 rt = tc / 7, cc = tc % 7;
  const bool att = slot < 16, ffn = !att && slot < 30;
  const int head = 8 * (xcd & 1) + (slot & 7);
  const int jf = slot - 16, ct = 7 * (xcd & 1) + (jf >= 7 ? jf - 7 : jf);
  const int rga = 2 * (xcd >> 1) + (att ? (slot >> 3) : (jf >= 7));

  if (w >= 4) {
    const int lw = w - 4;
    StageCtx sc;
    sc.rw2 = __builtin_amdgcn_make_buffer_rsrc((void *)gw2, 0, DF * XROW, 0x00020000);
    sc.rwo = __builtin_amdgcn_make_buffer_rsrc((void *)gwo, 0, DM * XROW, 0x00020000);
    sc.vob = (lane >> 3) * XROW + 16 * ((lane & 7) ^ (((lane >> 4) & 1) << 2));
    sc.so2 = rfl((896 * (lw >> 1) + 32 * (lw & 1)) * XROW + 128 * cc);
    sc.soo = rfl(28 * (8 * (lw >> 1) + (lw & 1)) * XROW + 128 * cc);
    sc.ld = rfl((u32)(size_t)L + (lw >> 1) * 8192 + (lw & 1) * 4096);
    u32x4 br[36];
    auto resid_dma = [&]() {
      const u32 vo = (lane >> 3) * XROW + 16 * ((lane & 7) ^ ((lane >> 4) & 3)), so = rfl((256 * xcd + 64 * rt + 16 * lw) * XROW + 128 * cc), ld = rfl((u32)(size_t)L + L_XT + lw * 2048);
      dmai<0, 0>(rx, vo, so, ld);
      dmai<1024, 8 * XROW>(rx, vo ^ 64, so, ld);
    };
    if (att) {
      const rsrc_t rw = __builtin_amdgcn_make_buffer_rsrc((void *)gwqkv, 0, DM * QKVROW, 0x00020000);
      gemmA_loader<true>(L, rx, rw, rfl((256 * rga + 64 * lw) * XROW), rfl(56 * head), lw, lane, sc, br, __builtin_amdgcn_make_buffer_rsrc((void *)gwphi, 0, 16 * 28 * 28 * 2, 0x00020000), rfl(head * 1568) STAMP_PASS);
      BAR();
      const u32 E = rfl(*(LAS u32 *)(L + L_E));
      if (has_tile) { sfor<0, 5>([&](auto S) { stage_dma<S>(sc); }); sfor<5, 7>([&](auto S) { stage_dma<S>(sc); }); resid_dma(); }
      sfor<15, 18>([&](auto S) { stage_load<S>(sc, br); });
      if (has_tile) vmwait<22>();
      BAR();
      __builtin_amdgcn_s_setprio(XPRIO);
      STAMP(4);
      for (int t = 0; t < 2; ++t) {
        lchar *sp = (lchar *)(size_t)(sc.ld + (2 + t) * L_SLOT + lane * 16);
        f32x4 s = *(LAS f32x4 *)sp;
        for (int ww = 1; ww < 4; ++ww) s += *(LAS f32x4 *)(sp + ww * 1024);
        st128_sc1(u32x4{pk2(s.x, s.y), E, pk2(s.z, s.w), E}, rws, lane * 16, WS_KVP + (head * 8 + rga) * 8192 + (t * 4 + lw) * 1024);
      }
      STAMP(5);
      poll_all(gws, WS_KVP + (head * 8 + (lane & 7)) * 8192 + (4 + lw) * 1024 + 63 * 16 + 12, E);
      STAMP(7);
      f32x2 ks[2][2];
#pragma unroll 1
      for (int spin = 0; spin < SPIN_CAP; ++spin) {
        u32x4 part[2][8];
        for (int t = 0; t < 2; ++t) for (int r = 0; r < 8; ++r) part[t][r] = ld128_sc1(rws, lane * 16, WS_KVP + (head * 8 + r) * 8192 + (t * 4 + lw) * 1024);
        u32 ta = E, to = E;
        for (int t = 0; t < 2; ++t) {
          ks[t][0] = f32x2{0.f, 0.f}; ks[t][1] = f32x2{0.f, 0.f};
          for (int r = 0; r < 8; ++r) {
            ks[t][0] += f32x2{bf2f(part[t][r][0]), bfhi(part[t][r][0])}; ks[t][1] += f32x2{bf2f(part[t][r][2]), bfhi(part[t][r][2])};
            ta &= part[t][r][1] & part[t][r][3]; to |= part[t][r][1] | part[t][r][3];
          }
        }
        if (__all(ta == E && to == E)) break;
        __builtin_amdgcn_s_sleep(POLL_SLEEP);
      }
      for (int t = 0; t < 2; ++t) {
        u32x2 pk = {pk2(ks[t][0].x, ks[t][0].y), pk2(ks[t][1].x, ks[t][1].y)};
        if (lw == 3 && h) pk = u32x2{0, 0};
        *(LAS u32x2 *)(L + L_KVF + ((t * 2 + (lw >> 1)) * 64 + lane) * 16 + (lw & 1) * 8) = pk;
      }
      __builtin_amdgcn_s_setprio(0);
      STAMP(8);
      if (!has_tile) {
        BAR();
        if (xcd == 0 && slot == 12 && lw == 0) {
#pragma unroll 1
          for (int spin = 0; spin < SPIN_CAP; ++spin) {
            bool ok = true;
            for (int i = 0; i < 32; ++i) ok &= ld_flag(gws, WS_STAT + (64 * i + lane) * 16 + 4) == E;
            if (__all(ok)) break;
            __builtin_amdgcn_s_sleep(8);
          }
          if (lane == 0) st_flag(gws, WS_EPOCH, E);
        }
        STAMP_OUT();
        return;
      }
    } else if (ffn) {
      const rsrc_t rw = __builtin_amdgcn_make_buffer_rsrc((void *)gw1, 0, DM * W1ROW, 0x00020000);
      gemmA_loader<false>(L, rx, rw, rfl((256 * rga + 64 * lw) * XROW), rfl(256 * ct), lw, lane, sc, br, rx, 0 STAMP_PASS);
      BAR();
      if (HOLD == 1 || HOLD == 3) asm volatile("s_barrier" ::: "memory");
      sfor<0, 5>([&](auto S) { stage_dma<S>(sc); });
      vmwait<0>();
      BAR();
      sfor<5, 9>([&](auto S) { stage_dma<S>(sc); });
      resid_dma();
      sfor<15, 18>([&](auto S) { stage_load<S>(sc, br); });
    } else {
      warm_up(pc0, rws, gw2, gwo, xcd, (slot - 30) * 8 + w, lane, (u32)(size_t)L);
      sfor<0, 9>([&](auto S) { stage_dma<S>(sc); });
      resid_dma();
      sfor<9, 18>([&](auto S) { stage_load<S>(sc, br); });
      vmwait<36>();
      BAR();
    }
    STAMP(10);
    const u32 ldv = sc.ld + lane * 16;
    sfor<0, 7>([&](auto K) {
      constexpr int k = K;
      if constexpr (k == 0) vmwait<12>();
      if constexpr (k == 1) vmwait<0>();
      BAR();
      if constexpr (k == 0) { if (att) sfor<7, 9>([&](auto S) { stage_dma<S>(sc); }); }
      sfor<refill_end(k - 1), refill_end(k)>([&](auto Pc) {
        constexpr int pc = Pc;
        *(LAS u32x4 *)((lchar *)(size_t)ldv + ((9 + pc / 4 + 4) % NSLOT) * L_SLOT + (pc & 3) * 1024) = br[pc];
        if (SPREAD) asm volatile("s_nop %0" ::"i"(SPREAD ? SPREAD - 1 : 0) : "memory");
      });
    });
    STAMP(12);
    asm volatile("s_barrier" ::: "memory");
    STAMP_OUT();
    return;
  }

  const Early early = early_issue(gws, rx, 8 * b + 2 * w, lane);
  u32 E;
  auto touches = [&]() {
    if (att || ffn) {
      constexpr u32 OOB = 0x7ffffff0u;
      const u32 lb = (u32)(size_t)L;
      { const u32 g15 = att ? (slot & 7) : 8 + (jf >= 7 ? jf - 7 : jf), i = (g15 * 4 + w) * 22 + lane;
        touch(rx, (lane < 22 && i < 1280) ? (256 * rga + (i & 255)) * XROW + 128 * (2 + (i >> 8)) : OOB, lb); }
      if (att) {
        const rsrc_t rq = __builtin_amdgcn_make_buffer_rsrc((void *)gwqkv, 0, DM * QKVROW, 0x00020000);
        const u32 i = slot * 240 + w * 60 + lane, row = (i * 43691u) >> 19, rem = i - 12 * row;
        touch(rq, lane < 60 ? (128 + row) * QKVROW + 896 * (rem >> 2) + ((448 * (xcd & 1)) & ~127u) + 128 * (rem & 3) : OOB, lb);
      } else {
        const rsrc_t r1 = __builtin_amdgcn_make_buffer_rsrc((void *)gw1, 0, DM * W1ROW, 0x00020000);
        const u32 l8 = 4 * (jf >= 7) + w, i = 64 * l8 + lane, j = 512 + 16 * l8 + lane;
        touch(r1, (128 + (i >> 1)) * W1ROW + 256 * ct + 128 * (i & 1), lb);
        touch(r1, lane < 16 ? (128 + (j >> 1)) * W1ROW + 256 * ct + 128 * (j & 1) : OOB, lb);
      }
    }
  };
  if (TOUCH_A == 2) touches();
  auto first = [&]() {
    E = early_finish(early, rws, 8 * b + 2 * w, lane); if (tid == 0) *(LAS u32 *)(L + L_E) = E;
    if (TOUCH_A == 1) touches();
  };
  const int rh = w & 1, kg = w >> 1;
  const u32 rb = 8 * xcd + 2 * rt + rh;
  const u32 row = 32 * rb + (lane & 31), col0 = 64 * cc + 32 * kg;
  u32x4 pq[2][2][2];
  const u32 hfa = WS_HFA + (rb * 14 + 7 * kg + ((lane & 7) < 7 ? (lane & 7) : 0)) * 64;
  u32 hlook = 0;
  auto o_stage = [&]() {
    u32x4 kf[2][2];
    for (int t = 0; t < 2; ++t) for (int s = 0; s < 2; ++s) kf[t][s] = *(LAS u32x4 *)(L + L_KVF + ((t * 2 + s) * 64 + lane) * 16);
    f32x16 num[2] = {zero16(), zero16()};
    for (int t = 0; t < 2; ++t) for (int s = 0; s < 2; ++s) for (int r = 0; r < 2; ++r) num[r] = mfma(kf[t][s], pq[r][t][s], num[r]);
    for (int r = 0; r < 2; ++r) {
      const float rc = __builtin_amdgcn_rcpf(both_halves(num[r][12]).x + 1e-6f);
      for (int i = 0; i < 16; i += 2) { const f32x2 v = f32x2{num[r][i], num[r][i + 1]} * rc; num[r][i] = v.x; num[r][i + 1] = v.y; }
      if (!h) { num[r][12] = 0.f; num[r][13] = 0.f; num[r][14] = 0.f; num[r][15] = 0.f; }
      const u32 so = WS_O + ((8 * rga + 2 * w + r) * 32 + 2 * head) * 1024;
      st128_sc1(pack8(num[r], 0), rws, lane * 16, so);
      st128_sc1(pack8(num[r], 1), rws, lane * 16, so + 1024);
    }
  };
  if (att) {
    if (tid < 16) *(LAS u32 *)(L + L_ZERO + 4 * tid) = 0;
    u32x4 idf[2];
    for (int s = 0; s < 2; ++s)
      for (int d = 0; d < 4; ++d) {
        const int k0 = 16 * s + 8 * (d >> 1) + 4 * h + 2 * (d & 1), n = lane & 31;
        idf[s][d] = (n == k0 ? 0x3F80u : 0u) | (n == k0 + 1 ? 0x3F800000u : 0u);
      }
    for (int s = 0; s < 2; ++s) asm volatile("" : "+v"(idf[s]));
    f32x16 acc[2][3]; u32x4 stat[2];
    const u32 vostat = WS_STAT + (256 * rga + 64 * w + (lane & 31)) * 16;
    gemmA_compute<true>(L, rx, rfl((256 * rga + 64 * w) * XROW), w, lane, acc, first, [&]() { if (TOUCH_A == 3) touches(); }, [&]() { for (int r = 0; r < 2; ++r) stat[r] = ld128_sc1(rws, vostat + 512 * r, 0); } STAMP_PASS);
    RowStat st[2];
    for (int r = 0; r < 2; ++r) {
      st[r] = row_stats(stat_settle(stat[r], rws, vostat + 512 * r, E), h);
      *(LAS float *)(L + L_TOK + w * 256 + r * 128 + 4 * (lane & 31)) = st[r].rstd;
    }
    BAR();
    STAMP(3);
    {
      const u32x4 wk = waug(cs_read(L, 0, lane), 0), wv = waug(cs_read(L, 1, lane), 0), wq = waug(cs_read(L, 2, lane), 0);
      for (int r = 0; r < 2; ++r) { acc[r][0] = mfma(wk, st[r].xaug, acc[r][0]); acc[r][1] = mfma(st[r].xaug, wv, acc[r][1]); acc[r][2] = mfma(wq, st[r].xaug, acc[r][2]); }
    }
    u32x4 wp[2];
    {
      const int blk = (lane >> 4) & 1, q = (lane & 15) >> 2, p = lane & 3;
      const u32 col = (blk && p == 3) ? 24 : 16 * blk + 4 * p;
      for (int s = 0; s < 2; ++s) {
        u32x2 r[2];
        for (int t = 0; t < 2; ++t) {
          const int dp = 16 * s + 8 * t + 4 * h + q;
          const u32 a = (dp >= 24 && dp < 28) ? L_ZERO + 8 * p : L_WPHI + (dp >= 28 ? dp - 4 : dp) * 56 + col * 2;
          r[t] = trd(L + a);
        }
        wp[s] = u32x4{r[0].x, r[0].y, r[1].x, r[1].y};
      }
    }
    f32x16 kvp[2] = {zero16(), zero16()};
    {
      f32x16 P[2];
      for (int r = 0; r < 2; ++r) P[r] = mfma(wp[0], pack8(acc[r][0], 0), zero16());
      for (int r = 0; r < 2; ++r) P[r] = mfma(wp[1], pack8(acc[r][0], 1), P[r]);
      u32x4 vf[2][2];
      for (int r = 0; r < 2; ++r) {
        f32x16 v = acc[r][1];
        for (int g = 0; g < 4; ++g) {
          const f32x4 rs = *(LAS f32x4 *)(L + L_TOK + w * 256 + r * 128 + (8 * g + 4 * h) * 4);
          for (int e = 0; e < 4; e += 2) { const f32x2 t2 = f32x2{v[4 * g + e], v[4 * g + e + 1]} * f32x2{rs[e], rs[e + 1]}; v[4 * g + e] = t2.x; v[4 * g + e + 1] = t2.y; }
        }
        for (int s = 0; s < 2; ++s) { vf[r][s] = pack8(v, s); if ((lane & 31) == 24) vf[r][s] = u32x4{0x3F803F80u, 0x3F803F80u, 0x3F803F80u, 0x3F803F80u}; }
      }
      u32x4 pf[2][2][2];
      for (int r = 0; r < 2; ++r) feature_map(P[r], st[r].rstd, h, pf[r]);
      f32x16 Z[2][2];
      for (int r = 0; r < 2; ++r) for (int t = 0; t < 2; ++t) Z[r][t] = mfma(pf[r][t][0], idf[0], zero16());
      for (int r = 0; r < 2; ++r) for (int t = 0; t < 2; ++t) Z[r][t] = mfma(pf[r][t][1], idf[1], Z[r][t]);
      for (int r = 0; r < 2; ++r) for (int s = 0; s < 2; ++s) for (int t = 0; t < 2; ++t) kvp[t] = mfma(pack8(Z[r][t], s), vf[r][s], kvp[t]);
    }
    for (int t = 0; t < 2; ++t)
      for (int g = 0; g < 4; ++g)
        *(LAS f32x4 *)(L + (2 + t) * L_SLOT + (g >> 1) * 8192 + (g & 1) * 4096 + w * 1024 + lane * 16) = f32x4{kvp[t][4 * g], kvp[t][4 * g + 1], kvp[t][4 * g + 2], kvp[t][4 * g + 3]};
    STAMP(4);
    BAR();
    {
      f32x16 P[2];
      for (int r = 0; r < 2; ++r) P[r] = mfma(wp[0], pack8(acc[r][2], 0), zero16());
      for (int r = 0; r < 2; ++r) P[r] = mfma(wp[1], pack8(acc[r][2], 1), P[r]);
      for (int r = 0; r < 2; ++r) {
        feature_map(P[r], st[r].rstd, h, pq[r]);
        for (int t = 0; t < 2; ++t) for (int s = 0; s < 2; ++s) asm volatile("" : "+v"(pq[r][t][s]));
        if (r == 0 && has_tile) hlook = ld_flag(gws, hfa);
        __builtin_amdgcn_sched_barrier(0);
      }
    }
    STAMP(6);
    if (!has_tile) {
      BAR();
      o_stage();
      vmwait<0>();
      if (lane < 2) st_flag(gws, WS_OF + ((8 * rga + 2 * w + lane) * 16 + head) * 64, E);
      STAMP_OUT();
      return;
    }
  } else if (ffn) {
    u32 bias[4];
    for (int t = 0; t < 4; ++t) bias[t] = *(const GAS unsigned short *)(gb1 + (128 * ct + 32 * t + (lane & 31)) * 2);
    f32x16 acc[2][4]; u32x4 stat[2];
    const u32 vostat = WS_STAT + (256 * rga + 64 * w + (lane & 31)) * 16;
    gemmA_compute<false>(L, rx, rfl((256 * rga + 64 * w) * XROW), w, lane, acc, first, [&]() { if (TOUCH_A == 3) touches(); }, [&]() { for (int r = 0; r < 2; ++r) stat[r] = ld128_sc1(rws, vostat + 512 * r, 0); } STAMP_PASS);
    RowStat st[2];
    for (int r = 0; r < 2; ++r) st[r] = row_stats(stat_settle(stat[r], rws, vostat + 512 * r, E), h);
    BAR();
    STAMP(3);
    if (HOLD == 2) __builtin_amdgcn_s_setprio(3);
    for (int t = 0; t < 4; ++t) {
      const u32x4 wa = waug(cs_read(L, t, lane), bias[t] | (bias[t] << 16));
      for (int r = 0; r < 2; ++r) {
        f32x16 a = mfma(wa, st[r].xaug, acc[r][t]);
        for (int i = 0; i < 16; i += 2) { f32x2 v = {relu(a[i]), relu(a[i + 1])}; v *= v; a[i] = v.x; a[i + 1] = v.y; }
        const u32 so = WS_H + ((8 * rga + 2 * w + r) * 112 + 8 * ct + 2 * t) * 1024;
        st128_sc1(pack8(a, 0), rws, lane * 16, so);
        st128_sc1(pack8(a, 1), rws, lane * 16, so + 1024);
      }
      if (HOLD == 3 && t == 1) asm volatile("s_barrier" ::: "memory");
      if (t == 2) {
        vmwait<4>();
        if (lane < 2) st_flag(gws, WS_HFA + ((8 * rga + 2 * w + lane) * 14 + ct) * 64, E);
      }
    }
    if (HOLD == 1) asm volatile("s_barrier" ::: "memory");
    if (HOLD == 2) __builtin_amdgcn_s_setprio(0);
    STAMP(4);
    vmwait<0>();
    if (lane < 2) st_flag(gws, WS_HFB + ((8 * rga + 2 * w + lane) * 14 + ct) * 64, E);
    STAMP(5);
    for (int t = 0; t < 2; ++t) for (int r = 0; r < 2; ++r) for (int s = 0; s < 2; ++s) pq[t][r][s] = u32x4{0, 0, 0, 0};
  } else {
    warm_up(pc0, rws, gw2, gwo, xcd, (slot - 30) * 8 + w, lane, (u32)(size_t)L);
    first();
    for (int t = 0; t < 2; ++t) for (int r = 0; r < 2; ++r) for (int s = 0; s < 2; ++s) pq[t][r][s] = u32x4{0, 0, 0, 0};
  }

  {
    constexpr int RD = RING;
    const u32 soh = WS_H + (rb * 112 + 56 * kg) * 1024, soo = WS_O + (rb * 32 + 16 * kg) * 1024;
    u32x4 ring[RD];
    u32x2 b2v[4]; u32x4 sg0;
    const int blk = (lane >> 4) & 1, q = (lane & 15) >> 2, p = lane & 3;
    u32 bb0[2], bb[2];
    for (int c2 = 0; c2 < 2; ++c2) bb0[c2] = (u32)(size_t)L + kg * 8192 + (4 * h + q) * 128 + ((c2 ^ kg ^ (q >> 1)) * 64) + 32 * blk + 8 * p;
    f32x16 af[2] = {zero16(), zero16()}, aa[2] = {zero16(), zero16()};
    constexpr int PD = BPD;
    u32x4 bf[PD + 1][2];
    const u32 hfb = WS_HFB + (rb * 14 + 7 * kg + ((lane & 7) < 7 ? (lane & 7) : 0)) * 64, ofo = WS_OF + (rb * 16 + 8 * kg + (lane & 7)) * 64;
    u32 look = 0, soe = 0;
    bool o_early = false;
    float rs2; f32x2 bx[8], sg[8], xx[8];
    u32x4 oa[16];
    if (!__all(hlook == E)) poll_all(gws, hfa, E);
    STAMP(10);
    if (!att) BAR();
    sfor<0, 72>([&](auto I) {
      constexpr int i = I;
      if constexpr (i == 56) {
        for (int g = 0; g < 4; ++g)
          *(LAS f32x4 *)(L + 6 * L_SLOT + w * 4096 + g * 1024 + lane * 16) = f32x4{af[1][4 * g], af[1][4 * g + 1], af[1][4 * g + 2], af[1][4 * g + 3]};
        BAR();
        for (int g = 0; g < 4; ++g) {
          const f32x4 pf = *(LAS f32x4 *)(L + 6 * L_SLOT + (w ^ 2) * 4096 + g * 1024 + lane * 16);
          for (int e = 0; e < 2; ++e) {
            const int j = 4 * g + 2 * e;
            const f32x2 y = (f32x2{af[0][j], af[0][j + 1]} + f32x2{pf[2 * e], pf[2 * e + 1]}) * rs2 + bx[2 * g + e];
            const f32x2 t = y * -LOG2E;
            const f32x2 d = f32x2{__builtin_amdgcn_exp2f(t.x), __builtin_amdgcn_exp2f(t.y)} + 1.0f;
            sg[2 * g + e] = y * f32x2{__builtin_amdgcn_rcpf(d.x), __builtin_amdgcn_rcpf(d.y)};
            asm volatile("" : "+v"(sg[2 * g + e]));
          }
        }
        STAMP(14);
        if (!o_early) {
          if (!__all(look == E)) poll_all(gws, ofo, E);
          for (int j = 0; j < OB; ++j) oa[j] = ld128_sc1(rws, lane * 16, soo + j * 1024);
        }
        __builtin_amdgcn_sched_barrier(0);
      }
      if constexpr (i >= 16 && (i & 7) == 0 && i != 56) asm volatile("s_barrier" ::: "memory");
      if constexpr (i == 0) STAMP(11);
      if constexpr (i == 0) {
        for (int j = 0; j < RD; ++j) ring[j] = ld128_sc1(rws, lane * 16, soh + h_step(j) * 1024);
        __builtin_amdgcn_sched_barrier(0);
        for (int c2 = 0; c2 < 2; ++c2) { bb[c2] = bb0[c2] + 4 * L_SLOT; asm volatile("" : "+v"(bb[c2])); for (int j = 0; j < PD; ++j) bf[j][c2] = trfrag((lchar *)(size_t)bb[c2] + j * 2048, 1024); }
      }
      static_assert(28 - RD - 6 < 16, "no look between the o stores and their counted wait");
      if constexpr (i == 28 - RD - 6) look = ld_flag(gws, hfb);
      if constexpr (i == 28 - RD) { if (!__all(look == E)) poll_all(gws, hfb, E); }
      if constexpr (i == 44) {
        for (int g = 0; g < 4; ++g) b2v[g] = *(const GAS u32x2 *)(gb2 + (col0 + 8 * g + 4 * h) * 2);
        sg0 = ld128_sc1(rws, WS_STAT + row * 16, 0);
      }
      if constexpr (i == 40) look = ld_flag(gws, ofo);
      if constexpr (i == 47) { o_early = __all(look == E); soe = o_early ? soo : WS_END; }
      if constexpr (i >= 48 && i < 48 + OB) oa[i - 48] = ld128_sc1(rws, lane * 16, soe + (i - 48) * 1024);
      if constexpr (i == 50) look = ld_flag(gws, ofo);
      if constexpr (i == 52) {
        u32x4 g4 = sg0;
        if (!__all(g4[1] == E && g4[3] == E)) g4 = stat_settle(g4, rws, WS_STAT + row * 16, E);
        const float rs1 = __builtin_amdgcn_rcpf(asf(g4[2]));
        rs2 = rs1 * rs1;
        for (int g = 0; g < 4; ++g) for (int e = 0; e < 2; ++e) { bx[2 * g + e] = f32x2{bf2f(b2v[g][e]), bfhi(b2v[g][e])}; asm volatile("" : "+v"(bx[2 * g + e])); }
      }
      if constexpr (i == 60) {
        for (int g = 0; g < 4; ++g) {
          const u32 r64 = 32 * rh + (lane & 31);
          const u32x2 xr = *(LAS u32x2 *)(L + L_XT + r64 * 128 + 16 * ((4 * kg + g) ^ ((r64 >> 1) & 7)) + 8 * h);
          for (int e = 0; e < 2; ++e) { xx[2 * g + e] = f32x2{bf2f(xr[e]), bfhi(xr[e])}; asm volatile("" : "+v"(xx[2 * g + e])); }
        }
      }
      if constexpr (i == 16) { if (att) o_stage(); }
      if constexpr (i == 16 + OFD) {
        if (att) { vmwait<OFD>(); if (lane < 2) st_flag(gws, WS_OF + ((8 * rga + 2 * w + lane) * 16 + head) * 64, E); }
      }
      u32x4 a;
      if constexpr (i < 56) a = ring[i % RD]; else a = oa[i - 56];
      if constexpr (i + PD < 72) {
        constexpr int n = i + PD;
        if constexpr ((n & 3) == 0) for (int c2 = 0; c2 < 2; ++c2) { bb[c2] = bb0[c2] + (((n >> 2) + 4) % NSLOT) * L_SLOT; asm volatile("" : "+v"(bb[c2])); }
        for (int c2 = 0; c2 < 2; ++c2) bf[n % (PD + 1)][c2] = trfrag((lchar *)(size_t)bb[c2] + (n & 3) * 2048, 1024);
      }
      if constexpr (i < 56) { af[0] = mfma(bf[i % (PD + 1)][0], a, af[0]); af[1] = mfma(bf[i % (PD + 1)][1], a, af[1]); asm volatile("" : "+v"(af[0]), "+v"(af[1])); }
      else { aa[0] = mfma(bf[i % (PD + 1)][0], a, aa[0]); aa[1] = mfma(bf[i % (PD + 1)][1], a, aa[1]); asm volatile("" : "+v"(aa[0]), "+v"(aa[1])); }
      if constexpr (i + RD < 56) ring[i % RD] = ld128_sc1(rws, lane * 16, soh + h_step(i + RD) * 1024);
      if constexpr (i >= 56 && i - 56 + OB < 16) oa[i - 56 + OB] = ld128_sc1(rws, lane * 16, soo + (i - 56 + OB) * 1024);
      __builtin_amdgcn_sched_barrier(0);
    });
    STAMP(12);
    for (int g = 0; g < 4; ++g)
      *(LAS f32x4 *)(L + 8 * L_SLOT + w * 4096 + g * 1024 + lane * 16) = f32x4{aa[1][4 * g], aa[1][4 * g + 1], aa[1][4 * g + 2], aa[1][4 * g + 3]};
    BAR();
    u32x2 o2[4];
    for (int g = 0; g < 4; ++g) {
      const f32x4 pa = *(LAS f32x4 *)(L + 8 * L_SLOT + (w ^ 2) * 4096 + g * 1024 + lane * 16);
      for (int e = 0; e < 2; ++e) {
        const int i = 4 * g + 2 * e;
        const f32x2 r = (f32x2{aa[0][i], aa[0][i + 1]} + f32x2{pa[2 * e], pa[2 * e + 1]}) * sg[2 * g + e] + xx[2 * g + e];
        o2[g][e] = pk2(r.x, r.y);
      }
    }
    const rsrc_t rout = __builtin_amdgcn_make_buffer_rsrc((void *)gout, 0, 2048 * XROW, 0x00020000);
    for (int g = 0; g < 4; g += 2) {
      auto rx0 = __builtin_amdgcn_permlane32_swap(o2[g].x, o2[g + 1].x, false, false);
      auto ry0 = __builtin_amdgcn_permlane32_swap(o2[g].y, o2[g + 1].y, false, false);
      __builtin_amdgcn_raw_buffer_store_b128(u32x4{rx0[0], ry0[0], rx0[1], ry0[1]}, rout, row * XROW + (col0 + 8 * (g + h)) * 2, 0, OUT_AUX);
    }
    STAMP(13);
    STAMP(15);
    STAMP_OUT();
  }
}

extern "C" void kernel_launch(void *const *d_in, const int *in_sizes, int n_in, void *d_out, int out_size, void *d_ws, size_t ws_size, hipStream_t stream) {
  fused_hedgehog_ffn<<<256, 512, 0, stream>>>((const char *)d_in[0], (const char *)d_in[1], (const char *)d_in[2], (const char *)d_in[3], (const char *)d_in[5],
                                              (const char *)d_in[6], (const char *)d_in[7], (const char *)d_in[8], (char *)d_out, (char *)d_ws);
}
```

```cpp
#include <hip/hip_runtime.h>
#include <hip/hip_bf16.h>
#include <stdint.h>
#include <type_traits>

typedef unsigned u32;
typedef u32 u32x4 __attribute__((ext_vector_type(4)));
typedef u32 u32x2 __attribute__((ext_vector_type(2)));
typedef float f32x16 __attribute__((ext_vector_type(16)));
typedef float f32x4 __attribute__((ext_vector_type(4)));
typedef float f32x2 __attribute__((ext_vector_type(2)));
typedef short s16x8 __attribute__((ext_vector_type(8)));
typedef short s16x4 __attribute__((ext_vector_type(4)));
typedef __bf16 bf16x2 __attribute__((ext_vector_type(2)));
typedef __bf16 bf16x8 __attribute__((ext_vector_type(8)));
#define LAS __attribute__((address_space(3)))
#define GAS __attribute__((address_space(1)))
#define DI __device__ __forceinline__
typedef __amdgpu_buffer_rsrc_t rsrc_t;
typedef LAS char lchar;

namespace {
constexpr int DM = 448, DF = 1792;
constexpr u32 XROW = DM * 2;
constexpr u32 QKVROW = 3 * DM * 2;
constexpr u32 W1ROW = DF * 2;
constexpr u32 WS_EPOCH = 0;
constexpr u32 WS_STAT = 4096;
constexpr u32 WS_KF = 65536;
constexpr u32 WS_HFA = 98304;
constexpr u32 WS_HFB = WS_HFA + 57344;
constexpr u32 WS_OF = WS_HFB + 57344;
constexpr u32 WS_KVP = 294912;
constexpr u32 WS_H = WS_KVP + (1u << 20);
constexpr u32 WS_O = WS_H + 64 * 112 * 1024;
constexpr u32 WS_END = WS_O + 64 * 32 * 1024;
constexpr u32 L_SLOT = 16384, NSLOT = 9;
constexpr u32 A_SLOT = 49152, A_WOFF = 32768;
constexpr u32 L_MISC = NSLOT * L_SLOT;
constexpr u32 L_WPHI = L_MISC;
constexpr u32 L_ZERO = L_MISC + 2048;
constexpr u32 L_E = L_MISC + 2112;
constexpr u32 L_BRDY = L_MISC + 2128;
constexpr u32 L_DUMMY = L_MISC + 2304;
constexpr u32 L_CSP = L_MISC + 2560;
constexpr u32 L_TOK = L_CSP + 512;
constexpr u32 L_KVF = L_TOK + 1024;
constexpr u32 L_XT = L_KVF + 4096;
constexpr u32 L_END = L_XT + 8192;
constexpr u32 LDS_BYTES = 163840;
static_assert(L_END <= LDS_BYTES, "lds");
constexpr int SPIN_CAP = 1 << 16;
constexpr float LOG2E = 1.4426950408889634f;
#define CS_DOT2 0
#define SGB 1
#define LPRIO 0
#define FIRST_AT -1
#define TOUCH_A 0
#define POLL_SLEEP 3
#define HOLD 1
#define XPRIO 3
#define WARM 7
#define BPD 3
#define OB 8
#define FFN_ALL 0
#define OFD 10
#define OUT_AUX 0
#define SPREAD 0
#define PIN_C 0
#define RING 10

#define STAMP(i) do {} while (0)
#define STAMP_DECL do {} while (0)
#define STAMP_OUT() do {} while (0)
#define STAMP_ARG
#define STAMP_PASS
template <int B, int E, class F> DI void sfor(F &&f) {
  if constexpr (B < E) { f(std::integral_constant<int, B>{}); sfor<B + 1, E>(f); }
}
DI u32 rfl(u32 v) { return __builtin_amdgcn_readfirstlane(v); }
DI f32x16 mfma(u32x4 a, u32x4 b, f32x16 c) {
  return __builtin_amdgcn_mfma_f32_32x32x16_bf16(__builtin_bit_cast(bf16x8, a), __builtin_bit_cast(bf16x8, b), c, 0, 0, 0);
}
DI u32 pk2(float lo, float hi) { f32x2 p = {lo, hi}; return __builtin_bit_cast(u32, __builtin_convertvector(p, bf16x2)); }
DI float dot2(u32 a, u32 b, float c) { return __builtin_amdgcn_fdot2_f32_bf16(__builtin_bit_cast(bf16x2, a), __builtin_bit_cast(bf16x2, b), c, false); }
DI float bf2f(u32 lo16) { return __builtin_bit_cast(float, lo16 << 16); }
DI float bfhi(u32 v) { return __builtin_bit_cast(float, v & 0xffff0000u); }
DI float asf(u32 v) { return __builtin_bit_cast(float, v); }
DI f32x2 both_halves(float v) {
  const u32 a = __builtin_bit_cast(u32, v);
  auto r = __builtin_amdgcn_permlane32_swap(a, a, false, false);
  return f32x2{asf(r[0]), asf(r[1])};
}
DI float half_lo(float v) { return both_halves(v).x; }
DI u32x4 pack8(const f32x16 &x, int s) { return u32x4{pk2(x[8 * s], x[8 * s + 1]), pk2(x[8 * s + 2], x[8 * s + 3]), pk2(x[8 * s + 4], x[8 * s + 5]), pk2(x[8 * s + 6], x[8 * s + 7])}; }
DI u32x2 trd(lchar *p) {
  typedef short v4i16 __attribute__((ext_vector_type(4)));
  return __builtin_bit_cast(u32x2, __builtin_amdgcn_ds_read_tr16_b64_v4i16((LAS v4i16 *)p));
}
DI u32x4 trfrag(lchar *p, int tstride) { u32x2 a = trd(p), b = trd(p + tstride); return u32x4{a.x, a.y, b.x, b.y}; }
DI float relu(float v) { return __builtin_amdgcn_fmed3f(v, 0.f, __builtin_inff()); }
DI f32x16 zero16() { f32x16 z; for (int i = 0; i < 16; ++i) z[i] = 0.f; return z; }
#define BAR() asm volatile("s_waitcnt lgkmcnt(0)\n\ts_barrier" ::: "memory")
template <int N> DI void vmwait() { asm volatile("s_waitcnt vmcnt(%0)" ::"i"(N) : "memory"); }
DI void dma16(rsrc_t r, u32 voff, u32 soff, u32 ldsaddr) {
  asm volatile("s_mov_b32 m0, %0\n\ts_nop 0\n\tbuffer_load_dwordx4 %1, %2, %3 offen lds" ::"s"(ldsaddr), "v"(voff), "s"(r), "s"(soff) : "memory", "m0");
}
template <int OFF> DI void ald128(u32x4 &d, rsrc_t r, u32 voff, u32 soff) {
  asm volatile("buffer_load_dwordx4 %0, %1, %2, %3 offen offset:%4" : "=v"(d) : "v"(voff), "s"(r), "s"(soff), "i"(OFF) : "memory");
}
DI u32x4 ld128_sc1(rsrc_t r, u32 voff, u32 soff) { return __builtin_amdgcn_raw_buffer_load_b128(r, voff, soff, 16); }
DI void st128_sc1(u32x4 v, rsrc_t r, u32 voff, u32 soff) {
  __builtin_amdgcn_raw_buffer_store_b128(v, r, voff, soff, 16);
  asm volatile("s_nop 1" ::"v"(v) : "memory");
}
typedef GAS u32 gu32;
DI u32 ld_flag(const char *ws, u32 off) { return __hip_atomic_load((gu32 *)(ws + off), __ATOMIC_RELAXED, __HIP_MEMORY_SCOPE_AGENT); }
DI void st_flag(char *ws, u32 off, u32 v) { __hip_atomic_store((gu32 *)(ws + off), v, __ATOMIC_RELAXED, __HIP_MEMORY_SCOPE_AGENT); }
DI void poll_all(const char *ws, u32 off, u32 E) {
#pragma unroll 1
  for (int spin = 0; spin < SPIN_CAP; ++spin) {
    if (__all(ld_flag(ws, off) == E)) break;
    __builtin_amdgcn_s_sleep(POLL_SLEEP);
  }
  __builtin_amdgcn_fence(__ATOMIC_ACQUIRE, "wavefront");
}
template <u32 LI, u32 SI> DI void dmai(rsrc_t r, u32 voff, u32 sob, u32 ldsb) {
  u32 t;
  asm volatile("s_add_u32 m0, %2, %5\n\ts_add_u32 %0, %1, %6\n\tbuffer_load_dwordx4 %3, %4, %0 offen lds"
               : "=&s"(t) : "s"(sob), "s"(ldsb), "v"(voff), "s"(r), "i"(LI), "i"(SI) : "memory", "m0", "scc");
}
DI void dmar(rsrc_t r, u32 voff, u32 soff, u32 lds) {
  asm volatile("s_mov_b32 m0, %0\n\ts_nop 0\n\tbuffer_load_dwordx4 %1, %2, %3 offen lds" ::"s"(lds), "v"(voff), "s"(r), "s"(soff) : "memory", "m0");
}
template <int CTRL, int RMASK> DI float dpp_add(float v) {
  return v + asf(__builtin_amdgcn_update_dpp(0u, __builtin_bit_cast(u32, v), CTRL, RMASK, 0xf, true));
}
DI float wave_sum63(float v) {
  v = dpp_add<0x111, 0xf>(v); v = dpp_add<0x112, 0xf>(v); v = dpp_add<0x114, 0xf>(v); v = dpp_add<0x118, 0xf>(v);
  v = dpp_add<0x142, 0xa>(v); v = dpp_add<0x143, 0xc>(v);
  return v;
}
DI void st64_sc1(char *ws, u32 off, u32 lo, u32 hi) {
  __hip_atomic_store((GAS unsigned long long *)(ws + off), (unsigned long long)lo | ((unsigned long long)hi << 32), __ATOMIC_RELAXED, __HIP_MEMORY_SCOPE_AGENT);
}
struct Early { u32 ev; u32x4 xa, xb; };
DI Early early_issue(char *ws, rsrc_t rx, int row0, int lane) {
  Early e;
  e.ev = ld_flag(ws, WS_EPOCH);
  const u32 vo = lane < 56 ? (u32)lane * 16 : 0x7ffffff0u;
  e.xa = __builtin_amdgcn_raw_buffer_load_b128(rx, vo, row0 * XROW, 0); e.xb = __builtin_amdgcn_raw_buffer_load_b128(rx, vo, (row0 + 1) * XROW, 0);
  return e;
}
DI u32 early_finish(const Early &e, rsrc_t rws, int row0, int lane) {
  const u32 E = rfl(e.ev) + 1, ONES = 0x3F803F80u;
  float s[2], q[2];
  for (int r = 0; r < 2; ++r) {
    const u32x4 &x = r ? e.xb : e.xa;
    s[r] = dot2(x[0], ONES, 0.f); q[r] = dot2(x[0], x[0], 0.f);
    for (int d = 1; d < 4; ++d) { s[r] = dot2(x[d], ONES, s[r]); q[r] = dot2(x[d], x[d], q[r]); }
    s[r] = wave_sum63(s[r]); q[r] = wave_sum63(q[r]);
    const float mean = s[r] * (1.0f / DM);
    const float sig = __builtin_amdgcn_sqrtf(fmaxf(q[r] * (1.0f / DM) - mean * mean, 0.f) + 1e-5f);
    const u32 vo = lane == 63 ? WS_STAT + (row0 + r) * 16 : 0x7ffffff0u;
    __builtin_amdgcn_raw_buffer_store_b64(u32x2{__builtin_bit_cast(u32, mean), E}, rws, vo, 0, 16);
    __builtin_amdgcn_raw_buffer_store_b64(u32x2{__builtin_bit_cast(u32, sig), E}, rws, vo + 8, 0, 16);
  }
  return E;
}
DI u32x4 stat_settle(u32x4 g, rsrc_t rws, u32 vostat, u32 E) {
#pragma unroll 1
  for (int spin = 0; spin < SPIN_CAP && !__all(g[1] == E && g[3] == E); ++spin) { __builtin_amdgcn_s_sleep(1); g = ld128_sc1(rws, vostat, 0); }
  return g;
}
struct RowStat { float rstd; u32x4 xaug; };
DI RowStat row_stats(u32x4 g, int h) {
  const float m = -asf(g[0]), sig = asf(g[2]);
  RowStat r; r.rstd = __builtin_amdgcn_rcpf(sig);
  const float ml = m - bfhi(pk2(m, m)), sl = sig - bfhi(pk2(sig, sig));
  const u32 mm = pk2(m, ml), ss = pk2(sig, sl);
  r.xaug = h ? u32x4{0, 0, 0, 0} : u32x4{mm, mm, ss, 0};
  return r;
}
DI u32x4 waug(float cs, u32 bb) {
  const u32 hh = pk2(cs, cs); const float cl = cs - bfhi(hh);
  return u32x4{hh, pk2(cl, cl), bb, 0};
}
DI float cs_read(lchar *L, int t, int lane) { return *(LAS float *)(L + L_CSP + (t * 32 + (lane & 31)) * 4); }

DI void touch(rsrc_t r, u32 voff, u32 lb) {
  asm volatile("s_mov_b32 m0, %0\n\ts_nop 0\n\tbuffer_load_dword %1, %2, 0 offen lds" ::"s"(lb + L_DUMMY), "v"(voff), "s"(r) : "memory", "m0");
}
constexpr u32 CODE_BYTES = 50 * 1024, CODE_PIECES = (CODE_BYTES + 8191) / 8192;
DI void warm_up(unsigned long long pc, rsrc_t rws, const char *gw2, const char *gwo, int xcd, int wv, int lane, u32 lb) {
  if (WARM & 1) {
    const rsrc_t rc = __builtin_amdgcn_make_buffer_rsrc((void *)(pc & ~127ull), 0, CODE_BYTES, 0x00020000);
    if (wv < (int)CODE_PIECES) touch(rc, wv * 8192 + lane * 128, lb);
  }
  const int g = xcd * 16 + wv;
  if (WARM & 2) { if (g < 27) touch(rws, WS_KF + g * 8192 + lane * 128, lb); }
  if (WARM & 4) {
    const rsrc_t r2 = __builtin_amdgcn_make_buffer_rsrc((void *)gw2, 0, DF * XROW, 0x00020000), ro = __builtin_amdgcn_make_buffer_rsrc((void *)gwo, 0, DM * XROW, 0x00020000);
    touch(r2, g * 8192 + lane * 128, lb);
    if (g < 68) touch(r2, (128 + g) * 8192 + lane * 128, lb);
    else if (g < 117) touch(ro, (g - 68) * 8192 + lane * 128, lb);
  }
}
template <bool ATT, class First, class After0, class Mid>
DI void gemmA_compute(lchar *L, int w, int lane, f32x16 (&acc)[2][ATT ? 3 : 4], First &&first, After0 &&after0, Mid &&mid STAMP_ARG) {
  constexpr int T = ATT ? 3 : 4, ROWB = ATT ? 64 : 256;
  const int h = lane >> 5, blk = (lane >> 4) & 1, q = (lane & 15) >> 2, p = lane & 3;
  const u32 lb = (u32)(size_t)L;
  u32 wb0[T], xb0[4], wb[T], xb[4], cb0, cb;
  for (int t = 0; t < T; ++t) wb0[t] = lb + A_WOFF + (ATT ? t * 4096 + (8 * h + q) * 64 : (8 * h + q) * 256 + 64 * (t ^ q)) + 32 * blk + 8 * p;
  for (int u = 0; u < 4; ++u) xb0[u] = lb + w * 8192 + (lane & 31) * 128 + 16 * ((2 * u + h) ^ ((lane >> 1) & 7));
  const int tw = w < T ? w : T - 1;
  cb0 = lb + A_WOFF + (ATT ? tw * 4096 + (8 * h + q) * 64 : (8 * h + q) * 256 + 64 * (tw ^ q)) + 32 * blk + 8 * p;
  f32x2 ca = {0.f, 0.f};
  for (int r = 0; r < 2; ++r) for (int t = 0; t < T; ++t) acc[r][t] = zero16();
  u32x4 xf[2][2], wf[2][T], cf[2];
  sfor<0, 29>([&](auto I) {
    constexpr int i = I, c = i >> 2, u = i & 3;
    if constexpr (i == 0 && FIRST_AT < 0) { first(); STAMP(14); }
    if constexpr (u == 0 && i < 28) {
      BAR();
      if constexpr (c == 0) STAMP(1);
      constexpr u32 so = (c % 3) * A_SLOT;
      for (int t = 0; t < T; ++t) { wb[t] = wb0[t] + so; asm volatile("" : "+v"(wb[t])); }
      for (int j = 0; j < 4; ++j) { xb[j] = xb0[j] + so; asm volatile("" : "+v"(xb[j])); }
      cb = cb0 + so; asm volatile("" : "+v"(cb));
    }
    if constexpr (i < 28) {
      cf[i & 1] = trfrag((lchar *)(size_t)cb + 16 * u * ROWB, 4 * ROWB);
      for (int r = 0; r < 2; ++r) xf[i & 1][r] = *(LAS u32x4 *)((lchar *)(size_t)xb[u] + r * 4096);
      for (int t = 0; t < T; ++t) wf[i & 1][t] = trfrag((lchar *)(size_t)wb[t] + 16 * u * ROWB, 4 * ROWB);
    }
    if constexpr (i == FIRST_AT) first();
    if constexpr (i == 1) after0();
    if constexpr (i == 20) mid();
    if constexpr (i > 0) {
      constexpr int j = (i - 1) & 1;
      for (int t = 0; t < T; ++t)
        for (int r = 0; r < 2; ++r) acc[r][t] = (ATT && t == 1) ? mfma(xf[j][r], wf[j][t], acc[r][t]) : mfma(wf[j][t], xf[j][r], acc[r][t]);
      for (int d = 0; d < 4; ++d) ca += f32x2{bf2f(cf[j][d]), bfhi(cf[j][d])};
      asm volatile("" : "+v"(ca));
      for (int m = 0; m < 2 * T; ++m) { __builtin_amdgcn_sched_group_barrier(0x8, 1, 0); __builtin_amdgcn_sched_group_barrier(0x100, 2, 0); __builtin_amdgcn_sched_group_barrier(0x2, 2, 0); }
    }
    __builtin_amdgcn_sched_barrier(0);
  });
  { const f32x2 a = both_halves(ca.x + ca.y); if (lane < 32 && w < T) *(LAS float *)(L + L_CSP + (w * 32 + lane) * 4) = a.x + a.y; }
  STAMP(2);
}
constexpr int w2_row(int st) { return st < 7 ? 128 * st : 128 * (st - 7) + 64; }
constexpr int h_step(int i) { return i < 28 ? 8 * (i >> 2) + (i & 3) : 8 * ((i - 28) >> 2) + 4 + (i & 3); }
struct StageCtx { rsrc_t rw2, rwo; u32 vob, so2, soo, ld; };
template <int ST> DI void stage_dma(const StageCtx &c) {
  sfor<0, 4>([&](auto I) {
    constexpr u32 i = I, dst = ((ST + 4) % NSLOT) * L_SLOT + i * 1024;
    dmai<dst, (w2_row(ST) + 8 * i) * XROW>(c.rw2, c.vob, c.so2, c.ld);
  });
}
template <int ST> DI void stage_load(const StageCtx &c, u32x4 (&br)[36]) {
  sfor<0, 4>([&](auto I) {
    constexpr u32 i = I;
    if constexpr (ST < 14) br[4 * (ST - 9) + i] = __builtin_amdgcn_raw_buffer_load_b128(c.rw2, c.vob, c.so2 + (w2_row(ST) + 8 * i) * XROW, 0);
    else br[4 * (ST - 9) + i] = __builtin_amdgcn_raw_buffer_load_b128(c.rwo, c.vob, c.soo + (56 * (ST - 14) + (i == 3 ? 20 : 8 * i)) * XROW, 0);
  });
}
template <bool ATT>
DI void gemmA_loader(lchar *L, rsrc_t rx, rsrc_t rw, u32 sox, u32 sow, int lw, int lane, const StageCtx &sc, u32x4 (&br)[36], rsrc_t rp, u32 wpo STAMP_ARG) {
  constexpr int NW = ATT ? 3 : 4, N = 8 + NW;
  const u32 lb = (u32)(size_t)L;
  const u32 vox0 = (lane >> 3) * XROW + 16 * ((lane & 7) ^ ((lane >> 4) & 3)), vox1 = vox0 ^ 64;
  const u32 ldx = rfl(lb + lw * 8192);
  const u32 vow = ATT ? (lane >> 2) * QKVROW + ((lane & 3) == 3 ? 40 : 16 * (lane & 3)) : (lane >> 4) * W1ROW + 16 * ((lane & 15) ^ ((lane >> 4) << 2));
  const u32 swb = rfl(sow + 16 * lw * (ATT ? QKVROW : W1ROW)), ldw = rfl(lb + A_WOFF + lw * (ATT ? 1024 : 4096));
  auto issue = [&](auto C) {
    constexpr u32 c = C, so = (c % 3) * A_SLOT;
    sfor<0, NW>([&](auto I) {
      constexpr u32 i = I;
      if constexpr (ATT) dmai<so + i * 4096, 64 * c * QKVROW + (i == 0 ? 896 : i == 1 ? 1792 : 0)>(rw, vow, swb, ldw);
      else dmai<so + i * 1024, (64 * c + 4 * i) * W1ROW>(rw, vow, swb, ldw);
    });
    sfor<0, 8>([&](auto I) { constexpr u32 i = I; dmai<so + i * 1024, 8 * i * XROW + 128 * c>(rx, (i & 1) ? vox1 : vox0, sox, ldx); });
  };
  sfor<0, 7>([&](auto C) {
    constexpr int c = C;
    if constexpr (ATT && c == 1) { if (lw < 2) vmwait<N + 1>(); else vmwait<N>(); }
    else vmwait<(c == 6 ? 12 : N)>();
    asm volatile("s_barrier" ::: "memory");
    if constexpr (c == 0) STAMP(1);
    if constexpr (ATT && c == 0) {
      if (lw < 2) dmar(rp, lane * 16 + lw * 1024, wpo, lb + L_WPHI + lw * 1024);
    }
    if constexpr (c + 2 < 7) issue(std::integral_constant<int, c + 2>{});
    else { stage_load<9 + 3 * (c - 5)>(sc, br); stage_load<10 + 3 * (c - 5)>(sc, br); stage_load<11 + 3 * (c - 5)>(sc, br); }
  });
  STAMP(2);
}

DI void feature_map(const f32x16 &P, float rstd, int h, u32x4 (&pf)[2][2]) {
  const float c = rstd * LOG2E;
  float mx = fmaxf(fmaxf(P[0], P[1]), P[2]), mn = fminf(fminf(P[0], P[1]), P[2]);
  for (int i = 3; i < 15; i += 2) { mx = fmaxf(fmaxf(mx, P[i]), P[i + 1]); mn = fminf(fminf(mn, P[i]), P[i + 1]); }
  mx = fmaxf(mx, P[15]); mn = fminf(mn, P[15]);
  { f32x2 a = both_halves(mx), b = both_halves(mn); mx = fmaxf(a.x, a.y); mn = fminf(b.x, b.y); }
  const float mxc = -mx * c, mnc = mn * c;
  f32x16 ep, en;
  for (int i = 0; i < 16; i += 2) {
    const f32x2 p2 = {P[i], P[i + 1]}, ap = p2 * c + mxc, an = mnc - p2 * c;
    ep[i] = __builtin_amdgcn_exp2f(ap.x); ep[i + 1] = __builtin_amdgcn_exp2f(ap.y);
    en[i] = __builtin_amdgcn_exp2f(an.x); en[i + 1] = __builtin_amdgcn_exp2f(an.y);
  }
  f32x2 sp2 = {ep[0], ep[1]}, sn2 = {en[0], en[1]};
  for (int i = 2; i < 12; i += 2) { sp2 += f32x2{ep[i], ep[i + 1]}; sn2 += f32x2{en[i], en[i + 1]}; }
  const float keep = h ? 0.f : 1.f;
  sp2 += (f32x2{ep[12], ep[13]} + f32x2{ep[14], ep[15]}) * keep;
  sn2 += (f32x2{en[12], en[13]} + f32x2{en[14], en[15]}) * keep;
  const f32x2 a = both_halves(sp2.x + sp2.y), b = both_halves(sn2.x + sn2.y);
  const float rp = __builtin_amdgcn_rcpf(a.x + a.y), rn = __builtin_amdgcn_rcpf(b.x + b.y);
  for (int i = 0; i < 16; i += 2) {
    const f32x2 u = f32x2{ep[i], ep[i + 1]} * rp, v = f32x2{en[i], en[i + 1]} * rn;
    ep[i] = u.x; ep[i + 1] = u.y; en[i] = v.x; en[i + 1] = v.y;
  }
  pf[0][0] = pack8(ep, 0); pf[0][1] = pack8(ep, 1); pf[1][0] = pack8(en, 0); pf[1][1] = pack8(en, 1);
}

constexpr int refill_end(int k) { return k < 0 ? 0 : k == 0 ? 7 : k == 1 ? 14 : k == 2 ? 20 : k == 3 ? 26 : k == 4 ? 32 : 36; }
constexpr bool refill_ok() { for (int k = 0; k < 6; ++k) for (int p = refill_end(k - 1); p < refill_end(k); ++p) { const int g = 9 + p / 4, s = 4 + 2 * k; if (s < g - 8 || 4 * (s + 2) > 4 * g - 2) return false; } return true; }
static_assert(refill_ok(), "refill schedule");
}

__global__ void __launch_bounds__(512, 2) fused_hedgehog_ffn(const char *__restrict__ gx, const char *__restrict__ gwqkv, const char *__restrict__ gwphi, const char *__restrict__ gwo,
                                                              const char *__restrict__ gw1, const char *__restrict__ gb1, const char *__restrict__ gw2, const char *__restrict__ gb2,
                                                              char *__restrict__ gout, char *__restrict__ gws) {
  __shared__ __attribute__((aligned(1024))) char smem[LDS_BYTES];
  lchar *L = (lchar *)smem;
  unsigned long long pc0;
  asm volatile("s_getpc_b64 %0" : "=s"(pc0));
  asm volatile("" ::"s"(gx), "s"(gwqkv), "s"(gwphi), "s"(gwo), "s"(gw1), "s"(gb1), "s"(gw2), "s"(gb2), "s"(gout), "s"(gws));
  const int tid = threadIdx.x, lane = tid & 63, w = rfl(tid >> 6);
  const int b = blockIdx.x, xcd = b & 7;
  int slot = b >> 3;
  STAMP_DECL; STAMP(0);
  const rsrc_t rx = __builtin_amdgcn_make_buffer_rsrc((void *)gx, 0, 2048 * XROW, 0x00020000);
  if (__builtin_expect(w >= 4 && slot < 30, 1)) {
    const bool a = slot < 16;
    const u32 lw = w - 4, lb = (u32)(size_t)L, hi = slot >= 23;
    const u32 rg0 = (xcd & 6) + (a ? slot >> 3 : hi), sox = (256 * rg0 + 64 * lw) * XROW, ldx = lb + lw * 8192;
    const u32 vox = (lane >> 3) * XROW + 16 * ((lane & 7) ^ ((lane >> 4) & 3));
    const rsrc_t rw = __builtin_amdgcn_make_buffer_rsrc((void *)(a ? gwqkv : gw1), 0, a ? DM * QKVROW : DM * W1ROW, 0x00020000);
    const u32 vow = a ? (lane >> 2) * QKVROW + ((lane & 3) == 3 ? 40 : 16 * (lane & 3)) : (lane >> 4) * W1ROW + 16 * ((lane & 15) ^ ((lane >> 4) << 2));
    const u32 swb = a ? 56 * (8 * (xcd & 1) + (slot & 7)) + 16 * lw * QKVROW : 256 * (7 * (xcd & 1) + slot - 16 - 7 * hi) + 16 * lw * W1ROW;
    const u32 ldw = lb + A_WOFF + lw * (a ? 1024 : 4096), ls = a ? 4096 : 1024, cs = a ? 64 * QKVROW : 64 * W1ROW;
    sfor<0, 2>([&](auto C) {
      constexpr u32 c = C;
      dmar(rw, vow, swb + c * cs + (a ? 896 : 0), ldw + c * A_SLOT);
      dmar(rw, vow, swb + c * cs + (a ? 1792 : 4 * W1ROW), ldw + c * A_SLOT + ls);
      dmar(rw, vow, swb + c * cs + (a ? 0 : 8 * W1ROW), ldw + c * A_SLOT + 2 * ls);
      if (!a) dmar(rw, vow, swb + c * cs + 12 * W1ROW, ldw + c * A_SLOT + 3 * ls);
      sfor<0, 8>([&](auto I) { constexpr u32 i = I; dmai<c * A_SLOT + i * 1024, 8 * i * XROW + 128 * c>(rx, (i & 1) ? vox ^ 64 : vox, sox, ldx); });
    });
  }
  asm volatile("" : "+s"(slot));
  const int h = lane >> 5;
  const rsrc_t rws = __builtin_amdgcn_make_buffer_rsrc((void *)gws, 0, WS_END, 0x00020000);
  const int tc = slot >= 16 ? slot - 16 : slot + 16;
  const bool has_tile = tc < 28;
  const u32 rt = tc / 7, cc = tc % 7;
  const bool att = slot < 16, ffn = !att && slot < 30;
  const int head = 8 * (xcd & 1) + (slot & 7);
  const int jf = slot - 16, ct = 7 * (xcd & 1) + (jf >= 7 ? jf - 7 : jf);
  const int rga = 2 * (xcd >> 1) + (att ? (slot >> 3) : (jf >= 7));

  if (w >= 4) {
    const int lw = w - 4;
    StageCtx sc;
    sc.rw2 = __builtin_amdgcn_make_buffer_rsrc((void *)gw2, 0, DF * XROW, 0x00020000);
    sc.rwo = __builtin_amdgcn_make_buffer_rsrc((void *)gwo, 0, DM * XROW, 0x00020000);
    sc.vob = (lane >> 3) * XROW + 16 * ((lane & 7) ^ (((lane >> 4) & 1) << 2));
    sc.so2 = rfl((896 * (lw >> 1) + 32 * (lw & 1)) * XROW + 128 * cc);
    sc.soo = rfl(28 * (8 * (lw >> 1) + (lw & 1)) * XROW + 128 * cc);
    sc.ld = rfl((u32)(size_t)L + (lw >> 1) * 8192 + (lw & 1) * 4096);
    u32x4 br[36];
    auto resid_dma = [&]() {
      const u32 vo = (lane >> 3) * XROW + 16 * ((lane & 7) ^ ((lane >> 4) & 3)), so = rfl((256 * xcd + 64 * rt + 16 * lw) * XROW + 128 * cc), ld = rfl((u32)(size_t)L + L_XT + lw * 2048);
      dmai<0, 0>(rx, vo, so, ld);
      dmai<1024, 8 * XROW>(rx, vo ^ 64, so, ld);
    };
    if (att) {
      const rsrc_t rw = __builtin_amdgcn_make_buffer_rsrc((void *)gwqkv, 0, DM * QKVROW, 0x00020000);
      gemmA_loader<true>(L, rx, rw, rfl((256 * rga + 64 * lw) * XROW), rfl(56 * head), lw, lane, sc, br, __builtin_amdgcn_make_buffer_rsrc((void *)gwphi, 0, 16 * 28 * 28 * 2, 0x00020000), rfl(head * 1568) STAMP_PASS);
      BAR();
      const u32 E = rfl(*(LAS u32 *)(L + L_E));
      if (has_tile) { sfor<0, 5>([&](auto S) { stage_dma<S>(sc); }); sfor<5, 7>([&](auto S) { stage_dma<S>(sc); }); resid_dma(); }
      sfor<15, 18>([&](auto S) { stage_load<S>(sc, br); });
      BAR();
      __builtin_amdgcn_s_setprio(XPRIO);
      STAMP(4);
      for (int t = 0; t < 2; ++t) {
        lchar *sp = (lchar *)(size_t)(sc.ld + (2 + t) * L_SLOT + lane * 16);
        f32x4 s = *(LAS f32x4 *)sp;
        for (int ww = 1; ww < 4; ++ww) s += *(LAS f32x4 *)(sp + ww * 1024);
        st128_sc1(u32x4{pk2(s.x, s.y), E, pk2(s.z, s.w), E}, rws, lane * 16, WS_KVP + (head * 8 + rga) * 8192 + (t * 4 + lw) * 1024);
      }
      STAMP(5);
      vmwait<24>();
      if (lane == 0) *(LAS u32 *)(L + L_BRDY + 4 * lw) = E;
      poll_all(gws, WS_KVP + (head * 8 + (lane & 7)) * 8192 + (4 + lw) * 1024 + 63 * 16 + 12, E);
      STAMP(7);
      f32x2 ks[2][2];
#pragma unroll 1
      for (int spin = 0; spin < SPIN_CAP; ++spin) {
        u32x4 part[2][8];
        for (int t = 0; t < 2; ++t) for (int r = 0; r < 8; ++r) part[t][r] = ld128_sc1(rws, lane * 16, WS_KVP + (head * 8 + r) * 8192 + (t * 4 + lw) * 1024);
        u32 ta = E, to = E;
        for (int t = 0; t < 2; ++t) {
          ks[t][0] = f32x2{0.f, 0.f}; ks[t][1] = f32x2{0.f, 0.f};
          for (int r = 0; r < 8; ++r) {
            ks[t][0] += f32x2{bf2f(part[t][r][0]), bfhi(part[t][r][0])}; ks[t][1] += f32x2{bf2f(part[t][r][2]), bfhi(part[t][r][2])};
            ta &= part[t][r][1] & part[t][r][3]; to |= part[t][r][1] | part[t][r][3];
          }
        }
        if (__all(ta == E && to == E)) break;
        __builtin_amdgcn_s_sleep(POLL_SLEEP);
      }
      for (int t = 0; t < 2; ++t) {
        u32x2 pk = {pk2(ks[t][0].x, ks[t][0].y), pk2(ks[t][1].x, ks[t][1].y)};
        if (lw == 3 && h) pk = u32x2{0, 0};
        *(LAS u32x2 *)(L + L_KVF + ((t * 2 + (lw >> 1)) * 64 + lane) * 16 + (lw & 1) * 8) = pk;
      }
      __builtin_amdgcn_s_setprio(0);
      STAMP(8);
      if (!has_tile) {
        BAR();
        if (xcd == 0 && slot == 12 && lw == 0) {
#pragma unroll 1
          for (int spin = 0; spin < SPIN_CAP; ++spin) {
            bool ok = true;
            for (int i = 0; i < 32; ++i) ok &= ld_flag(gws, WS_STAT + (64 * i + lane) * 16 + 4) == E;
            if (__all(ok)) break;
            __builtin_amdgcn_s_sleep(8);
          }
          if (lane == 0) st_flag(gws, WS_EPOCH, E);
        }
        STAMP_OUT();
        return;
      }
    } else if (ffn) {
      const rsrc_t rw = __builtin_amdgcn_make_buffer_rsrc((void *)gw1, 0, DM * W1ROW, 0x00020000);
      gemmA_loader<false>(L, rx, rw, rfl((256 * rga + 64 * lw) * XROW), rfl(256 * ct), lw, lane, sc, br, rx, 0 STAMP_PASS);
      BAR();
      if (HOLD == 1 || HOLD == 3) asm volatile("s_barrier" ::: "memory");
      sfor<0, 5>([&](auto S) { stage_dma<S>(sc); });
      vmwait<0>();
      BAR();
      sfor<5, 9>([&](auto S) { stage_dma<S>(sc); });
      resid_dma();
      sfor<15, 18>([&](auto S) { stage_load<S>(sc, br); });
    } else {
      warm_up(pc0, rws, gw2, gwo, xcd, (slot - 30) * 8 + w, lane, (u32)(size_t)L);
      sfor<0, 9>([&](auto S) { stage_dma<S>(sc); });
      resid_dma();
      sfor<9, 18>([&](auto S) { stage_load<S>(sc, br); });
      vmwait<36>();
      BAR();
    }
    STAMP(10);
    const u32 ldv = sc.ld + lane * 16;
    sfor<0, 7>([&](auto K) {
      constexpr int k = K;
      if constexpr (k == 0) vmwait<12>();
      if constexpr (k == 1) vmwait<0>();
      BAR();
      if constexpr (k == 0) { if (att) sfor<7, 9>([&](auto S) { stage_dma<S>(sc); }); }
      sfor<refill_end(k - 1), refill_end(k)>([&](auto Pc) {
        constexpr int pc = Pc;
        *(LAS u32x4 *)((lchar *)(size_t)ldv + ((9 + pc / 4 + 4) % NSLOT) * L_SLOT + (pc & 3) * 1024) = br[pc];
        if (SPREAD) asm volatile("s_nop %0" ::"i"(SPREAD ? SPREAD - 1 : 0) : "memory");
      });
    });
    STAMP(12);
    asm volatile("s_barrier" ::: "memory");
    STAMP_OUT();
    return;
  }

  const Early early = early_issue(gws, rx, 8 * b + 2 * w, lane);
  u32 E;
  auto touches = [&]() {
    if (att || ffn) {
      constexpr u32 OOB = 0x7ffffff0u;
      const u32 lb = (u32)(size_t)L;
      { const u32 g15 = att ? (slot & 7) : 8 + (jf >= 7 ? jf - 7 : jf), i = (g15 * 4 + w) * 22 + lane;
        touch(rx, (lane < 22 && i < 1280) ? (256 * rga + (i & 255)) * XROW + 128 * (2 + (i >> 8)) : OOB, lb); }
      if (att) {
        const rsrc_t rq = __builtin_amdgcn_make_buffer_rsrc((void *)gwqkv, 0, DM * QKVROW, 0x00020000);
        const u32 i = slot * 240 + w * 60 + lane, row = (i * 43691u) >> 19, rem = i - 12 * row;
        touch(rq, lane < 60 ? (128 + row) * QKVROW + 896 * (rem >> 2) + ((448 * (xcd & 1)) & ~127u) + 128 * (rem & 3) : OOB, lb);
      } else {
        const rsrc_t r1 = __builtin_amdgcn_make_buffer_rsrc((void *)gw1, 0, DM * W1ROW, 0x00020000);
        const u32 l8 = 4 * (jf >= 7) + w, i = 64 * l8 + lane, j = 512 + 16 * l8 + lane;
        touch(r1, (128 + (i >> 1)) * W1ROW + 256 * ct + 128 * (i & 1), lb);
        touch(r1, lane < 16 ? (128 + (j >> 1)) * W1ROW + 256 * ct + 128 * (j & 1) : OOB, lb);
      }
    }
  };
  if (TOUCH_A == 2) touches();
  auto first = [&]() {
    E = early_finish(early, rws, 8 * b + 2 * w, lane); if (tid == 0) *(LAS u32 *)(L + L_E) = E; if (tid < 4) *(LAS u32 *)(L + L_BRDY + 4 * tid) = ~E;
    if (TOUCH_A == 1) touches();
  };
  const int rh = w & 1, kg = w >> 1;
  const u32 rb = 8 * xcd + 2 * rt + rh;
  const u32 row = 32 * rb + (lane & 31), col0 = 64 * cc + 32 * kg;
  u32x4 pq[2][2][2];
  const u32 hfa = WS_HFA + (rb * 14 + 7 * kg + ((lane & 7) < 7 ? (lane & 7) : 0)) * 64;
  u32 hlook = 0;
  auto o_stage = [&]() {
    u32x4 kf[2][2];
    for (int t = 0; t < 2; ++t) for (int s = 0; s < 2; ++s) kf[t][s] = *(LAS u32x4 *)(L + L_KVF + ((t * 2 + s) * 64 + lane) * 16);
    f32x16 num[2] = {zero16(), zero16()};
    for (int t = 0; t < 2; ++t) for (int s = 0; s < 2; ++s) for (int r = 0; r < 2; ++r) num[r] = mfma(kf[t][s], pq[r][t][s], num[r]);
    for (int r = 0; r < 2; ++r) {
      const float rc = __builtin_amdgcn_rcpf(both_halves(num[r][12]).x + 1e-6f);
      for (int i = 0; i < 16; i += 2) { const f32x2 v = f32x2{num[r][i], num[r][i + 1]} * rc; num[r][i] = v.x; num[r][i + 1] = v.y; }
      if (!h) { num[r][12] = 0.f; num[r][13] = 0.f; num[r][14] = 0.f; num[r][15] = 0.f; }
      const u32 so = WS_O + ((8 * rga + 2 * w + r) * 32 + 2 * head) * 1024;
      st128_sc1(pack8(num[r], 0), rws, lane * 16, so);
      st128_sc1(pack8(num[r], 1), rws, lane * 16, so + 1024);
    }
  };
  if (att) {
    if (tid < 16) *(LAS u32 *)(L + L_ZERO + 4 * tid) = 0;
    u32x4 idf[2];
    for (int s = 0; s < 2; ++s)
      for (int d = 0; d < 4; ++d) {
        const int k0 = 16 * s + 8 * (d >> 1) + 4 * h + 2 * (d & 1), n = lane & 31;
        idf[s][d] = (n == k0 ? 0x3F80u : 0u) | (n == k0 + 1 ? 0x3F800000u : 0u);
      }
    for (int s = 0; s < 2; ++s) asm volatile("" : "+v"(idf[s]));
    f32x16 acc[2][3]; u32x4 stat[2];
    const u32 vostat = WS_STAT + (256 * rga + 64 * w + (lane & 31)) * 16;
    gemmA_compute<true>(L, w, lane, acc, first, [&]() { if (TOUCH_A == 3) touches(); }, [&]() { for (int r = 0; r < 2; ++r) stat[r] = ld128_sc1(rws, vostat + 512 * r, 0); } STAMP_PASS);
    RowStat st[2];
    for (int r = 0; r < 2; ++r) {
      st[r] = row_stats(stat_settle(stat[r], rws, vostat + 512 * r, E), h);
      *(LAS float *)(L + L_TOK + w * 256 + r * 128 + 4 * (lane & 31)) = st[r].rstd;
    }
    BAR();
    STAMP(3);
    {
      const u32x4 wk = waug(cs_read(L, 0, lane), 0), wv = waug(cs_read(L, 1, lane), 0), wq = waug(cs_read(L, 2, lane), 0);
      for (int r = 0; r < 2; ++r) { acc[r][0] = mfma(wk, st[r].xaug, acc[r][0]); acc[r][1] = mfma(st[r].xaug, wv, acc[r][1]); acc[r][2] = mfma(wq, st[r].xaug, acc[r][2]); }
    }
    u32x4 wp[2];
    {
      const int blk = (lane >> 4) & 1, q = (lane & 15) >> 2, p = lane & 3;
      const u32 col = (blk && p == 3) ? 24 : 16 * blk + 4 * p;
      for (int s = 0; s < 2; ++s) {
        u32x2 r[2];
        for (int t = 0; t < 2; ++t) {
          const int dp = 16 * s + 8 * t + 4 * h + q;
          const u32 a = (dp >= 24 && dp < 28) ? L_ZERO + 8 * p : L_WPHI + (dp >= 28 ? dp - 4 : dp) * 56 + col * 2;
          r[t] = trd(L + a);
        }
        wp[s] = u32x4{r[0].x, r[0].y, r[1].x, r[1].y};
      }
    }
    f32x16 kvp[2] = {zero16(), zero16()};
    {
      f32x16 P[2];
      for (int r = 0; r < 2; ++r) P[r] = mfma(wp[0], pack8(acc[r][0], 0), zero16());
      for (int r = 0; r < 2; ++r) P[r] = mfma(wp[1], pack8(acc[r][0], 1), P[r]);
      u32x4 vf[2][2];
      for (int r = 0; r < 2; ++r) {
        f32x16 v = acc[r][1];
        for (int g = 0; g < 4; ++g) {
          const f32x4 rs = *(LAS f32x4 *)(L + L_TOK + w * 256 + r * 128 + (8 * g + 4 * h) * 4);
          for (int e = 0; e < 4; e += 2) { const f32x2 t2 = f32x2{v[4 * g + e], v[4 * g + e + 1]} * f32x2{rs[e], rs[e + 1]}; v[4 * g + e] = t2.x; v[4 * g + e + 1] = t2.y; }
        }
        for (int s = 0; s < 2; ++s) { vf[r][s] = pack8(v, s); if ((lane & 31) == 24) vf[r][s] = u32x4{0x3F803F80u, 0x3F803F80u, 0x3F803F80u, 0x3F803F80u}; }
      }
      u32x4 pf[2][2][2];
      for (int r = 0; r < 2; ++r) feature_map(P[r], st[r].rstd, h, pf[r]);
      f32x16 Z[2][2];
      for (int r = 0; r < 2; ++r) for (int t = 0; t < 2; ++t) Z[r][t] = mfma(pf[r][t][0], idf[0], zero16());
      for (int r = 0; r < 2; ++r) for (int t = 0; t < 2; ++t) Z[r][t] = mfma(pf[r][t][1], idf[1], Z[r][t]);
      for (int r = 0; r < 2; ++r) for (int s = 0; s < 2; ++s) for (int t = 0; t < 2; ++t) kvp[t] = mfma(pack8(Z[r][t], s), vf[r][s], kvp[t]);
    }
    for (int t = 0; t < 2; ++t)
      for (int g = 0; g < 4; ++g)
        *(LAS f32x4 *)(L + (2 + t) * L_SLOT + (g >> 1) * 8192 + (g & 1) * 4096 + w * 1024 + lane * 16) = f32x4{kvp[t][4 * g], kvp[t][4 * g + 1], kvp[t][4 * g + 2], kvp[t][4 * g + 3]};
    STAMP(4);
    BAR();
    {
      f32x16 P[2];
      for (int r = 0; r < 2; ++r) P[r] = mfma(wp[0], pack8(acc[r][2], 0), zero16());
      for (int r = 0; r < 2; ++r) P[r] = mfma(wp[1], pack8(acc[r][2], 1), P[r]);
      for (int r = 0; r < 2; ++r) {
        feature_map(P[r], st[r].rstd, h, pq[r]);
        for (int t = 0; t < 2; ++t) for (int s = 0; s < 2; ++s) asm volatile("" : "+v"(pq[r][t][s]));
        if (r == 0 && has_tile) hlook = ld_flag(gws, hfa);
        __builtin_amdgcn_sched_barrier(0);
      }
    }
    STAMP(6);
    if (!has_tile) {
      BAR();
      o_stage();
      vmwait<0>();
      if (lane < 2) st_flag(gws, WS_OF + ((8 * rga + 2 * w + lane) * 16 + head) * 64, E);
      STAMP_OUT();
      return;
    }
  } else if (ffn) {
    u32 bias[4];
    for (int t = 0; t < 4; ++t) bias[t] = *(const GAS unsigned short *)(gb1 + (128 * ct + 32 * t + (lane & 31)) * 2);
    f32x16 acc[2][4]; u32x4 stat[2];
    const u32 vostat = WS_STAT + (256 * rga + 64 * w + (lane & 31)) * 16;
    gemmA_compute<false>(L, w, lane, acc, first, [&]() { if (TOUCH_A == 3) touches(); }, [&]() { for (int r = 0; r < 2; ++r) stat[r] = ld128_sc1(rws, vostat + 512 * r, 0); } STAMP_PASS);
    RowStat st[2];
    for (int r = 0; r < 2; ++r) st[r] = row_stats(stat_settle(stat[r], rws, vostat + 512 * r, E), h);
    BAR();
    STAMP(3);
    if (HOLD == 2) __builtin_amdgcn_s_setprio(3);
    for (int t = 0; t < 4; ++t) {
      const u32x4 wa = waug(cs_read(L, t, lane), bias[t] | (bias[t] << 16));
      for (int r = 0; r < 2; ++r) {
        f32x16 a = mfma(wa, st[r].xaug, acc[r][t]);
        for (int i = 0; i < 16; i += 2) { f32x2 v = {relu(a[i]), relu(a[i + 1])}; v *= v; a[i] = v.x; a[i + 1] = v.y; }
        const u32 so = WS_H + ((8 * rga + 2 * w + r) * 112 + 8 * ct + 2 * t) * 1024;
        st128_sc1(pack8(a, 0), rws, lane * 16, so);
        st128_sc1(pack8(a, 1), rws, lane * 16, so + 1024);
      }
      if (HOLD == 3 && t == 1) asm volatile("s_barrier" ::: "memory");
      if (t == 2) {
        vmwait<4>();
        if (lane < 2) st_flag(gws, WS_HFA + ((8 * rga + 2 * w + lane) * 14 + ct) * 64, E);
      }
    }
    if (HOLD == 1) asm volatile("s_barrier" ::: "memory");
    if (HOLD == 2) __builtin_amdgcn_s_setprio(0);
    STAMP(4);
    vmwait<0>();
    if (lane < 2) st_flag(gws, WS_HFB + ((8 * rga + 2 * w + lane) * 14 + ct) * 64, E);
    STAMP(5);
    for (int t = 0; t < 2; ++t) for (int r = 0; r < 2; ++r) for (int s = 0; s < 2; ++s) pq[t][r][s] = u32x4{0, 0, 0, 0};
  } else {
    warm_up(pc0, rws, gw2, gwo, xcd, (slot - 30) * 8 + w, lane, (u32)(size_t)L);
    first();
    for (int t = 0; t < 2; ++t) for (int r = 0; r < 2; ++r) for (int s = 0; s < 2; ++s) pq[t][r][s] = u32x4{0, 0, 0, 0};
  }

  {
    constexpr int RD = RING;
    const u32 soh = WS_H + (rb * 112 + 56 * kg) * 1024, soo = WS_O + (rb * 32 + 16 * kg) * 1024;
    u32x4 ring[RD];
    u32x2 b2v[4]; u32x4 sg0;
    const int blk = (lane >> 4) & 1, q = (lane & 15) >> 2, p = lane & 3;
    u32 bb0[2], bb[2];
    for (int c2 = 0; c2 < 2; ++c2) bb0[c2] = (u32)(size_t)L + kg * 8192 + (4 * h + q) * 128 + ((c2 ^ kg ^ (q >> 1)) * 64) + 32 * blk + 8 * p;
    f32x16 af[2] = {zero16(), zero16()}, aa[2] = {zero16(), zero16()};
    constexpr int PD = BPD;
    u32x4 bf[PD + 1][2];
    const u32 hfb = WS_HFB + (rb * 14 + 7 * kg + ((lane & 7) < 7 ? (lane & 7) : 0)) * 64, ofo = WS_OF + (rb * 16 + 8 * kg + (lane & 7)) * 64;
    u32 look = 0, soe = 0;
    bool o_early = false;
    float rs2; f32x2 bx[8], sg[8], xx[8];
    u32x4 oa[16];
    if (!(att && __all(hlook == E))) poll_all(gws, hfa, E);
    STAMP(10);
    if (!att) BAR();
    else {
#pragma unroll 1
      for (int spin = 0; spin < SPIN_CAP; ++spin) {
        const u32x4 r = *(volatile LAS u32x4 *)(L + L_BRDY);
        if (r[0] == E && r[1] == E && r[2] == E && r[3] == E) break;
        __builtin_amdgcn_s_sleep(1);
      }
      asm volatile("" ::: "memory");
    }
    sfor<0, 72>([&](auto I) {
      constexpr int i = I;
      if constexpr (i == 56) {
        for (int g = 0; g < 4; ++g)
          *(LAS f32x4 *)(L + 6 * L_SLOT + w * 4096 + g * 1024 + lane * 16) = f32x4{af[1][4 * g], af[1][4 * g + 1], af[1][4 * g + 2], af[1][4 * g + 3]};
        BAR();
        for (int g = 0; g < 4; ++g) {
          const f32x4 pf = *(LAS f32x4 *)(L + 6 * L_SLOT + (w ^ 2) * 4096 + g * 1024 + lane * 16);
          for (int e = 0; e < 2; ++e) {
            const int j = 4 * g + 2 * e;
            const f32x2 y = (f32x2{af[0][j], af[0][j + 1]} + f32x2{pf[2 * e], pf[2 * e + 1]}) * rs2 + bx[2 * g + e];
            const f32x2 t = y * -LOG2E;
            const f32x2 d = f32x2{__builtin_amdgcn_exp2f(t.x), __builtin_amdgcn_exp2f(t.y)} + 1.0f;
            sg[2 * g + e] = y * f32x2{__builtin_amdgcn_rcpf(d.x), __builtin_amdgcn_rcpf(d.y)};
            asm volatile("" : "+v"(sg[2 * g + e]));
          }
        }
        STAMP(14);
        if (!o_early) {
          if (!__all(look == E)) poll_all(gws, ofo, E);
          for (int j = 0; j < OB; ++j) oa[j] = ld128_sc1(rws, lane * 16, soo + j * 1024);
        }
        __builtin_amdgcn_sched_barrier(0);
      }
      if constexpr (i >= 16 && (i & 7) == 0 && i != 56) asm volatile("s_barrier" ::: "memory");
      if constexpr (i == 0) STAMP(11);
      if constexpr (i == 0) {
        for (int j = 0; j < RD; ++j) ring[j] = ld128_sc1(rws, lane * 16, soh + h_step(j) * 1024);
        __builtin_amdgcn_sched_barrier(0);
        for (int c2 = 0; c2 < 2; ++c2) { bb[c2] = bb0[c2] + 4 * L_SLOT; asm volatile("" : "+v"(bb[c2])); for (int j = 0; j < PD; ++j) bf[j][c2] = trfrag((lchar *)(size_t)bb[c2] + j * 2048, 1024); }
      }
      static_assert(28 - RD - 6 < 16, "no look between the o stores and their counted wait");
      if constexpr (i == 28 - RD - 6) look = ld_flag(gws, hfb);
      if constexpr (i == 28 - RD) { if (!__all(look == E)) poll_all(gws, hfb, E); }
      if constexpr (i == 44) {
        for (int g = 0; g < 4; ++g) b2v[g] = *(const GAS u32x2 *)(gb2 + (col0 + 8 * g + 4 * h) * 2);
        sg0 = ld128_sc1(rws, WS_STAT + row * 16, 0);
      }
      if constexpr (i == 40) look = ld_flag(gws, ofo);
      if constexpr (i == 47) { o_early = __all(look == E); soe = o_early ? soo : WS_END; }
      if constexpr (i >= 48 && i < 48 + OB) oa[i - 48] = ld128_sc1(rws, lane * 16, soe + (i - 48) * 1024);
      if constexpr (i == 50) look = ld_flag(gws, ofo);
      if constexpr (i == 52) {
        u32x4 g4 = sg0;
        if (!__all(g4[1] == E && g4[3] == E)) g4 = stat_settle(g4, rws, WS_STAT + row * 16, E);
        const float rs1 = __builtin_amdgcn_rcpf(asf(g4[2]));
        rs2 = rs1 * rs1;
        for (int g = 0; g < 4; ++g) for (int e = 0; e < 2; ++e) { bx[2 * g + e] = f32x2{bf2f(b2v[g][e]), bfhi(b2v[g][e])}; asm volatile("" : "+v"(bx[2 * g + e])); }
      }
      if constexpr (i == 60) {
        for (int g = 0; g < 4; ++g) {
          const u32 r64 = 32 * rh + (lane & 31);
          const u32x2 xr = *(LAS u32x2 *)(L + L_XT + r64 * 128 + 16 * ((4 * kg + g) ^ ((r64 >> 1) & 7)) + 8 * h);
          for (int e = 0; e < 2; ++e) { xx[2 * g + e] = f32x2{bf2f(xr[e]), bfhi(xr[e])}; asm volatile("" : "+v"(xx[2 * g + e])); }
        }
      }
      if constexpr (i == 16) { if (att) o_stage(); }
      if constexpr (i == 16 + OFD) {
        if (att) { vmwait<OFD>(); if (lane < 2) st_flag(gws, WS_OF + ((8 * rga + 2 * w + lane) * 16 + head) * 64, E); }
      }
      u32x4 a;
      if constexpr (i < 56) a = ring[i % RD]; else a = oa[i - 56];
      if constexpr (i + PD < 72) {
        constexpr int n = i + PD;
        if constexpr ((n & 3) == 0) for (int c2 = 0; c2 < 2; ++c2) { bb[c2] = bb0[c2] + (((n >> 2) + 4) % NSLOT) * L_SLOT; asm volatile("" : "+v"(bb[c2])); }
        for (int c2 = 0; c2 < 2; ++c2) bf[n % (PD + 1)][c2] = trfrag((lchar *)(size_t)bb[c2] + (n & 3) * 2048, 1024);
      }
      if constexpr (i < 56) { af[0] = mfma(bf[i % (PD + 1)][0], a, af[0]); af[1] = mfma(bf[i % (PD + 1)][1], a, af[1]); asm volatile("" : "+v"(af[0]), "+v"(af[1])); }
      else { aa[0] = mfma(bf[i % (PD + 1)][0], a, aa[0]); aa[1] = mfma(bf[i % (PD + 1)][1], a, aa[1]); asm volatile("" : "+v"(aa[0]), "+v"(aa[1])); }
      if constexpr (i + RD < 56) ring[i % RD] = ld128_sc1(rws, lane * 16, soh + h_step(i + RD) * 1024);
      if constexpr (i >= 56 && i - 56 + OB < 16) oa[i - 56 + OB] = ld128_sc1(rws, lane * 16, soo + (i - 56 + OB) * 1024);
      __builtin_amdgcn_sched_barrier(0);
    });
    STAMP(12);
    for (int g = 0; g < 4; ++g)
      *(LAS f32x4 *)(L + 8 * L_SLOT + w * 4096 + g * 1024 + lane * 16) = f32x4{aa[1][4 * g], aa[1][4 * g + 1], aa[1][4 * g + 2], aa[1][4 * g + 3]};
    BAR();
    u32x2 o2[4];
    for (int g = 0; g < 4; ++g) {
      const f32x4 pa = *(LAS f32x4 *)(L + 8 * L_SLOT + (w ^ 2) * 4096 + g * 1024 + lane * 16);
      for (int e = 0; e < 2; ++e) {
        const int i = 4 * g + 2 * e;
        const f32x2 r = (f32x2{aa[0][i], aa[0][i + 1]} + f32x2{pa[2 * e], pa[2 * e + 1]}) * sg[2 * g + e] + xx[2 * g + e];
        o2[g][e] = pk2(r.x, r.y);
      }
    }
    const rsrc_t rout = __builtin_amdgcn_make_buffer_rsrc((void *)gout, 0, 2048 * XROW, 0x00020000);
    for (int g = 0; g < 4; g += 2) {
      auto rx0 = __builtin_amdgcn_permlane32_swap(o2[g].x, o2[g + 1].x, false, false);
      auto ry0 = __builtin_amdgcn_permlane32_swap(o2[g].y, o2[g + 1].y, false, false);
      __builtin_amdgcn_raw_buffer_store_b128(u32x4{rx0[0], ry0[0], rx0[1], ry0[1]}, rout, row * XROW + (col0 + 8 * (g + h)) * 2, 0, OUT_AUX);
    }
    STAMP(13);
    STAMP(15);
    STAMP_OUT();
  }
}

extern "C" void kernel_launch(void *const *d_in, const int *in_sizes, int n_in, void *d_out, int out_size, void *d_ws, size_t ws_size, hipStream_t stream) {
  fused_hedgehog_ffn<<<256, 512, 0, stream>>>((const char *)d_in[0], (const char *)d_in[1], (const char *)d_in[2], (const char *)d_in[3], (const char *)d_in[5],
                                              (const char *)d_in[6], (const char *)d_in[7], (const char *)d_in[8], (char *)d_out, (char *)d_ws);
}
```

```cpp
#include <hip/hip_runtime.h>
#include <hip/hip_bf16.h>
#include <stdint.h>
#include <type_traits>

typedef unsigned u32;
typedef u32 u32x4 __attribute__((ext_vector_type(4)));
typedef u32 u32x2 __attribute__((ext_vector_type(2)));
typedef float f32x16 __attribute__((ext_vector_type(16)));
typedef float f32x4 __attribute__((ext_vector_type(4)));
typedef float f32x2 __attribute__((ext_vector_type(2)));
typedef __bf16 bf16x2 __attribute__((ext_vector_type(2)));
typedef __bf16 bf16x8 __attribute__((ext_vector_type(8)));
#define LAS __attribute__((address_space(3)))
#define GAS __attribute__((address_space(1)))
#define DI __device__ __forceinline__
typedef __amdgpu_buffer_rsrc_t rsrc_t;
typedef LAS char lchar;

namespace {
constexpr int DM = 448, DF = 1792;
constexpr u32 XROW = DM * 2;
constexpr u32 QKVROW = 3 * DM * 2;
constexpr u32 W1ROW = DF * 2;
constexpr u32 WS_EPOCH = 0;
constexpr u32 WS_STAT = 4096;
constexpr u32 WS_HFA = 98304;
constexpr u32 WS_HFB = WS_HFA + 57344;
constexpr u32 WS_OF = WS_HFB + 57344;
constexpr u32 WS_KVP = 294912;
constexpr u32 WS_H = WS_KVP + (1u << 20);
constexpr u32 WS_O = WS_H + 64 * 112 * 1024;
constexpr u32 WS_END = WS_O + 64 * 32 * 1024;
constexpr u32 L_SLOT = 16384, NSLOT = 9;
constexpr u32 A_SLOT = 49152, A_WOFF = 32768;
constexpr u32 L_MISC = NSLOT * L_SLOT;
constexpr u32 L_WPHI = L_MISC;
constexpr u32 L_ZERO = L_MISC + 2048;
constexpr u32 L_E = L_MISC + 2112;
constexpr u32 L_BRDY = L_MISC + 2128;
constexpr u32 L_DUMMY = L_MISC + 2304;
constexpr u32 L_CSP = L_MISC + 2560;
constexpr u32 L_TOK = L_CSP + 512;
constexpr u32 L_KVF = L_TOK + 1024;
constexpr u32 L_XT = L_KVF + 4096;
constexpr u32 L_END = L_XT + 8192;
constexpr u32 LDS_BYTES = 163840;
static_assert(L_END <= LDS_BYTES, "lds");
constexpr int SPIN_CAP = 1 << 16;
constexpr float LOG2E = 1.4426950408889634f;
constexpr int POLL_SLEEP = 3;
constexpr int RING = 10;
constexpr int BPD = 3;
constexpr int OB = 8;
constexpr int OFD = 10;

template <int B, int E, class F> DI void sfor(F &&f) {
  if constexpr (B < E) { f(std::integral_constant<int, B>{}); sfor<B + 1, E>(f); }
}
DI u32 rfl(u32 v) { return __builtin_amdgcn_readfirstlane(v); }
DI f32x16 mfma(u32x4 a, u32x4 b, f32x16 c) {
  return __builtin_amdgcn_mfma_f32_32x32x16_bf16(__builtin_bit_cast(bf16x8, a), __builtin_bit_cast(bf16x8, b), c, 0, 0, 0);
}
DI u32 pk2(float lo, float hi) { f32x2 p = {lo, hi}; return __builtin_bit_cast(u32, __builtin_convertvector(p, bf16x2)); }
DI float dot2(u32 a, u32 b, float c) { return __builtin_amdgcn_fdot2_f32_bf16(__builtin_bit_cast(bf16x2, a), __builtin_bit_cast(bf16x2, b), c, false); }
DI float bf2f(u32 lo16) { return __builtin_bit_cast(float, lo16 << 16); }
DI float bfhi(u32 v) { return __builtin_bit_cast(float, v & 0xffff0000u); }
DI float asf(u32 v) { return __builtin_bit_cast(float, v); }
DI f32x2 both_halves(float v) {
  const u32 a = __builtin_bit_cast(u32, v);
  auto r = __builtin_amdgcn_permlane32_swap(a, a, false, false);
  return f32x2{asf(r[0]), asf(r[1])};
}
DI u32x4 pack8(const f32x16 &x, int s) { return u32x4{pk2(x[8 * s], x[8 * s + 1]), pk2(x[8 * s + 2], x[8 * s + 3]), pk2(x[8 * s + 4], x[8 * s + 5]), pk2(x[8 * s + 6], x[8 * s + 7])}; }
DI u32x2 trd(lchar *p) {
  typedef short v4i16 __attribute__((ext_vector_type(4)));
  return __builtin_bit_cast(u32x2, __builtin_amdgcn_ds_read_tr16_b64_v4i16((LAS v4i16 *)p));
}
DI u32x4 trfrag(lchar *p, int tstride) { u32x2 a = trd(p), b = trd(p + tstride); return u32x4{a.x, a.y, b.x, b.y}; }
DI float relu(float v) { const int b = __builtin_bit_cast(int, v); return __builtin_bit_cast(float, b > 0 ? b : 0); }
DI f32x16 zero16() { f32x16 z; for (int i = 0; i < 16; ++i) z[i] = 0.f; return z; }
#define BAR() asm volatile("s_waitcnt lgkmcnt(0)\n\ts_barrier" ::: "memory")
template <int N> DI void vmwait() { asm volatile("s_waitcnt vmcnt(%0)" ::"i"(N) : "memory"); }
DI u32x4 ld128_sc1(rsrc_t r, u32 voff, u32 soff) { return __builtin_amdgcn_raw_buffer_load_b128(r, voff, soff, 16); }
DI void st128_sc1(u32x4 v, rsrc_t r, u32 voff, u32 soff) {
  __builtin_amdgcn_raw_buffer_store_b128(v, r, voff, soff, 16);
  asm volatile("s_nop 1" ::"v"(v) : "memory");
}
typedef GAS u32 gu32;
DI u32 ld_flag(const char *ws, u32 off) { return __hip_atomic_load((gu32 *)(ws + off), __ATOMIC_RELAXED, __HIP_MEMORY_SCOPE_AGENT); }
DI void st_flag(char *ws, u32 off, u32 v) { __hip_atomic_store((gu32 *)(ws + off), v, __ATOMIC_RELAXED, __HIP_MEMORY_SCOPE_AGENT); }
DI void poll_all(const char *ws, u32 off, u32 E) {
#pragma unroll 1
  for (int spin = 0; spin < SPIN_CAP; ++spin) {
    if (__all(ld_flag(ws, off) == E)) break;
    __builtin_amdgcn_s_sleep(POLL_SLEEP);
  }
  __builtin_amdgcn_fence(__ATOMIC_ACQUIRE, "wavefront");
}
template <u32 LI, u32 SI> DI void dmai(rsrc_t r, u32 voff, u32 sob, u32 ldsb) {
  u32 t;
  asm volatile("s_add_u32 m0, %2, %5\n\ts_add_u32 %0, %1, %6\n\tbuffer_load_dwordx4 %3, %4, %0 offen lds"
               : "=&s"(t) : "s"(sob), "s"(ldsb), "v"(voff), "s"(r), "i"(LI), "i"(SI) : "memory", "m0", "scc");
}
DI void dmar(rsrc_t r, u32 voff, u32 soff, u32 lds) {
  asm volatile("s_mov_b32 m0, %0\n\ts_nop 0\n\tbuffer_load_dwordx4 %1, %2, %3 offen lds" ::"s"(lds), "v"(voff), "s"(r), "s"(soff) : "memory", "m0");
}
template <int CTRL, int RMASK> DI float dpp_add(float v) {
  return v + asf(__builtin_amdgcn_update_dpp(0u, __builtin_bit_cast(u32, v), CTRL, RMASK, 0xf, true));
}
DI float wave_sum63(float v) {
  v = dpp_add<0x111, 0xf>(v); v = dpp_add<0x112, 0xf>(v); v = dpp_add<0x114, 0xf>(v); v = dpp_add<0x118, 0xf>(v);
  v = dpp_add<0x142, 0xa>(v); v = dpp_add<0x143, 0xc>(v);
  return v;
}
struct Early { u32 ev; u32x4 xa, xb; };
DI Early early_issue(char *ws, rsrc_t rx, int row0, int lane) {
  Early e;
  e.ev = ld_flag(ws, WS_EPOCH);
  const u32 vo = lane < 56 ? (u32)lane * 16 : 0x7ffffff0u;
  e.xa = __builtin_amdgcn_raw_buffer_load_b128(rx, vo, row0 * XROW, 0); e.xb = __builtin_amdgcn_raw_buffer_load_b128(rx, vo, (row0 + 1) * XROW, 0);
  return e;
}
DI u32 early_finish(const Early &e, rsrc_t rws, int row0, int lane) {
  const u32 E = rfl(e.ev) + 1, ONES = 0x3F803F80u;
  float s[2], q[2];
  for (int r = 0; r < 2; ++r) {
    const u32x4 &x = r ? e.xb : e.xa;
    s[r] = dot2(x[0], ONES, 0.f); q[r] = dot2(x[0], x[0], 0.f);
    for (int d = 1; d < 4; ++d) { s[r] = dot2(x[d], ONES, s[r]); q[r] = dot2(x[d], x[d], q[r]); }
    s[r] = wave_sum63(s[r]); q[r] = wave_sum63(q[r]);
    const float mean = s[r] * (1.0f / DM);
    const float sig = __builtin_amdgcn_sqrtf(fmaxf(q[r] * (1.0f / DM) - mean * mean, 0.f) + 1e-5f);
    const u32 vo = lane == 63 ? WS_STAT + (row0 + r) * 16 : 0x7ffffff0u;
    __builtin_amdgcn_raw_buffer_store_b64(u32x2{__builtin_bit_cast(u32, mean), E}, rws, vo, 0, 16);
    __builtin_amdgcn_raw_buffer_store_b64(u32x2{__builtin_bit_cast(u32, sig), E}, rws, vo + 8, 0, 16);
  }
  return E;
}
DI u32x4 stat_settle(u32x4 g, rsrc_t rws, u32 vostat, u32 E) {
#pragma unroll 1
  for (int spin = 0; spin < SPIN_CAP && !__all(g[1] == E && g[3] == E); ++spin) { __builtin_amdgcn_s_sleep(1); g = ld128_sc1(rws, vostat, 0); }
  return g;
}
struct RowStat { float rstd; u32x4 xaug; };
DI RowStat row_stats(u32x4 g, int h) {
  const float m = -asf(g[0]), sig = asf(g[2]);
  RowStat r; r.rstd = __builtin_amdgcn_rcpf(sig);
  const float ml = m - bfhi(pk2(m, m)), sl = sig - bfhi(pk2(sig, sig));
  const u32 mm = pk2(m, ml), ss = pk2(sig, sl);
  r.xaug = h ? u32x4{0, 0, 0, 0} : u32x4{mm, mm, ss, 0};
  return r;
}
DI u32x4 waug(float cs, u32 bb) {
  const u32 hh = pk2(cs, cs); const float cl = cs - bfhi(hh);
  return u32x4{hh, pk2(cl, cl), bb, 0};
}
DI float cs_read(lchar *L, int t, int lane) { return *(LAS float *)(L + L_CSP + (t * 32 + (lane & 31)) * 4); }

template <bool SC1 = false> DI void touch(rsrc_t r, u32 voff, u32 lb) {
  if constexpr (SC1) asm volatile("s_mov_b32 m0, %0\n\ts_nop 0\n\tbuffer_load_dword %1, %2, 0 offen sc1 lds" ::"s"(lb + L_DUMMY), "v"(voff), "s"(r) : "memory", "m0");
  else asm volatile("s_mov_b32 m0, %0\n\ts_nop 0\n\tbuffer_load_dword %1, %2, 0 offen lds" ::"s"(lb + L_DUMMY), "v"(voff), "s"(r) : "memory", "m0");
}
constexpr u32 CODE_BYTES = 50 * 1024, CODE_PIECES = (CODE_BYTES + 8191) / 8192;
DI void warm_up(unsigned long long pc, rsrc_t rws, const char *gw2, const char *gwo, int xcd, int wv, int lane, u32 lb) {
  {
    const rsrc_t rc = __builtin_amdgcn_make_buffer_rsrc((void *)(pc & ~127ull), 0, CODE_BYTES, 0x00020000);
    if (wv < (int)CODE_PIECES) touch(rc, wv * 8192 + lane * 128, lb);
  }
  const int g = xcd * 16 + wv;
  if (g < 22) touch<true>(rws, WS_HFA + g * 8192 + lane * 128, lb);
  {
    const rsrc_t r2 = __builtin_amdgcn_make_buffer_rsrc((void *)gw2, 0, DF * XROW, 0x00020000), ro = __builtin_amdgcn_make_buffer_rsrc((void *)gwo, 0, DM * XROW, 0x00020000);
    touch(r2, g * 8192 + lane * 128, lb);
    if (g < 68) touch(r2, (128 + g) * 8192 + lane * 128, lb);
    else if (g < 117) touch(ro, (g - 68) * 8192 + lane * 128, lb);
  }
}
template <bool ATT, class First, class Mid>
DI void gemmA_compute(lchar *L, int w, int lane, f32x16 (&acc)[2][ATT ? 3 : 4], First &&first, Mid &&mid) {
  constexpr int T = ATT ? 3 : 4, ROWB = ATT ? 64 : 256;
  const int h = lane >> 5, blk = (lane >> 4) & 1, q = (lane & 15) >> 2, p = lane & 3;
  const u32 lb = (u32)(size_t)L;
  u32 wb0[T], xb0[4], wb[T], xb[4], cb0, cb;
  for (int t = 0; t < T; ++t) wb0[t] = lb + A_WOFF + (ATT ? t * 4096 + (8 * h + q) * 64 : (8 * h + q) * 256 + 64 * (t ^ q)) + 32 * blk + 8 * p;
  for (int u = 0; u < 4; ++u) xb0[u] = lb + w * 8192 + (lane & 31) * 128 + 16 * ((2 * u + h) ^ ((lane >> 1) & 7));
  const int tw = w < T ? w : T - 1;
  cb0 = lb + A_WOFF + (ATT ? tw * 4096 + (8 * h + q) * 64 : (8 * h + q) * 256 + 64 * (tw ^ q)) + 32 * blk + 8 * p;
  f32x2 ca = {0.f, 0.f};
  for (int r = 0; r < 2; ++r) for (int t = 0; t < T; ++t) acc[r][t] = zero16();
  u32x4 xf[2][2], wf[2][T], cf[2];
  sfor<0, 29>([&](auto I) {
    constexpr int i = I, c = i >> 2, u = i & 3;
    if constexpr (i == 0) first();
    if constexpr (u == 0 && i < 28) {
      BAR();
      constexpr u32 so = (c % 3) * A_SLOT;
      for (int t = 0; t < T; ++t) { wb[t] = wb0[t] + so; asm volatile("" : "+v"(wb[t])); }
      for (int j = 0; j < 4; ++j) { xb[j] = xb0[j] + so; asm volatile("" : "+v"(xb[j])); }
      cb = cb0 + so; asm volatile("" : "+v"(cb));
    }
    if constexpr (i < 28) {
      cf[i & 1] = trfrag((lchar *)(size_t)cb + 16 * u * ROWB, 4 * ROWB);
      for (int r = 0; r < 2; ++r) xf[i & 1][r] = *(LAS u32x4 *)((lchar *)(size_t)xb[u] + r * 4096);
      for (int t = 0; t < T; ++t) wf[i & 1][t] = trfrag((lchar *)(size_t)wb[t] + 16 * u * ROWB, 4 * ROWB);
    }
    if constexpr (i == 20) mid();
    if constexpr (i > 0) {
      constexpr int j = (i - 1) & 1;
      for (int t = 0; t < T; ++t)
        for (int r = 0; r < 2; ++r) acc[r][t] = (ATT && t == 1) ? mfma(xf[j][r], wf[j][t], acc[r][t]) : mfma(wf[j][t], xf[j][r], acc[r][t]);
      for (int d = 0; d < 4; ++d) ca += f32x2{bf2f(cf[j][d]), bfhi(cf[j][d])};
      asm volatile("" : "+v"(ca));
      for (int m = 0; m < 2 * T; ++m) { __builtin_amdgcn_sched_group_barrier(0x8, 1, 0); __builtin_amdgcn_sched_group_barrier(0x100, 2, 0); __builtin_amdgcn_sched_group_barrier(0x2, 2, 0); }
    }
    __builtin_amdgcn_sched_barrier(0);
  });
  { const f32x2 a = both_halves(ca.x + ca.y); if (lane < 32 && w < T) *(LAS float *)(L + L_CSP + (w * 32 + lane) * 4) = a.x + a.y; }
}
constexpr int w2_row(int st) { return st < 7 ? 128 * st : 128 * (st - 7) + 64; }
constexpr int h_step(int i) { return i < 28 ? 8 * (i >> 2) + (i & 3) : 8 * ((i - 28) >> 2) + 4 + (i & 3); }
struct StageCtx { rsrc_t rw2, rwo; u32 vob, so2, soo, ld; };
template <int ST> DI void stage_dma(const StageCtx &c) {
  sfor<0, 4>([&](auto I) {
    constexpr u32 i = I, dst = ((ST + 4) % NSLOT) * L_SLOT + i * 1024;
    dmai<dst, (w2_row(ST) + 8 * i) * XROW>(c.rw2, c.vob, c.so2, c.ld);
  });
}
template <int ST> DI void stage_load(const StageCtx &c, u32x4 (&br)[36]) {
  sfor<0, 4>([&](auto I) {
    constexpr u32 i = I;
    if constexpr (ST < 14) br[4 * (ST - 9) + i] = __builtin_amdgcn_raw_buffer_load_b128(c.rw2, c.vob, c.so2 + (w2_row(ST) + 8 * i) * XROW, 0);
    else br[4 * (ST - 9) + i] = __builtin_amdgcn_raw_buffer_load_b128(c.rwo, c.vob, c.soo + (56 * (ST - 14) + (i == 3 ? 20 : 8 * i)) * XROW, 0);
  });
}
template <bool ATT>
DI void gemmA_loader(lchar *L, rsrc_t rx, rsrc_t rw, u32 sox, u32 sow, int lw, int lane, const StageCtx &sc, u32x4 (&br)[36], rsrc_t rp, u32 wpo) {
  constexpr int NW = ATT ? 3 : 4, N = 8 + NW;
  const u32 lb = (u32)(size_t)L;
  const u32 vox0 = (lane >> 3) * XROW + 16 * ((lane & 7) ^ ((lane >> 4) & 3)), vox1 = vox0 ^ 64;
  const u32 ldx = rfl(lb + lw * 8192);
  const u32 vow = ATT ? (lane >> 2) * QKVROW + ((lane & 3) == 3 ? 40 : 16 * (lane & 3)) : (lane >> 4) * W1ROW + 16 * ((lane & 15) ^ ((lane >> 4) << 2));
  const u32 swb = rfl(sow + 16 * lw * (ATT ? QKVROW : W1ROW)), ldw = rfl(lb + A_WOFF + lw * (ATT ? 1024 : 4096));
  auto issue = [&](auto C) {
    constexpr u32 c = C, so = (c % 3) * A_SLOT;
    sfor<0, NW>([&](auto I) {
      constexpr u32 i = I;
      if constexpr (ATT) dmai<so + i * 4096, 64 * c * QKVROW + (i == 0 ? 896 : i == 1 ? 1792 : 0)>(rw, vow, swb, ldw);
      else dmai<so + i * 1024, (64 * c + 4 * i) * W1ROW>(rw, vow, swb, ldw);
    });
    sfor<0, 8>([&](auto I) { constexpr u32 i = I; dmai<so + i * 1024, 8 * i * XROW + 128 * c>(rx, (i & 1) ? vox1 : vox0, sox, ldx); });
  };
  sfor<0, 7>([&](auto C) {
    constexpr int c = C;
    if constexpr (ATT && c == 1) { if (lw < 2) vmwait<N + 1>(); else vmwait<N>(); }
    else vmwait<(c == 6 ? 12 : N)>();
    asm volatile("s_barrier" ::: "memory");
    if constexpr (ATT && c == 0) {
      if (lw < 2) dmar(rp, lane * 16 + lw * 1024, wpo, lb + L_WPHI + lw * 1024);
    }
    if constexpr (c + 2 < 7) issue(std::integral_constant<int, c + 2>{});
    else { stage_load<9 + 3 * (c - 5)>(sc, br); stage_load<10 + 3 * (c - 5)>(sc, br); stage_load<11 + 3 * (c - 5)>(sc, br); }
  });
}

DI void feature_map(const f32x16 &P, float rstd, int h, u32x4 (&pf)[2][2]) {
  const float c = rstd * LOG2E;
  float mx = fmaxf(fmaxf(P[0], P[1]), P[2]), mn = fminf(fminf(P[0], P[1]), P[2]);
  for (int i = 3; i < 15; i += 2) { mx = fmaxf(fmaxf(mx, P[i]), P[i + 1]); mn = fminf(fminf(mn, P[i]), P[i + 1]); }
  mx = fmaxf(mx, P[15]); mn = fminf(mn, P[15]);
  { f32x2 a = both_halves(mx), b = both_halves(mn); mx = fmaxf(a.x, a.y); mn = fminf(b.x, b.y); }
  const float mxc = -mx * c, mnc = mn * c;
  f32x16 ep, en;
  for (int i = 0; i < 16; i += 2) {
    const f32x2 p2 = {P[i], P[i + 1]}, ap = p2 * c + mxc, an = mnc - p2 * c;
    ep[i] = __builtin_amdgcn_exp2f(ap.x); ep[i + 1] = __builtin_amdgcn_exp2f(ap.y);
    en[i] = __builtin_amdgcn_exp2f(an.x); en[i + 1] = __builtin_amdgcn_exp2f(an.y);
  }
  f32x2 sp2 = {ep[0], ep[1]}, sn2 = {en[0], en[1]};
  for (int i = 2; i < 12; i += 2) { sp2 += f32x2{ep[i], ep[i + 1]}; sn2 += f32x2{en[i], en[i + 1]}; }
  const float keep = h ? 0.f : 1.f;
  sp2 += (f32x2{ep[12], ep[13]} + f32x2{ep[14], ep[15]}) * keep;
  sn2 += (f32x2{en[12], en[13]} + f32x2{en[14], en[15]}) * keep;
  const f32x2 a = both_halves(sp2.x + sp2.y), b = both_halves(sn2.x + sn2.y);
  const float rp = __builtin_amdgcn_rcpf(a.x + a.y), rn = __builtin_amdgcn_rcpf(b.x + b.y);
  for (int i = 0; i < 16; i += 2) {
    const f32x2 u = f32x2{ep[i], ep[i + 1]} * rp, v = f32x2{en[i], en[i + 1]} * rn;
    ep[i] = u.x; ep[i + 1] = u.y; en[i] = v.x; en[i + 1] = v.y;
  }
  pf[0][0] = pack8(ep, 0); pf[0][1] = pack8(ep, 1); pf[1][0] = pack8(en, 0); pf[1][1] = pack8(en, 1);
}

constexpr int refill_end(int k) { return k < 0 ? 0 : k == 0 ? 7 : k == 1 ? 14 : k == 2 ? 20 : k == 3 ? 26 : k == 4 ? 32 : 36; }
constexpr bool refill_ok() { for (int k = 0; k < 6; ++k) for (int p = refill_end(k - 1); p < refill_end(k); ++p) { const int g = 9 + p / 4, s = 4 + 2 * k; if (s < g - 8 || 4 * (s + 2) > 4 * g - 2) return false; } return true; }
static_assert(refill_ok(), "refill schedule");
}

__global__ void __launch_bounds__(512, 2) fused_hedgehog_ffn(const char *__restrict__ gx, const char *__restrict__ gwqkv, const char *__restrict__ gwphi, const char *__restrict__ gwo,
                                                              const char *__restrict__ gw1, const char *__restrict__ gb1, const char *__restrict__ gw2, const char *__restrict__ gb2,
                                                              char *__restrict__ gout, char *__restrict__ gws) {
  __shared__ __attribute__((aligned(1024))) char smem[LDS_BYTES];
  lchar *L = (lchar *)smem;
  unsigned long long pc0;
  asm volatile("s_getpc_b64 %0" : "=s"(pc0));
  asm volatile("" ::"s"(gx), "s"(gwqkv), "s"(gwphi), "s"(gwo), "s"(gw1), "s"(gb1), "s"(gw2), "s"(gb2), "s"(gout), "s"(gws));
  const int tid = threadIdx.x, lane = tid & 63, w = rfl(tid >> 6);
  const int b = blockIdx.x, xcd = b & 7;
  int slot = b >> 3;
  const rsrc_t rx = __builtin_amdgcn_make_buffer_rsrc((void *)gx, 0, 2048 * XROW, 0x00020000);
  if (__builtin_expect(w >= 4 && slot < 30, 1)) {
    const bool a = slot < 16;
    const u32 lw = w - 4, lb = (u32)(size_t)L, hi = slot >= 23;
    const u32 rg0 = (xcd & 6) + (a ? slot >> 3 : hi), sox = (256 * rg0 + 64 * lw) * XROW, ldx = lb + lw * 8192;
    const u32 vox = (lane >> 3) * XROW + 16 * ((lane & 7) ^ ((lane >> 4) & 3));
    const rsrc_t rw = __builtin_amdgcn_make_buffer_rsrc((void *)(a ? gwqkv : gw1), 0, a ? DM * QKVROW : DM * W1ROW, 0x00020000);
    const u32 vow = a ? (lane >> 2) * QKVROW + ((lane & 3) == 3 ? 40 : 16 * (lane & 3)) : (lane >> 4) * W1ROW + 16 * ((lane & 15) ^ ((lane >> 4) << 2));
    const u32 swb = a ? 56 * (8 * (xcd & 1) + (slot & 7)) + 16 * lw * QKVROW : 256 * (7 * (xcd & 1) + slot - 16 - 7 * hi) + 16 * lw * W1ROW;
    const u32 ldw = lb + A_WOFF + lw * (a ? 1024 : 4096), ls = a ? 4096 : 1024, cs = a ? 64 * QKVROW : 64 * W1ROW;
    sfor<0, 2>([&](auto C) {
      constexpr u32 c = C;
      dmar(rw, vow, swb + c * cs + (a ? 896 : 0), ldw + c * A_SLOT);
      dmar(rw, vow, swb + c * cs + (a ? 1792 : 4 * W1ROW), ldw + c * A_SLOT + ls);
      dmar(rw, vow, swb + c * cs + (a ? 0 : 8 * W1ROW), ldw + c * A_SLOT + 2 * ls);
      if (!a) dmar(rw, vow, swb + c * cs + 12 * W1ROW, ldw + c * A_SLOT + 3 * ls);
      sfor<0, 8>([&](auto I) { constexpr u32 i = I; dmai<c * A_SLOT + i * 1024, 8 * i * XROW + 128 * c>(rx, (i & 1) ? vox ^ 64 : vox, sox, ldx); });
    });
  }
  asm volatile("" : "+s"(slot));
  const int h = lane >> 5;
  const rsrc_t rws = __builtin_amdgcn_make_buffer_rsrc((void *)gws, 0, WS_END, 0x00020000);
  const int tc = slot >= 16 ? slot - 16 : slot + 16;
  const bool has_tile = tc < 28;
  const u32 rt = tc / 7, cc = tc % 7;
  const bool att = slot < 16, ffn = !att && slot < 30;
  const int head = 8 * (xcd & 1) + (slot & 7);
  const int jf = slot - 16, ct = 7 * (xcd & 1) + (jf >= 7 ? jf - 7 : jf);
  const int rga = 2 * (xcd >> 1) + (att ? (slot >> 3) : (jf >= 7));

  if (w >= 4) {
    const int lw = w - 4;
    StageCtx sc;
    sc.rw2 = __builtin_amdgcn_make_buffer_rsrc((void *)gw2, 0, DF * XROW, 0x00020000);
    sc.rwo = __builtin_amdgcn_make_buffer_rsrc((void *)gwo, 0, DM * XROW, 0x00020000);
    sc.vob = (lane >> 3) * XROW + 16 * ((lane & 7) ^ (((lane >> 4) & 1) << 2));
    sc.so2 = rfl((896 * (lw >> 1) + 32 * (lw & 1)) * XROW + 128 * cc);
    sc.soo = rfl(28 * (8 * (lw >> 1) + (lw & 1)) * XROW + 128 * cc);
    sc.ld = rfl((u32)(size_t)L + (lw >> 1) * 8192 + (lw & 1) * 4096);
    u32x4 br[36];
    auto resid_dma = [&]() {
      const u32 vo = (lane >> 3) * XROW + 16 * ((lane & 7) ^ ((lane >> 4) & 3)), so = rfl((256 * xcd + 64 * rt + 16 * lw) * XROW + 128 * cc), ld = rfl((u32)(size_t)L + L_XT + lw * 2048);
      dmai<0, 0>(rx, vo, so, ld);
      dmai<1024, 8 * XROW>(rx, vo ^ 64, so, ld);
    };
    if (att) {
      const rsrc_t rw = __builtin_amdgcn_make_buffer_rsrc((void *)gwqkv, 0, DM * QKVROW, 0x00020000);
      gemmA_loader<true>(L, rx, rw, rfl((256 * rga + 64 * lw) * XROW), rfl(56 * head), lw, lane, sc, br, __builtin_amdgcn_make_buffer_rsrc((void *)gwphi, 0, 16 * 28 * 28 * 2, 0x00020000), rfl(head * 1568));
      BAR();
      const u32 E = rfl(*(LAS u32 *)(L + L_E));
      if (has_tile) { sfor<0, 5>([&](auto S) { stage_dma<S>(sc); }); sfor<5, 7>([&](auto S) { stage_dma<S>(sc); }); resid_dma(); }
      sfor<15, 18>([&](auto S) { stage_load<S>(sc, br); });
      BAR();
      __builtin_amdgcn_s_setprio(3);
      for (int t = 0; t < 2; ++t) {
        lchar *sp = (lchar *)(size_t)(sc.ld + (2 + t) * L_SLOT + lane * 16);
        f32x4 s = *(LAS f32x4 *)sp;
        for (int ww = 1; ww < 4; ++ww) s += *(LAS f32x4 *)(sp + ww * 1024);
        st128_sc1(u32x4{pk2(s.x, s.y), E, pk2(s.z, s.w), E}, rws, lane * 16, WS_KVP + (head * 8 + rga) * 8192 + (t * 4 + lw) * 1024);
      }
      vmwait<24>();
      if (lane == 0) *(LAS u32 *)(L + L_BRDY + 4 * lw) = E;
      poll_all(gws, WS_KVP + (head * 8 + (lane & 7)) * 8192 + (4 + lw) * 1024 + 63 * 16 + 12, E);
      f32x2 ks[2][2];
#pragma unroll 1
      for (int spin = 0; spin < SPIN_CAP; ++spin) {
        u32x4 part[2][8];
        for (int t = 0; t < 2; ++t) for (int r = 0; r < 8; ++r) part[t][r] = ld128_sc1(rws, lane * 16, WS_KVP + (head * 8 + r) * 8192 + (t * 4 + lw) * 1024);
        u32 ta = E, to = E;
        for (int t = 0; t < 2; ++t) {
          ks[t][0] = f32x2{0.f, 0.f}; ks[t][1] = f32x2{0.f, 0.f};
          for (int r = 0; r < 8; ++r) {
            ks[t][0] += f32x2{bf2f(part[t][r][0]), bfhi(part[t][r][0])}; ks[t][1] += f32x2{bf2f(part[t][r][2]), bfhi(part[t][r][2])};
            ta &= part[t][r][1] & part[t][r][3]; to |= part[t][r][1] | part[t][r][3];
          }
        }
        if (__all(ta == E && to == E)) break;
        __builtin_amdgcn_s_sleep(POLL_SLEEP);
      }
      for (int t = 0; t < 2; ++t) {
        u32x2 pk = {pk2(ks[t][0].x, ks[t][0].y), pk2(ks[t][1].x, ks[t][1].y)};
        if (lw == 3 && h) pk = u32x2{0, 0};
        *(LAS u32x2 *)(L + L_KVF + ((t * 2 + (lw >> 1)) * 64 + lane) * 16 + (lw & 1) * 8) = pk;
      }
      __builtin_amdgcn_s_setprio(0);
      if (!has_tile) {
        BAR();
        if (xcd == 0 && slot == 12 && lw == 0) {
#pragma unroll 1
          for (int spin = 0; spin < SPIN_CAP; ++spin) {
            bool ok = true;
            for (int i = 0; i < 32; ++i) ok &= ld_flag(gws, WS_STAT + (64 * i + lane) * 16 + 4) == E;
            if (__all(ok)) break;
            __builtin_amdgcn_s_sleep(8);
          }
          if (lane == 0) st_flag(gws, WS_EPOCH, E);
        }
        return;
      }
    } else if (ffn) {
      const rsrc_t rw = __builtin_amdgcn_make_buffer_rsrc((void *)gw1, 0, DM * W1ROW, 0x00020000);
      gemmA_loader<false>(L, rx, rw, rfl((256 * rga + 64 * lw) * XROW), rfl(256 * ct), lw, lane, sc, br, rx, 0);
      BAR();
      asm volatile("s_barrier" ::: "memory");
      sfor<0, 5>([&](auto S) { stage_dma<S>(sc); });
      vmwait<0>();
      BAR();
      sfor<5, 9>([&](auto S) { stage_dma<S>(sc); });
      resid_dma();
      sfor<15, 18>([&](auto S) { stage_load<S>(sc, br); });
    } else {
      warm_up(pc0, rws, gw2, gwo, xcd, (slot - 30) * 8 + w, lane, (u32)(size_t)L);
      sfor<0, 9>([&](auto S) { stage_dma<S>(sc); });
      resid_dma();
      sfor<9, 18>([&](auto S) { stage_load<S>(sc, br); });
      vmwait<36>();
      BAR();
    }
    const u32 ldv = sc.ld + lane * 16;
    sfor<0, 7>([&](auto K) {
      constexpr int k = K;
      if constexpr (k == 0) vmwait<12>();
      if constexpr (k == 1) vmwait<0>();
      BAR();
      if constexpr (k == 0) { if (att) sfor<7, 9>([&](auto S) { stage_dma<S>(sc); }); }
      sfor<refill_end(k - 1), refill_end(k)>([&](auto Pc) {
        constexpr int pc = Pc;
        *(LAS u32x4 *)((lchar *)(size_t)ldv + ((9 + pc / 4 + 4) % NSLOT) * L_SLOT + (pc & 3) * 1024) = br[pc];
      });
    });
    asm volatile("s_barrier" ::: "memory");
    return;
  }

  const Early early = early_issue(gws, rx, 8 * b + 2 * w, lane);
  u32 E;
  auto first = [&]() {
    E = early_finish(early, rws, 8 * b + 2 * w, lane); if (tid == 0) *(LAS u32 *)(L + L_E) = E; if (tid < 4) *(LAS u32 *)(L + L_BRDY + 4 * tid) = ~E;
  };
  const int rh = w & 1, kg = w >> 1;
  const u32 rb = 8 * xcd + 2 * rt + rh;
  const u32 row = 32 * rb + (lane & 31), col0 = 64 * cc + 32 * kg;
  u32x4 pq[2][2][2];
  const u32 hfa = WS_HFA + (rb * 14 + 7 * kg + ((lane & 7) < 7 ? (lane & 7) : 0)) * 64;
  u32 hlook = 0;
  auto o_stage = [&]() {
    u32x4 kf[2][2];
    for (int t = 0; t < 2; ++t) for (int s = 0; s < 2; ++s) kf[t][s] = *(LAS u32x4 *)(L + L_KVF + ((t * 2 + s) * 64 + lane) * 16);
    f32x16 num[2] = {zero16(), zero16()};
    for (int t = 0; t < 2; ++t) for (int s = 0; s < 2; ++s) for (int r = 0; r < 2; ++r) num[r] = mfma(kf[t][s], pq[r][t][s], num[r]);
    for (int r = 0; r < 2; ++r) {
      const float rc = __builtin_amdgcn_rcpf(both_halves(num[r][12]).x + 1e-6f);
      for (int i = 0; i < 16; i += 2) { const f32x2 v = f32x2{num[r][i], num[r][i + 1]} * rc; num[r][i] = v.x; num[r][i + 1] = v.y; }
      if (!h) { num[r][12] = 0.f; num[r][13] = 0.f; num[r][14] = 0.f; num[r][15] = 0.f; }
      const u32 so = WS_O + ((8 * rga + 2 * w + r) * 32 + 2 * head) * 1024;
      st128_sc1(pack8(num[r], 0), rws, lane * 16, so);
      st128_sc1(pack8(num[r], 1), rws, lane * 16, so + 1024);
    }
  };
  if (att) {
    if (tid < 16) *(LAS u32 *)(L + L_ZERO + 4 * tid) = 0;
    u32x4 idf[2];
    for (int s = 0; s < 2; ++s)
      for (int d = 0; d < 4; ++d) {
        const int k0 = 16 * s + 8 * (d >> 1) + 4 * h + 2 * (d & 1), n = lane & 31;
        idf[s][d] = (n == k0 ? 0x3F80u : 0u) | (n == k0 + 1 ? 0x3F800000u : 0u);
      }
    for (int s = 0; s < 2; ++s) asm volatile("" : "+v"(idf[s]));
    f32x16 acc[2][3]; u32x4 stat[2];
    const u32 vostat = WS_STAT + (256 * rga + 64 * w + (lane & 31)) * 16;
    gemmA_compute<true>(L, w, lane, acc, first, [&]() { for (int r = 0; r < 2; ++r) stat[r] = ld128_sc1(rws, vostat + 512 * r, 0); });
    RowStat st[2];
    for (int r = 0; r < 2; ++r) {
      st[r] = row_stats(stat_settle(stat[r], rws, vostat + 512 * r, E), h);
      *(LAS float *)(L + L_TOK + w * 256 + r * 128 + 4 * (lane & 31)) = st[r].rstd;
    }
    BAR();
    {
      const u32x4 wk = waug(cs_read(L, 0, lane), 0), wv = waug(cs_read(L, 1, lane), 0), wq = waug(cs_read(L, 2, lane), 0);
      for (int r = 0; r < 2; ++r) { acc[r][0] = mfma(wk, st[r].xaug, acc[r][0]); acc[r][1] = mfma(st[r].xaug, wv, acc[r][1]); acc[r][2] = mfma(wq, st[r].xaug, acc[r][2]); }
    }
    u32x4 wp[2];
    {
      const int blk = (lane >> 4) & 1, q = (lane & 15) >> 2, p = lane & 3;
      const u32 col = (blk && p == 3) ? 24 : 16 * blk + 4 * p;
      for (int s = 0; s < 2; ++s) {
        u32x2 r[2];
        for (int t = 0; t < 2; ++t) {
          const int dp = 16 * s + 8 * t + 4 * h + q;
          const u32 a = (dp >= 24 && dp < 28) ? L_ZERO + 8 * p : L_WPHI + (dp >= 28 ? dp - 4 : dp) * 56 + col * 2;
          r[t] = trd(L + a);
        }
        wp[s] = u32x4{r[0].x, r[0].y, r[1].x, r[1].y};
      }
    }
    f32x16 kvp[2] = {zero16(), zero16()};
    {
      f32x16 P[2];
      for (int r = 0; r < 2; ++r) P[r] = mfma(wp[0], pack8(acc[r][0], 0), zero16());
      for (int r = 0; r < 2; ++r) P[r] = mfma(wp[1], pack8(acc[r][0], 1), P[r]);
      u32x4 vf[2][2];
      for (int r = 0; r < 2; ++r) {
        f32x16 v = acc[r][1];
        for (int g = 0; g < 4; ++g) {
          const f32x4 rs = *(LAS f32x4 *)(L + L_TOK + w * 256 + r * 128 + (8 * g + 4 * h) * 4);
          for (int e = 0; e < 4; e += 2) { const f32x2 t2 = f32x2{v[4 * g + e], v[4 * g + e + 1]} * f32x2{rs[e], rs[e + 1]}; v[4 * g + e] = t2.x; v[4 * g + e + 1] = t2.y; }
        }
        for (int s = 0; s < 2; ++s) { vf[r][s] = pack8(v, s); if ((lane & 31) == 24) vf[r][s] = u32x4{0x3F803F80u, 0x3F803F80u, 0x3F803F80u, 0x3F803F80u}; }
      }
      u32x4 pf[2][2][2];
      for (int r = 0; r < 2; ++r) feature_map(P[r], st[r].rstd, h, pf[r]);
      f32x16 Z[2][2];
      for (int r = 0; r < 2; ++r) for (int t = 0; t < 2; ++t) Z[r][t] = mfma(pf[r][t][0], idf[0], zero16());
      for (int r = 0; r < 2; ++r) for (int t = 0; t < 2; ++t) Z[r][t] = mfma(pf[r][t][1], idf[1], Z[r][t]);
      for (int r = 0; r < 2; ++r) for (int s = 0; s < 2; ++s) for (int t = 0; t < 2; ++t) kvp[t] = mfma(pack8(Z[r][t], s), vf[r][s], kvp[t]);
    }
    for (int t = 0; t < 2; ++t)
      for (int g = 0; g < 4; ++g)
        *(LAS f32x4 *)(L + (2 + t) * L_SLOT + (g >> 1) * 8192 + (g & 1) * 4096 + w * 1024 + lane * 16) = f32x4{kvp[t][4 * g], kvp[t][4 * g + 1], kvp[t][4 * g + 2], kvp[t][4 * g + 3]};
    BAR();
    {
      f32x16 P[2];
      for (int r = 0; r < 2; ++r) P[r] = mfma(wp[0], pack8(acc[r][2], 0), zero16());
      for (int r = 0; r < 2; ++r) P[r] = mfma(wp[1], pack8(acc[r][2], 1), P[r]);
      for (int r = 0; r < 2; ++r) {
        feature_map(P[r], st[r].rstd, h, pq[r]);
        for (int t = 0; t < 2; ++t) for (int s = 0; s < 2; ++s) asm volatile("" : "+v"(pq[r][t][s]));
        if (r == 0 && has_tile) hlook = ld_flag(gws, hfa);
        __builtin_amdgcn_sched_barrier(0);
      }
    }
    if (!has_tile) {
      BAR();
      o_stage();
      vmwait<0>();
      if (lane < 2) st_flag(gws, WS_OF + ((8 * rga + 2 * w + lane) * 16 + head) * 64, E);
      return;
    }
  } else if (ffn) {
    u32 bias[4];
    for (int t = 0; t < 4; ++t) bias[t] = *(const GAS unsigned short *)(gb1 + (128 * ct + 32 * t + (lane & 31)) * 2);
    f32x16 acc[2][4]; u32x4 stat[2];
    const u32 vostat = WS_STAT + (256 * rga + 64 * w + (lane & 31)) * 16;
    gemmA_compute<false>(L, w, lane, acc, first, [&]() { for (int r = 0; r < 2; ++r) stat[r] = ld128_sc1(rws, vostat + 512 * r, 0); });
    RowStat st[2];
    for (int r = 0; r < 2; ++r) st[r] = row_stats(stat_settle(stat[r], rws, vostat + 512 * r, E), h);
    BAR();
    for (int t = 0; t < 4; ++t) {
      const u32x4 wa = waug(cs_read(L, t, lane), bias[t] | (bias[t] << 16));
      for (int r = 0; r < 2; ++r) {
        f32x16 a = mfma(wa, st[r].xaug, acc[r][t]);
        for (int i = 0; i < 16; i += 2) { f32x2 v = {relu(a[i]), relu(a[i + 1])}; v *= v; a[i] = v.x; a[i + 1] = v.y; }
        const u32 so = WS_H + ((8 * rga + 2 * w + r) * 112 + 8 * ct + 2 * t) * 1024;
        st128_sc1(pack8(a, 0), rws, lane * 16, so);
        st128_sc1(pack8(a, 1), rws, lane * 16, so + 1024);
      }
      if (t == 2) {
        vmwait<4>();
        if (lane < 2) st_flag(gws, WS_HFA + ((8 * rga + 2 * w + lane) * 14 + ct) * 64, E);
      }
    }
    asm volatile("s_barrier" ::: "memory");
    vmwait<0>();
    if (lane < 2) st_flag(gws, WS_HFB + ((8 * rga + 2 * w + lane) * 14 + ct) * 64, E);
    for (int t = 0; t < 2; ++t) for (int r = 0; r < 2; ++r) for (int s = 0; s < 2; ++s) pq[t][r][s] = u32x4{0, 0, 0, 0};
  } else {
    warm_up(pc0, rws, gw2, gwo, xcd, (slot - 30) * 8 + w, lane, (u32)(size_t)L);
    first();
    for (int t = 0; t < 2; ++t) for (int r = 0; r < 2; ++r) for (int s = 0; s < 2; ++s) pq[t][r][s] = u32x4{0, 0, 0, 0};
  }

  {
    constexpr int RD = RING;
    const u32 soh = WS_H + (rb * 112 + 56 * kg) * 1024, soo = WS_O + (rb * 32 + 16 * kg) * 1024;
    u32x4 ring[RD];
    u32x2 b2v[4]; u32x4 sg0;
    const int blk = (lane >> 4) & 1, q = (lane & 15) >> 2, p = lane & 3;
    u32 bb0[2], bb[2];
    for (int c2 = 0; c2 < 2; ++c2) bb0[c2] = (u32)(size_t)L + kg * 8192 + (4 * h + q) * 128 + ((c2 ^ kg ^ (q >> 1)) * 64) + 32 * blk + 8 * p;
    f32x16 af[2] = {zero16(), zero16()}, aa[2] = {zero16(), zero16()};
    constexpr int PD = BPD;
    u32x4 bf[PD + 1][2];
    const u32 hfb = WS_HFB + (rb * 14 + 7 * kg + ((lane & 7) < 7 ? (lane & 7) : 0)) * 64, ofo = WS_OF + (rb * 16 + 8 * kg + (lane & 7)) * 64;
    u32 look = 0, soe = 0;
    bool o_early = false;
    float rs2; f32x2 bx[8], sg[8], xx[8];
    u32x4 oa[16];
    if (!(att && __all(hlook == E))) poll_all(gws, hfa, E);
    if (!att) BAR();
    else {
#pragma unroll 1
      for (int spin = 0; spin < SPIN_CAP; ++spin) {
        const u32x4 r = *(volatile LAS u32x4 *)(L + L_BRDY);
        if (r[0] == E && r[1] == E && r[2] == E && r[3] == E) break;
        __builtin_amdgcn_s_sleep(1);
      }
      asm volatile("" ::: "memory");
    }
    sfor<0, 72>([&](auto I) {
      constexpr int i = I;
      if constexpr (i == 56) {
        for (int g = 0; g < 4; ++g)
          *(LAS f32x4 *)(L + 6 * L_SLOT + w * 4096 + g * 1024 + lane * 16) = f32x4{af[1][4 * g], af[1][4 * g + 1], af[1][4 * g + 2], af[1][4 * g + 3]};
        BAR();
        for (int g = 0; g < 4; ++g) {
          const f32x4 pf = *(LAS f32x4 *)(L + 6 * L_SLOT + (w ^ 2) * 4096 + g * 1024 + lane * 16);
          for (int e = 0; e < 2; ++e) {
            const int j = 4 * g + 2 * e;
            const f32x2 y = (f32x2{af[0][j], af[0][j + 1]} + f32x2{pf[2 * e], pf[2 * e + 1]}) * rs2 + bx[2 * g + e];
            const f32x2 t = y * -LOG2E;
            const f32x2 d = f32x2{__builtin_amdgcn_exp2f(t.x), __builtin_amdgcn_exp2f(t.y)} + 1.0f;
            sg[2 * g + e] = y * f32x2{__builtin_amdgcn_rcpf(d.x), __builtin_amdgcn_rcpf(d.y)};
            asm volatile("" : "+v"(sg[2 * g + e]));
          }
        }
        if (!o_early) {
          if (!__all(look == E)) poll_all(gws, ofo, E);
          for (int j = 0; j < OB; ++j) oa[j] = ld128_sc1(rws, lane * 16, soo + j * 1024);
        }
        __builtin_amdgcn_sched_barrier(0);
      }
      if constexpr (i >= 16 && (i & 7) == 0 && i != 56) asm volatile("s_barrier" ::: "memory");
      if constexpr (i == 0) {
        for (int j = 0; j < RD; ++j) ring[j] = ld128_sc1(rws, lane * 16, soh + h_step(j) * 1024);
        __builtin_amdgcn_sched_barrier(0);
        for (int c2 = 0; c2 < 2; ++c2) { bb[c2] = bb0[c2] + 4 * L_SLOT; asm volatile("" : "+v"(bb[c2])); for (int j = 0; j < PD; ++j) bf[j][c2] = trfrag((lchar *)(size_t)bb[c2] + j * 2048, 1024); }
      }
      static_assert(28 - RD - 6 < 16, "no look between the o stores and their counted wait");
      if constexpr (i == 28 - RD - 6) look = ld_flag(gws, hfb);
      if constexpr (i == 28 - RD) { if (!__all(look == E)) poll_all(gws, hfb, E); }
      if constexpr (i == 44) {
        for (int g = 0; g < 4; ++g) b2v[g] = *(const GAS u32x2 *)(gb2 + (col0 + 8 * g + 4 * h) * 2);
        sg0 = ld128_sc1(rws, WS_STAT + row * 16, 0);
      }
      if constexpr (i == 40) look = ld_flag(gws, ofo);
      if constexpr (i == 47) { o_early = __all(look == E); soe = o_early ? soo : WS_END; }
      if constexpr (i >= 48 && i < 48 + OB) oa[i - 48] = ld128_sc1(rws, lane * 16, soe + (i - 48) * 1024);
      if constexpr (i == 50) look = ld_flag(gws, ofo);
      if constexpr (i == 52) {
        u32x4 g4 = sg0;
        if (!__all(g4[1] == E && g4[3] == E)) g4 = stat_settle(g4, rws, WS_STAT + row * 16, E);
        const float rs1 = __builtin_amdgcn_rcpf(asf(g4[2]));
        rs2 = rs1 * rs1;
        for (int g = 0; g < 4; ++g) for (int e = 0; e < 2; ++e) { bx[2 * g + e] = f32x2{bf2f(b2v[g][e]), bfhi(b2v[g][e])}; asm volatile("" : "+v"(bx[2 * g + e])); }
      }
      if constexpr (i == 60) {
        for (int g = 0; g < 4; ++g) {
          const u32 r64 = 32 * rh + (lane & 31);
          const u32x2 xr = *(LAS u32x2 *)(L + L_XT + r64 * 128 + 16 * ((4 * kg + g) ^ ((r64 >> 1) & 7)) + 8 * h);
          for (int e = 0; e < 2; ++e) { xx[2 * g + e] = f32x2{bf2f(xr[e]), bfhi(xr[e])}; asm volatile("" : "+v"(xx[2 * g + e])); }
        }
      }
      if constexpr (i == 16) { if (att) o_stage(); }
      if constexpr (i == 16 + OFD) {
        if (att) { vmwait<OFD>(); if (lane < 2) st_flag(gws, WS_OF + ((8 * rga + 2 * w + lane) * 16 + head) * 64, E); }
      }
      u32x4 a;
      if constexpr (i < 56) a = ring[i % RD]; else a = oa[i - 56];
      if constexpr (i + PD < 72) {
        constexpr int n = i + PD;
        if constexpr ((n & 3) == 0) for (int c2 = 0; c2 < 2; ++c2) { bb[c2] = bb0[c2] + (((n >> 2) + 4) % NSLOT) * L_SLOT; asm volatile("" : "+v"(bb[c2])); }
        for (int c2 = 0; c2 < 2; ++c2) bf[n % (PD + 1)][c2] = trfrag((lchar *)(size_t)bb[c2] + (n & 3) * 2048, 1024);
      }
      if constexpr (i < 56) { af[0] = mfma(bf[i % (PD + 1)][0], a, af[0]); af[1] = mfma(bf[i % (PD + 1)][1], a, af[1]); asm volatile("" : "+v"(af[0]), "+v"(af[1])); }
      else { aa[0] = mfma(bf[i % (PD + 1)][0], a, aa[0]); aa[1] = mfma(bf[i % (PD + 1)][1], a, aa[1]); asm volatile("" : "+v"(aa[0]), "+v"(aa[1])); }
      if constexpr (i + RD < 56) ring[i % RD] = ld128_sc1(rws, lane * 16, soh + h_step(i + RD) * 1024);
      if constexpr (i >= 56 && i - 56 + OB < 16) oa[i - 56 + OB] = ld128_sc1(rws, lane * 16, soo + (i - 56 + OB) * 1024);
      __builtin_amdgcn_sched_barrier(0);
    });
    for (int g = 0; g < 4; ++g)
      *(LAS f32x4 *)(L + 8 * L_SLOT + w * 4096 + g * 1024 + lane * 16) = f32x4{aa[1][4 * g], aa[1][4 * g + 1], aa[1][4 * g + 2], aa[1][4 * g + 3]};
    BAR();
    u32x2 o2[4];
    for (int g = 0; g < 4; ++g) {
      const f32x4 pa = *(LAS f32x4 *)(L + 8 * L_SLOT + (w ^ 2) * 4096 + g * 1024 + lane * 16);
      for (int e = 0; e < 2; ++e) {
        const int i = 4 * g + 2 * e;
        const f32x2 r = (f32x2{aa[0][i], aa[0][i + 1]} + f32x2{pa[2 * e], pa[2 * e + 1]}) * sg[2 * g + e] + xx[2 * g + e];
        o2[g][e] = pk2(r.x, r.y);
      }
    }
    const rsrc_t rout = __builtin_amdgcn_make_buffer_rsrc((void *)gout, 0, 2048 * XROW, 0x00020000);
    for (int g = 0; g < 4; g += 2) {
      auto rx0 = __builtin_amdgcn_permlane32_swap(o2[g].x, o2[g + 1].x, false, false);
      auto ry0 = __builtin_amdgcn_permlane32_swap(o2[g].y, o2[g + 1].y, false, false);
      __builtin_amdgcn_raw_buffer_store_b128(u32x4{rx0[0], ry0[0], rx0[1], ry0[1]}, rout, row * XROW + (col0 + 8 * (g + h)) * 2, 0, 0);
    }
  }
}

extern "C" void kernel_launch(void *const *d_in, const int *in_sizes, int n_in, void *d_out, int out_size, void *d_ws, size_t ws_size, hipStream_t stream) {
  fused_hedgehog_ffn<<<256, 512, 0, stream>>>((const char *)d_in[0], (const char *)d_in[1], (const char *)d_in[2], (const char *)d_in[3], (const char *)d_in[5],
                                              (const char *)d_in[6], (const char *)d_in[7], (const char *)d_in[8], (char *)d_out, (char *)d_ws);
}
```
